```python
import torch

def _softmax(x, axis=-1):
    return torch.softmax(x, dim=axis)
def _gelu(x):
    return torch.nn.functional.gelu(x, approximate="tanh")
def _silu(x):
    return torch.nn.functional.silu(x)
def _sigmoid(x):
    return torch.sigmoid(x)
def _layernorm(x, axis=-1, eps=1e-5):
    m = x.mean(dim=axis, keepdim=True)
    v = x.var(dim=axis, keepdim=True, unbiased=False)
    return (x - m) / torch.sqrt(v + eps)
def _scatter_add(tgt, idx, src, axis=0):
    return tgt.clone().index_add_(axis, idx, src)
def _segment_sum(src, seg, n):
    out = torch.zeros((n,) + src.shape[1:], dtype=src.dtype)
    return out.index_add_(0, seg, src)
def _pad(x, pw):
    flat = tuple(v for pair in reversed(pw) for v in pair)
    return torch.nn.functional.pad(x, flat)

kernel_name = 'concat_heads__ssm_conv_s4d__linear_attn_block__prompt'

def setup_inputs():

    _w = (lambda a: (lambda t: t.to(torch.bfloat16) if t.is_floating_point() else t)(torch.as_tensor(a)))
    def _A_setup():
        Bsz, L, D, N = 1, 2048, 448, 256
        return {
            "u": _w((lambda *s: torch.randn(*s, dtype=torch.bfloat16) * 0.05)(Bsz, L, D)),
            "log_A_real": _w((lambda *s: torch.randn(*s, dtype=torch.bfloat16) * 6.4)(D, N) * 0.5),
            "A_imag": _w((lambda *s: torch.randn(*s, dtype=torch.bfloat16) * 0.05)(D, N) * 3.14),
            "C_real": _w((lambda *s: torch.randn(*s, dtype=torch.bfloat16) * 0.2)(D, N) / N ** 0.5),
            "C_imag": _w((lambda *s: torch.randn(*s, dtype=torch.bfloat16) * 0.2)(D, N) / N ** 0.5),
            "log_dt": _w((lambda *s: torch.randn(*s, dtype=torch.bfloat16) * 0.05)(D) * 0.5 - 4.0),
            "D_skip": _w((lambda *s: torch.randn(*s, dtype=torch.bfloat16) * 0.05)(D)),
        }
    _A_in = _A_setup()

    def _B_setup():
        B, S, D = 1, 2048, 448
        return {
            "x": _w((lambda *s: torch.randn(*s, dtype=torch.bfloat16) * 0.05)(B, S, D)),
            "wqkv": _w((lambda *s: torch.randn(*s, dtype=torch.bfloat16) * 0.8)(D, 3 * D)),
            "wo": _w((lambda *s: torch.randn(*s, dtype=torch.bfloat16) * 0.05)(D, D)),
        }
    _B_in = _B_setup()

    return {
        "A_u": _A_in["u"],
        "A_log_A_real": _A_in["log_A_real"],
        "A_A_imag": _A_in["A_imag"],
        "A_C_real": _A_in["C_real"],
        "A_C_imag": _A_in["C_imag"],
        "A_log_dt": _A_in["log_dt"],
        "A_D_skip": _A_in["D_skip"],
        "B_x": _B_in["x"],
        "B_wqkv": _B_in["wqkv"],
        "B_wo": _B_in["wo"],
    }

def reference(A_u, A_log_A_real, A_A_imag, A_C_real, A_C_imag, A_log_dt, A_D_skip, B_x, B_wqkv, B_wo):
    def _A_ref(u, log_A_real, A_imag, C_real, C_imag, log_dt, D_skip):
        L = u.shape[1]
        dt = torch.exp(log_dt)
        A = -torch.exp(log_A_real) + 1j * A_imag
        C = (C_real + 1j * C_imag) * dt[:, None]
        pos = (lambda x, dt: x.to(dt))(torch.arange(L), log_dt.dtype)
        P = torch.exp((A * dt[:, None])[:, :, None] * pos[None, None, :])
        K = 2.0 * torch.einsum("dn,dnl->dl", C, P).real
        L2 = 2 * L
        u_t = (lambda x, *d: x.permute(*d))(u, 0, 2, 1)
        Kf = (lambda x, *a, **k: torch.fft.rfft(x.float(), *a, **k))(K, L2)
        Uf = (lambda x, *a, **k: torch.fft.rfft(x.float(), *a, **k))(u_t, L2)
        y = (lambda x, *a, **k: torch.fft.irfft(x, *a, **k).to(torch.bfloat16))(Uf * Kf[None, :, :], L2)[:, :, :L]
        y = (lambda x, *d: x.permute(*d))(y, 0, 2, 1)
        return y + u * D_skip[None, None, :]

    def _B_ref(x, wqkv, wo):
        B, S, D = x.shape
        H = 16; Dh = D // H
        qkv = (lambda x, *d: x.permute(*d))((x @ wqkv).reshape(B, S, 3, H, Dh), 2, 0, 3, 1, 4)
        q, k, v = qkv[0], qkv[1], qkv[2]
        phi = lambda t: torch.where(t > 0, t + 1, torch.exp(t)) + 1e-6
        pq, pk = phi(q), phi(k)
        kv = torch.einsum("bhsd,bhse->bhde", pk, v)
        z = torch.einsum("bhsd->bhd", pk)
        num = torch.einsum("bhqd,bhde->bhqe", pq, kv)
        den = torch.einsum("bhqd,bhd->bhq", pq, z)[..., None] + 1e-6
        o = num / den
        return (lambda x, *d: x.permute(*d))(o, 0, 2, 1, 3).reshape(B, S, D) @ wo

    a = _A_ref(A_u, A_log_A_real, A_A_imag, A_C_real, A_C_imag, A_log_dt, A_D_skip)
    b = _B_ref(B_x, B_wqkv, B_wo)
    return torch.concatenate([a, b], axis=-1)
```

```cpp
#include <hip/hip_runtime.h>
#include <hip/hip_bf16.h>
#include <stdint.h>

typedef unsigned short u16;
static __device__ __forceinline__ float bf2f(u16 v) { return __uint_as_float(((unsigned)v) << 16); }
static __device__ __forceinline__ u16 f2bf(float f) { __hip_bfloat16 h = __float2bfloat16(f); return *(u16*)&h; }
static __device__ __forceinline__ float rbf(float f) { return bf2f(f2bf(f)); }

constexpr int L = 2048, D = 448, N = 256, H = 16, DH = 28;

__global__ void kgen(const u16* logA, const u16* Aim, const u16* Cr, const u16* Ci, const u16* logdt, float* K) {
  int d = blockIdx.x;
  float dt = rbf(expf(bf2f(logdt[d])));
  __shared__ float sa[N], sb[N], scr[N], sci[N];
  for (int n = threadIdx.x; n < N; n += blockDim.x) {
    sa[n] = -rbf(expf(bf2f(logA[d * N + n]))) * dt;
    sb[n] = bf2f(Aim[d * N + n]) * dt;
    scr[n] = bf2f(Cr[d * N + n]) * dt;
    sci[n] = bf2f(Ci[d * N + n]) * dt;
  }
  __syncthreads();
  for (int l = threadIdx.x; l < L; l += blockDim.x) {
    float p = rbf((float)l);
    float acc = 0.f;
    for (int n = 0; n < N; ++n) {
      float e = expf(sa[n] * p);
      float s, c;
      sincosf(sb[n] * p, &s, &c);
      acc += e * (scr[n] * c - sci[n] * s);
    }
    K[d * L + l] = 2.f * acc;
  }
}

__global__ void conv(const u16* u, const float* K, const u16* Dsk, u16* out) {
  int d = blockIdx.x;
  __shared__ float su[L], sk[L];
  for (int l = threadIdx.x; l < L; l += blockDim.x) { su[l] = bf2f(u[l * D + d]); sk[l] = K[d * L + l]; }
  __syncthreads();
  float ds = bf2f(Dsk[d]);
  for (int l = threadIdx.x; l < L; l += blockDim.x) {
    float acc = 0.f;
    for (int j = 0; j <= l; ++j) acc += sk[j] * su[l - j];
    float y = rbf(acc);
    out[l * (2 * D) + d] = f2bf(y + rbf(su[l] * ds));
  }
}

__device__ float phi(float t) { float r = t > 0.f ? rbf(t + 1.f) : rbf(expf(t)); return rbf(r + 1e-6f); }

__global__ void qkvk(const u16* x, const u16* w, u16* qkv) {
  int s = blockIdx.x;
  __shared__ float sx[D];
  for (int i = threadIdx.x; i < D; i += blockDim.x) sx[i] = bf2f(x[s * D + i]);
  __syncthreads();
  for (int c = threadIdx.x; c < 3 * D; c += blockDim.x) {
    float acc = 0.f;
    for (int k = 0; k < D; ++k) acc += sx[k] * bf2f(w[k * 3 * D + c]);
    float v = rbf(acc);
    if (c < 2 * D) v = phi(v);
    qkv[s * 3 * D + c] = f2bf(v);
  }
}

__global__ void kvk(const u16* qkv, float* kv, float* z) {
  int h = blockIdx.x;
  int t = threadIdx.x;
  if (t < DH * DH) {
    int d = t / DH, e = t % DH;
    float acc = 0.f;
    for (int s = 0; s < L; ++s) acc += bf2f(qkv[s * 3 * D + D + h * DH + d]) * bf2f(qkv[s * 3 * D + 2 * D + h * DH + e]);
    kv[h * DH * DH + t] = rbf(acc);
  } else if (t < DH * DH + DH) {
    int d = t - DH * DH;
    float acc = 0.f;
    for (int s = 0; s < L; ++s) acc += bf2f(qkv[s * 3 * D + D + h * DH + d]);
    z[h * DH + d] = rbf(acc);
  }
}

__global__ void outk(const u16* qkv, const float* kv, const float* z, const u16* wo, u16* out) {
  int s = blockIdx.x;
  __shared__ float so[D];
  for (int c = threadIdx.x; c < D; c += blockDim.x) {
    int h = c / DH, e = c % DH;
    float num = 0.f, den = 0.f;
    for (int d = 0; d < DH; ++d) {
      float q = bf2f(qkv[s * 3 * D + h * DH + d]);
      num += q * kv[h * DH * DH + d * DH + e];
      den += q * z[h * DH + d];
    }
    num = rbf(num); den = rbf(rbf(den) + 1e-6f);
    so[c] = rbf(num / den);
  }
  __syncthreads();
  for (int c = threadIdx.x; c < D; c += blockDim.x) {
    float acc = 0.f;
    for (int k = 0; k < D; ++k) acc += so[k] * bf2f(wo[k * D + c]);
    out[s * 2 * D + D + c] = f2bf(acc);
  }
}

extern "C" void kernel_launch(void* const* d_in, const int* in_sizes, int n_in, void* d_out, int out_size, void* d_ws, size_t ws_size, hipStream_t stream) {
  const u16* u = (const u16*)d_in[0];
  const u16* logA = (const u16*)d_in[1];
  const u16* Aim = (const u16*)d_in[2];
  const u16* Cr = (const u16*)d_in[3];
  const u16* Ci = (const u16*)d_in[4];
  const u16* logdt = (const u16*)d_in[5];
  const u16* Dsk = (const u16*)d_in[6];
  const u16* x = (const u16*)d_in[7];
  const u16* wqkv = (const u16*)d_in[8];
  const u16* wo = (const u16*)d_in[9];
  u16* out = (u16*)d_out;
  char* ws = (char*)d_ws;
  float* K = (float*)ws;
  u16* qkv = (u16*)(ws + (4 << 20));
  float* kv = (float*)(ws + (12 << 20));
  float* z = (float*)(ws + (13 << 20));
  kgen<<<D, 256, 0, stream>>>(logA, Aim, Cr, Ci, logdt, K);
  conv<<<D, 256, 0, stream>>>(u, K, Dsk, out);
  qkvk<<<L, 256, 0, stream>>>(x, wqkv, qkv);
  kvk<<<H, 1024, 0, stream>>>(qkv, kv, z);
  outk<<<L, 256, 0, stream>>>(qkv, kv, z, wo, out);
}
```

```cpp
#include <hip/hip_runtime.h>
#include <stdint.h>

typedef unsigned short u16;
typedef unsigned u32;
typedef unsigned long long u64;
typedef short s16x4 __attribute__((ext_vector_type(4)));
typedef __bf16 bf16x8 __attribute__((ext_vector_type(8)));
typedef __bf16 bf16x2 __attribute__((ext_vector_type(2)));
typedef _Float16 f16x8 __attribute__((ext_vector_type(8)));
typedef _Float16 f16x2 __attribute__((ext_vector_type(2)));
typedef float f32x16 __attribute__((ext_vector_type(16)));
typedef float f32x4 __attribute__((ext_vector_type(4)));
typedef float f32x2 __attribute__((ext_vector_type(2)));
typedef u32 u32x4 __attribute__((ext_vector_type(4)));
typedef u32 u32x2 __attribute__((ext_vector_type(2)));
#define LAS __attribute__((address_space(3)))
#define GAS __attribute__((address_space(1)))
#define DI __device__ __forceinline__
#define RLX_AGENT __ATOMIC_RELAXED, __HIP_MEMORY_SCOPE_AGENT

#define BMAP 0

constexpr u32 WS_FLAG = 4096;
constexpr u32 WS_KVP = 1u << 17;
constexpr u32 WS_OB = WS_KVP + (1u << 20);
constexpr u32 WS_UX = WS_OB + (2u << 20);
constexpr u32 WS_YX = WS_UX + (2u << 20);
constexpr u32 WS_END = WS_YX + (2u << 20);
constexpr u32 WS_DBG = WS_END;
constexpr int SPIN_CAP = 1 << 15;
constexpr u32 SENT = 0xFFFFFFFFu;

#define STAMP(i) do {} while (0)
#define STAMP_DECL
#define STAMP_FLUSH() do {} while (0)

DI float bflo(u32 v) { return __uint_as_float(v << 16); }
DI float bfhi(u32 v) { return __uint_as_float(v & 0xffff0000u); }
DI u32 pkbf(float a, float b) { f32x2 p = {a, b}; return __builtin_bit_cast(u32, __builtin_convertvector(p, bf16x2)); }
DI u32 pkh(f32x2 p) { return __builtin_bit_cast(u32, __builtin_convertvector(p, f16x2)); }
DI float rbf(float a) { return bflo(pkbf(a, a)); }
DI u32 san(u32 v) { return v < SENT - 1u ? v : SENT - 1u; }
DI u32x4 san4(u32x4 v) { return (u32x4){san(v.x), san(v.y), san(v.z), san(v.w)}; }
DI u32 umax3(u32 a, u32 b, u32 c) { u32 m = a > b ? a : b; return m > c ? m : c; }
DI u32 mx4(u32 m, u32x4 v) { return umax3(umax3(m, v.x, v.y), v.z, v.w); }
DI u64 next_tag(u64 e) { return e * 6364136223846793005ull + 1442695040888963407ull; }
DI f32x16 mfma32(u32x4 a, u32x4 b, f32x16 c) { return __builtin_amdgcn_mfma_f32_32x32x16_bf16(__builtin_bit_cast(bf16x8, a), __builtin_bit_cast(bf16x8, b), c, 0, 0, 0); }
DI f32x16 mfma32h(u32x4 a, u32x4 b, f32x16 c) { return __builtin_amdgcn_mfma_f32_32x32x16_f16(__builtin_bit_cast(f16x8, a), __builtin_bit_cast(f16x8, b), c, 0, 0, 0); }
DI f32x4 mfma16(u32x4 a, u32x4 b, f32x4 c) { return __builtin_amdgcn_mfma_f32_16x16x32_bf16(__builtin_bit_cast(bf16x8, a), __builtin_bit_cast(bf16x8, b), c, 0, 0, 0); }
DI u32x2 tr_read(LAS char* p) { return __builtin_bit_cast(u32x2, __builtin_amdgcn_ds_read_tr16_b64_v4i16((LAS s16x4*)p)); }
DI void dma16(const void* src, LAS char* dst) { __builtin_amdgcn_global_load_lds((const GAS void*)src, (LAS void*)dst, 16, 0, 0); }
#define VMCNT(n) asm volatile("s_waitcnt vmcnt(" #n ")" ::: "memory")
#define LGKM0() asm volatile("s_waitcnt lgkmcnt(0)" ::: "memory")
#define BAR() do { asm volatile("s_waitcnt lgkmcnt(0)" ::: "memory"); __builtin_amdgcn_s_barrier(); asm volatile("" ::: "memory"); } while (0)
#define ST_SC1(v, rs, off) __builtin_amdgcn_raw_buffer_store_b128((v), (rs), (off), 0, 16)
#define LD_SC1(rs, off) __builtin_amdgcn_raw_buffer_load_b128((rs), (off), 0, 16)

DI u32 phi2(float a, float b) {
  const u32 t = pkbf(a, b);
  const f32x2 x = {bflo(t), bfhi(t)};
  const f32x2 y = x * 1.4426950408889634f;
  const float e0 = __builtin_amdgcn_fmed3f(__builtin_amdgcn_exp2f(y.x), 0.f, 1.f);
  const float e1 = __builtin_amdgcn_fmed3f(__builtin_amdgcn_exp2f(y.y), 0.f, 1.f);
  const f32x2 x1 = x + 1.0f;
  const u32 r = pkbf(__builtin_fmaxf(x1.x, e0), __builtin_fmaxf(x1.y, e1));
  const f32x2 z = (f32x2){bflo(r), bfhi(r)} + 1e-6f;
  return pkbf(z.x, z.y);
}

DI void wait_flags(const char* ws, int idx, u64 tag) {
  const GAS u64* f = (const GAS u64*)(ws + WS_FLAG) + idx * 16;
#pragma nounroll
  for (int it = 0; it < SPIN_CAP; ++it) {
    const u64 v = __hip_atomic_load(f, RLX_AGENT);
    if (__all(v == tag)) break;
    __builtin_amdgcn_s_sleep(2);
  }
}

DI int b_block(int rb, int h) {
  return (h >> 1) + 8 * (2 * rb + (h & 1));
}

DI void role_b(LAS char* lds, const char* __restrict__ x, const char* __restrict__ wqkv, const char* __restrict__ wo,
               char* __restrict__ out, char* ws) {
  const int b = blockIdx.x, tid = threadIdx.x, lane = tid & 63;
  const int w = __builtin_amdgcn_readfirstlane(tid >> 6);
  STAMP_DECL;
  STAMP(0);
  const int h = 2 * (b & 7) + ((b >> 3) & 1), rb = b >> 4;
  const int start = (rb + 3 * (h & 1)) % 7;
  const __amdgpu_buffer_rsrc_t rs = __builtin_amdgcn_make_buffer_rsrc(ws, 0, WS_END, 0x00020000);
  const u64 epoch = __hip_atomic_load((const GAS u64*)ws, RLX_AGENT);
  const u32 kvp_off = WS_KVP + (h * 16 + rb) * 4096 + tid * 16;
  const u32 ob_off = WS_OB + ((rb * 4 + w) * 16 + h) * 2048 + lane * 16;
  { const u32x4 c = {SENT, SENT, SENT, SENT}; ST_SC1(c, rs, kvp_off); ST_SC1(c, rs, ob_off); ST_SC1(c, rs, ob_off + 1024); }

  constexpr int XR = 0, WR = 49152;
  const int wsb = (56 * h) & ~15;
  const int coff = (h & 1) * 4;
  const int wone = (h & 1) ? 0 : 28;
  const char* xs0 = x + (u32)((128 * rb + 32 * w + (lane >> 3)) * 896 + (((lane & 7) ^ (lane >> 4)) << 4));
  const char* xs1 = x + (u32)((128 * rb + 32 * w + 8 + (lane >> 3)) * 896 + (((lane & 7) ^ (4 + (lane >> 4))) << 4));
  const char* wsrc = wqkv + (u32)((16 * w + (lane >> 2)) * 2688 + wsb + (lane & 3) * 16);
  auto chunk = [&](int cc) { int c = cc + start; return c >= 7 ? c - 7 : c; };
  auto issue_x = [&](int cc) {
    const int c = chunk(cc); LAS char* d = lds + XR + (cc % 3) * 16384 + w * 4096;
    dma16(xs0 + c * 128, d); dma16(xs1 + c * 128, d + 1024);
    dma16(xs0 + c * 128 + 16 * 896, d + 2048); dma16(xs1 + c * 128 + 16 * 896, d + 3072);
  };
  auto issue_w = [&](int cc) {
    const int c = chunk(cc); LAS char* d = lds + WR + (cc & 3) * 8192 + w * 1024;
    dma16(wsrc + c * (64 * 2688) + 896, d); dma16(wsrc + c * (64 * 2688) + 1792, d + 4096);
  };
  auto issue_q = [&](int cc) {
    dma16(wsrc + chunk(cc) * (64 * 2688), lds + XR + (1 + (cc >> 2)) * 16384 + w * 4096 + (cc & 3) * 1024);
  };
  issue_w(0); issue_x(0); issue_w(1); issue_x(1); issue_w(2); issue_x(2);

  const int r32 = lane & 31, half = lane >> 5;
  const int xrd = w * 4096 + r32 * 128 + ((half ^ ((r32 >> 1) & 7)) << 4);
  const int wrd = (8 * half + ((lane >> 2) & 3)) * 64 + ((lane >> 4) & 1) * 32 + (lane & 3) * 8;
  u32x4 xf[28];
  f32x16 ak = {}, av = {};
  u64 tag = 0;
#pragma unroll
  for (int cc = 0; cc < 7; ++cc) {
    if (cc <= 4) VMCNT(12); else if (cc == 5) VMCNT(10); else VMCNT(7);
    BAR();
    if (cc == 0) { STAMP(1); }
    if (cc < 4) issue_w(cc + 3);
#pragma unroll
    for (int s = 0; s < 4; ++s) xf[cc * 4 + s] = *(LAS u32x4*)(lds + XR + (cc % 3) * 16384 + (xrd ^ (s << 5)));
    u32x2 wf[16];
#pragma unroll
    for (int s = 0; s < 4; ++s) {
      LAS char* p = lds + WR + (cc & 3) * 8192 + s * 1024 + wrd;
      wf[4 * s] = tr_read(p); wf[4 * s + 1] = tr_read(p + 256); wf[4 * s + 2] = tr_read(p + 4096); wf[4 * s + 3] = tr_read(p + 4096 + 256);
    }
    asm volatile("s_waitcnt lgkmcnt(0)" : "+v"(xf[cc * 4]), "+v"(xf[cc * 4 + 1]), "+v"(xf[cc * 4 + 2]), "+v"(xf[cc * 4 + 3]) :: "memory");
    if (cc < 4) issue_x(cc + 3);
    else if (cc == 4) { issue_q(0); issue_q(1); issue_q(2); issue_q(3); }
    else if (cc == 5) { issue_q(4); issue_q(5); issue_q(6); }
#pragma unroll
    for (int s = 0; s < 4; ++s) {
      ak = mfma32(xf[cc * 4 + s], (u32x4){wf[4 * s].x, wf[4 * s].y, wf[4 * s + 1].x, wf[4 * s + 1].y}, ak);
      av = mfma32(xf[cc * 4 + s], (u32x4){wf[4 * s + 2].x, wf[4 * s + 2].y, wf[4 * s + 3].x, wf[4 * s + 3].y}, av);
    }
  }
  STAMP(2);
  {
    u32x4 pk[2], pv[2];
#pragma unroll
    for (int s = 0; s < 2; ++s)
#pragma unroll
      for (int j = 0; j < 4; ++j) {
        pk[s][j] = phi2(ak[8 * s + 2 * j], ak[8 * s + 2 * j + 1]);
        const u32 v = pkbf(av[8 * s + 2 * j], av[8 * s + 2 * j + 1]);
        pv[s][j] = r32 == wone ? 0x3F803F80u : v;
      }
    f32x16 kv = {};
    kv = mfma32(pk[0], pv[0], kv); kv = mfma32(pk[1], pv[1], kv);
#pragma unroll
    for (int g = 0; g < 4; ++g) *(LAS f32x4*)(lds + WR + w * 4096 + g * 1024 + lane * 16) = (f32x4){kv[4 * g], kv[4 * g + 1], kv[4 * g + 2], kv[4 * g + 3]};
    VMCNT(0);
    BAR();
    f32x4 s4 = *(LAS f32x4*)(lds + WR + tid * 16);
#pragma unroll
    for (int ww = 1; ww < 4; ++ww) s4 += *(LAS f32x4*)(lds + WR + ww * 4096 + tid * 16);
    ST_SC1(san4(__builtin_bit_cast(u32x4, s4)), rs, kvp_off);
    tag = next_tag(epoch);
    if (tid == 0) __hip_atomic_store((GAS u64*)(ws + WS_FLAG) + b * 16, tag, RLX_AGENT);
  }
  STAMP(3);
  u32x4 pq[2];
  {
    f32x16 aq = {};
#pragma unroll
    for (int cc = 0; cc < 7; ++cc)
#pragma unroll
      for (int s = 0; s < 4; ++s) {
        LAS char* p = lds + XR + (1 + (cc >> 2)) * 16384 + s * 4096 + (cc & 3) * 1024 + wrd;
        const u32x2 lo = tr_read(p), hi = tr_read(p + 256);
        aq = mfma32((u32x4){lo.x, lo.y, hi.x, hi.y}, xf[cc * 4 + s], aq);
      }
#pragma unroll
    for (int s = 0; s < 2; ++s)
#pragma unroll
      for (int j = 0; j < 4; ++j) pq[s][j] = phi2(aq[8 * s + 2 * j], aq[8 * s + 2 * j + 1]);
  }
  STAMP(4);
  wait_flags(ws, (lane & 16) ? b_block(rb, lane & 15) : b_block(lane & 15, h), tag);
  STAMP(5);
  {
    u32x4 d[16];
  #pragma nounroll
  for (int it = 0; it < SPIN_CAP; ++it) {
      asm volatile("" ::: "memory");
#pragma unroll
      for (int p = 0; p < 16; ++p) d[p] = LD_SC1(rs, WS_KVP + h * 65536 + p * 4096 + tid * 16);
      u32 m = 0;
#pragma unroll
      for (int p = 0; p < 16; ++p) m = mx4(m, d[p]);
      if (__all(m != SENT)) break;
    }
    f32x4 s4 = __builtin_bit_cast(f32x4, d[0]);
#pragma unroll
    for (int p = 1; p < 16; ++p) s4 += __builtin_bit_cast(f32x4, d[p]);
    const int dw = 8 * w + 4 * half - coff;
    u32x2 o2 = {pkbf(s4.x, s4.y), pkbf(s4.z, s4.w)};
    if (dw < 0 || dw >= 28) o2 = (u32x2){0u, 0u};
    *(LAS u32x2*)(lds + 73728 + lane * 32 + w * 8) = o2;
  }
  STAMP(6);
  BAR();
  {
    const int i = (r32 & 3) + 4 * (r32 >> 3), hh = (r32 >> 2) & 1;
    const int ew = i < 14 ? 14 * hh + i + coff : wone;
    LAS char* p = lds + 73728 + (ew + 32 * half) * 32;
    const u32x4 a0 = *(LAS u32x4*)p, a1 = *(LAS u32x4*)(p + 16);
    f32x16 nm = {};
    nm = mfma32(a0, pq[0], nm); nm = mfma32(a1, pq[1], nm);
    const float den = rbf(rbf(nm[14]) + 1e-6f);
    const float rc = __builtin_amdgcn_rcpf(den);
    u32 o[8];
#pragma unroll
    for (int j = 0; j < 7; ++j) { const u32 t = pkbf(nm[2 * j], nm[2 * j + 1]); o[j] = san(pkbf(bflo(t) * rc, bfhi(t) * rc)); }
    o[7] = 0;
    ST_SC1(((u32x4){o[0], o[1], o[2], o[3]}), rs, ob_off);
    ST_SC1(((u32x4){o[4], o[5], o[6], o[7]}), rs, ob_off + 1024);
  }
  STAMP(7);
  const int rt = h & 3, ct = h >> 2;
  u32x4 wof[32];
  {
    const int sg = (lane & 15) ^ (4 * (lane >> 4));
    const bool live = ct < 3 || sg < 8;
    const char* src = wo + (u32)((28 * w + (lane >> 4)) * 896 + ct * 256 + sg * 16);
    auto issue_wo = [&](int pc) {
      if (live) {
#pragma unroll
        for (int ii = 0; ii < 7; ++ii) dma16(src + (112 * pc + 4 * ii) * 896, lds + (pc & 1) * 28672 + (w * 7 + ii) * 1024);
      }
    };
    const int q = (lane >> 2) & 3, gam = 4 * ((lane >> 4) & 1) + (lane & 3), ng = 4 * (gam & 1) + (gam >> 1);
    const int rd = (14 * half + q) * 256 + (w ^ ((2 * half + q) & 3)) * 64 + 8 * ng;
    issue_wo(0); issue_wo(1);
#pragma unroll
    for (int pc = 0; pc < 4; ++pc) {
      if (pc < 3) VMCNT(7); else VMCNT(0);
      BAR();
#pragma unroll
      for (int hl = 0; hl < 4; ++hl) {
        LAS char* p = lds + (pc & 1) * 28672 + rd + 28 * hl * 256;
        const u32x2 r0 = tr_read(p), r1 = tr_read(p + 1024), r2 = tr_read(p + 2048), r3 = tr_read(p + 3072);
        wof[pc * 8 + 2 * hl] = (u32x4){r0.x, r0.y, r1.x, r1.y};
        wof[pc * 8 + 2 * hl + 1] = (u32x4){r2.x, r2.y, r3.x, 0u};
      }
      if (pc < 2) {
#pragma unroll
        for (int i = 0; i < 8; ++i) asm volatile("" : "+v"(wof[pc * 8 + i]));
        BAR();
        issue_wo(pc + 2);
      }
    }
  }
  STAMP(8);
  {
    u32x4 d[8];
  #pragma nounroll
  for (int it = 0; it < SPIN_CAP; ++it) {
      asm volatile("" ::: "memory");
#pragma unroll
      for (int p = 0; p < 8; ++p) d[p] = LD_SC1(rs, WS_OB + ((rb * 4 + rt) * 16 + 4 * w) * 2048 + p * 1024 + lane * 16);
      u32 m = 0;
#pragma unroll
      for (int p = 0; p < 8; ++p) m = mx4(m, d[p]);
      if (__all(m != SENT)) break;
    }
    STAMP(9);
    BAR();
#pragma unroll
    for (int p = 0; p < 8; ++p) *(LAS u32x4*)(lds + (8 * w + p) * 1024 + lane * 16) = d[p];
    BAR();
  }
  if (ct < 3 || w < 2) {
    f32x16 acc = {};
#pragma unroll
    for (int k = 0; k < 32; ++k) acc = mfma32(wof[k], *(LAS u32x4*)(lds + k * 1024 + lane * 16), acc);
    char* dst = out + (u32)((128 * rb + 32 * rt + r32) * 1792 + 896 + ct * 256 + w * 64 + half * 32);
    *(u32x4*)dst = (u32x4){pkbf(acc[0], acc[1]), pkbf(acc[2], acc[3]), pkbf(acc[4], acc[5]), pkbf(acc[6], acc[7])};
    *(u32x4*)(dst + 16) = (u32x4){pkbf(acc[8], acc[9]), pkbf(acc[10], acc[11]), pkbf(acc[12], acc[13]), pkbf(acc[14], acc[15])};
  }
  STAMP(10);
  STAMP_FLUSH();
}

DI f32x2 cmul(f32x2 a, f32x2 b) {
  const f32x2 t = (f32x2){a.x, a.x} * b;
  return __builtin_elementwise_fma((f32x2){a.y, a.y}, (f32x2){-b.y, b.x}, t);
}
DI f32x2 cexp_p(float a2, float f, float p) {
  const float m = __builtin_amdgcn_exp2f(a2 * p), ph = __builtin_amdgcn_fractf(f * p);
  return (f32x2){m, m} * (f32x2){__builtin_amdgcn_cosf(ph), __builtin_amdgcn_sinf(ph)};
}

DI void role_a(LAS char* lds, const char* __restrict__ u, const u32* __restrict__ logA, const u32* __restrict__ Aim,
               const u32* __restrict__ Cre, const u32* __restrict__ Cim, const u16* __restrict__ logdt,
               const char* __restrict__ Dsk, char* __restrict__ out, char* ws) {
  const int a = blockIdx.x - 256, cb = a >> 5, jp = a & 31, tid = threadIdx.x, lane = tid & 63;
  const int w = __builtin_amdgcn_readfirstlane(tid >> 6);
  STAMP_DECL;
  STAMP(0);
  const __amdgpu_buffer_rsrc_t rs = __builtin_amdgcn_make_buffer_rsrc(ws, 0, WS_END, 0x00020000);
  const u64 epoch = __hip_atomic_load((const GAS u64*)ws, RLX_AGENT);
  const int ch = w >> 1, hf = w & 1, d = 2 * a + ch;
  const int pi = d * 128 + hf * 64 + lane;
  const u32 pA = logA[pi], pI = Aim[pi], pR = Cre[pi], pC = Cim[pi];
  const float ldt = bflo(logdt[d]);
  u32x4 ub[4];
  if (w < 2) {
#pragma unroll
    for (int i = 0; i < 4; ++i) ub[i] = *(const u32x4*)(u + (u32)((64 * jp + 32 * w + 4 * (lane & 7) + i) * 896 + cb * 128 + (lane >> 3) * 16));
  }
  u32x4 us[2];
#pragma unroll
  for (int i = 0; i < 2; ++i) us[i] = *(const u32x4*)(u + (u32)((64 * jp + 32 * i + (tid >> 3)) * 896 + cb * 128 + (tid & 7) * 16));
  const u32x4 dk = *(const u32x4*)(Dsk + cb * 128 + (tid & 7) * 16);
  {
    const u32x4 c = {SENT, SENT, SENT, SENT};
#pragma unroll
    for (int i = 0; i < 2; ++i) ST_SC1(c, rs, WS_YX + (cb * 32 + 16 * i + (tid >> 4)) * 8192 + jp * 256 + (tid & 15) * 16);
  }
  int tix[8];
#pragma unroll
  for (int j = 0; j < 8; ++j) {
    const int p = (int)rbf((float)(2047 - 8 * tid - j));
    const int low = p & 7, blk = p >> 8;
    const int row = blk == 0 ? low : blk == 1 ? 8 + (low >> 1) : blk < 4 ? 8 + 2 * blk + (low >> 2) : 12 + blk;
    tix[j] = (row * 32 + ((p >> 3) & 31)) * 4;
  }
  if (w < 2) {
#pragma unroll
    for (int k = 0; k < 4; ++k)
      ST_SC1(((u32x4){ub[0][k], ub[1][k], ub[2][k], ub[3][k]}), rs, WS_UX + (cb * 32 + 4 * (lane >> 3) + k) * 8192 + jp * 256 + w * 128 + (lane & 7) * 16);
  }
  u32 sk[8];
#pragma unroll
  for (int i = 0; i < 2; ++i)
#pragma unroll
    for (int k = 0; k < 4; ++k) sk[4 * i + k] = pkbf(bflo(us[i][k]) * bflo(dk[k]), bfhi(us[i][k]) * bfhi(dk[k]));
  STAMP(1);
  constexpr int SROW = 272, STG = 53 * SROW, TBL = 4 * STG, UC = 0, UCS = 4160, RV = 8 * UCS, RVS = 4608, EX = RV + 2 * RVS;
  const float dt = rbf(expf(ldt));
  u32 mx = umax3(umax3(pR & 0x7fffu, (pR >> 16) & 0x7fffu, pC & 0x7fffu), (pC >> 16) & 0x7fffu, 0u);
#pragma unroll
  for (int o = 32; o >= 1; o >>= 1) { const u32 t = __shfl_xor(mx, o); mx = mx > t ? mx : t; }
  int ex = mx >> 7; ex = ex < 13 ? 13 : ex > 254 ? 254 : ex;
  const float sc = __uint_as_float((u32)(266 - ex) << 23), isc = __uint_as_float((u32)(ex - 12) << 23);
  f32x16 kacc = {};
  u64 tag = 0;
  LAS char* stg = lds + w * STG;
  const int r32 = lane & 31, half = lane >> 5;
#pragma unroll 1
  for (int bt = 0; bt < 2; ++bt) {
    const float la = bt ? bfhi(pA) : bflo(pA), ai = bt ? bfhi(pI) : bflo(pI), cr = bt ? bfhi(pR) : bflo(pR), ci = bt ? bfhi(pC) : bflo(pC);
    const float a2 = -rbf(expf(la)) * dt * 1.4426950408889634f, f = ai * dt * 0.15915494309189535f;
    LAS u32* sw = (LAS u32*)(stg + lane * 4);
#define PUT(row, v) sw[(row) * (SROW / 4)] = pkh(v)
    {
      const f32x2 z8 = cexp_p(a2, f, 8.f), z16 = cmul(z8, z8);
      f32x2 x0 = (f32x2){cr, ci} * sc, x1 = cmul(x0, z8);
#pragma unroll
      for (int r = 0; r < 32; r += 2) {
        PUT(r, x0); PUT(r + 1, x1);
        if (r < 30) { x0 = cmul(x0, z16); x1 = cmul(x1, z16); }
      }
    }
    {
      const f32x2 y1 = cexp_p(a2, -f, 1.f), y256 = cexp_p(a2, -f, 256.f);
      const f32x2 y2 = cmul(y1, y1), y3 = cmul(y2, y1), y4 = cmul(y2, y2), y6 = cmul(y4, y2);
      const f32x2 y512 = cmul(y256, y256), y768 = cmul(y512, y256), y1024 = cmul(y512, y512);
      PUT(32, ((f32x2){1.f, 0.f})); PUT(33, y1); PUT(34, y2); PUT(35, y3); PUT(36, y4); PUT(37, cmul(y4, y1)); PUT(38, y6); PUT(39, cmul(y4, y3));
      PUT(40, y256); PUT(41, cmul(y256, y2)); PUT(42, cmul(y256, y4)); PUT(43, cmul(y256, y6));
      PUT(44, y512); PUT(45, cmul(y512, y4)); PUT(46, y768); PUT(47, cmul(y768, y4));
      PUT(48, y1024); PUT(49, cmul(y1024, y256)); PUT(50, cmul(y1024, y512)); PUT(51, cmul(y1024, y768)); PUT(52, cmul(y1024, y1024));
    }
#undef PUT
    LAS char* px = stg + r32 * SROW + half * 16;
    LAS char* py = stg + (32 + (r32 < 20 ? r32 : 20)) * SROW + half * 16;
#pragma unroll
    for (int s = 0; s < 8; ++s) kacc = mfma32h(*(LAS u32x4*)(py + 32 * s), *(LAS u32x4*)(px + 32 * s), kacc);
    if (bt == 0) {
      VMCNT(0);
      BAR();
      tag = next_tag(epoch);
      if (tid == 0) __hip_atomic_store((GAS u64*)(ws + WS_FLAG) + blockIdx.x * 16, tag, RLX_AGENT);
      STAMP(2);
    }
  }
  {
    const float fac = 2.f * dt * isc;
#pragma unroll
    for (int i = 0; i < 12; ++i) *(LAS float*)(lds + TBL + w * 3072 + (((i & 3) + 8 * (i >> 2) + 4 * half) * 32 + r32) * 4) = kacc[i] * fac;
  }
  STAMP(3);
  wait_flags(ws, 256 + cb * 32 + r32, tag);
  STAMP(4);
  u32x4 ud[6];
  {
    const __amdgpu_buffer_rsrc_t ru = __builtin_amdgcn_make_buffer_rsrc(ws + WS_UX + a * 8192, 0, 8192, 0x00020000);
#pragma unroll
    for (int i = 0; i < 2; ++i) { const u32 off = (u32)(tid + 256 * i) * 16u; ud[2 * i] = LD_SC1(ru, off); ud[2 * i + 1] = LD_SC1(ru, off + 16u); }
    ud[4] = (u32x4){0u, 0u, 0u, 0u};
    ud[5] = LD_SC1(ru, tid == 3 ? 0u : 8192u);
  }
  BAR();
#pragma unroll
  for (int c = 0; c < 2; ++c) {
    float kk[8];
#pragma unroll
    for (int j = 0; j < 8; ++j) kk[j] = *(LAS float*)(lds + TBL + c * 6144 + tix[j]) + *(LAS float*)(lds + TBL + c * 6144 + 3072 + tix[j]);
    *(LAS u32x4*)(lds + RV + c * RVS + tid * 16) = (u32x4){pkbf(kk[0], kk[1]), pkbf(kk[2], kk[3]), pkbf(kk[4], kk[5]), pkbf(kk[6], kk[7])};
  }
  if (tid < 64) *(LAS u32x4*)(lds + RV + half * RVS + 4096 + r32 * 16) = (u32x4){0u, 0u, 0u, 0u};
#pragma unroll
  for (int i = 0; i < 3; ++i) {
    if (i < 2 || tid < 4) {
      const u32 dw[7] = {ud[2 * i].x, ud[2 * i].y, ud[2 * i].z, ud[2 * i].w, ud[2 * i + 1].x, ud[2 * i + 1].y, ud[2 * i + 1].z};
      u32 p0[6], p1[6];
#pragma unroll
      for (int k = 0; k < 6; ++k) { p0[k] = __builtin_amdgcn_perm(dw[k + 1], dw[k], 0x05040100u); p1[k] = __builtin_amdgcn_perm(dw[k + 1], dw[k], 0x07060302u); }
#pragma unroll
      for (int s = 0; s < 4; ++s) {
        *(LAS u32x2*)(lds + UC + s * UCS + (i < 2 ? tid + 256 * i + 4 : tid) * 8) = (u32x2){p0[s], p0[s + 2]};
        *(LAS u32x2*)(lds + UC + (4 + s) * UCS + (i < 2 ? tid + 256 * i + 4 : tid) * 8) = (u32x2){p1[s], p1[s + 2]};
      }
    }
  }
  BAR();
  STAMP(5);
  {
    const int c16 = lane & 15, hq = lane >> 4;
    LAS char* pa = lds + RV + ch * RVS + 2 * (2032 - 256 * 7 + 128 * hf - 16 * c16 + 8 * hq);
    LAS char* pb = lds + UC + (4 * ch + ((c16 + 1) & 3)) * UCS + 2 * (128 * hf + 8 * hq + 4 * ((c16 + 1) >> 2));
    f32x4 acc[8] = {};
#pragma unroll
    for (int rho = 0; rho < 4; ++rho) {
      u32x4 fa[8], fb[8];
#pragma unroll
      for (int m = 0; m < 8; ++m) {
        fa[m] = *(LAS u32x4*)(pa + 64 * rho + 512 * (7 - m));
        const u32x2 lo = *(volatile LAS u32x2*)(pb + 64 * rho + 512 * m), hi = *(volatile LAS u32x2*)(pb + 64 * rho + 512 * m + 8);
        fb[m] = (u32x4){lo.x, lo.y, hi.x, hi.y};
      }
#pragma unroll
      for (int aa = 0; aa < 8; ++aa)
#pragma unroll
        for (int t = aa; t < 8; ++t) acc[t] = mfma16(fa[t - aa], fb[aa], acc[t]);
    }
    STAMP(6);
#pragma unroll
    for (int t = 0; t < 8; ++t) *(LAS f32x4*)(lds + EX + w * 8192 + t * 1024 + lane * 16) = acc[t];
    BAR();
#pragma unroll
    for (int i = 0; i < 2; ++i) {
      LAS char* p = lds + EX + (2 * w + i) * 1024 + lane * 16;
      const f32x4 y0 = *(LAS f32x4*)p + *(LAS f32x4*)(p + 8192), y1 = *(LAS f32x4*)(p + 16384) + *(LAS f32x4*)(p + 24576);
      const u32x4 v = {pkbf(y0.x, y1.x), pkbf(y0.y, y1.y), pkbf(y0.z, y1.z), pkbf(y0.w, y1.w)};
      ST_SC1(san4(v), rs, WS_YX + (cb * 32 + 4 * (2 * w + i) + hq) * 8192 + jp * 256 + c16 * 16);
    }
  }
  STAMP(7);
  {
    u32x4 y[2];
  #pragma nounroll
  for (int it = 0; it < SPIN_CAP; ++it) {
      asm volatile("" ::: "memory");
      y[0] = LD_SC1(rs, WS_YX + a * 8192 + tid * 16); y[1] = LD_SC1(rs, WS_YX + a * 8192 + 4096 + tid * 16);
      if (__all(mx4(mx4(0u, y[0]), y[1]) != SENT)) break;
      __builtin_amdgcn_s_sleep(2);
    }
    STAMP(8);
#pragma unroll
    for (int i = 0; i < 2; ++i)
#pragma unroll
      for (int g = 0; g < 4; ++g) *(LAS u32*)(lds + (16 * g + (tid & 15)) * 144 + (16 * i + (tid >> 4)) * 4) = y[i][g];
    BAR();
#pragma unroll
    for (int i = 0; i < 2; ++i) {
      const u32x4 v = *(LAS u32x4*)(lds + (32 * i + (tid >> 3)) * 144 + (tid & 7) * 16);
      u32x4 o;
#pragma unroll
      for (int k = 0; k < 4; ++k) o[k] = pkbf(bflo(v[k]) + bflo(sk[4 * i + k]), bfhi(v[k]) + bfhi(sk[4 * i + k]));
      *(u32x4*)(out + (u32)((64 * jp + 32 * i + (tid >> 3)) * 1792 + cb * 128 + (tid & 7) * 16)) = o;
    }
  }
  STAMP(9);
  STAMP_FLUSH();
}

__global__ void __launch_bounds__(256, 2)
fused_kernel(const char* __restrict__ u, const u32* __restrict__ logA, const u32* __restrict__ Aim, const u32* __restrict__ Cre,
             const u32* __restrict__ Cim, const u16* __restrict__ logdt, const char* __restrict__ Dsk, const char* __restrict__ x,
             const char* __restrict__ wqkv, const char* __restrict__ wo, char* __restrict__ out, char* ws) {
  __shared__ __attribute__((aligned(16))) char smem[81920];
  LAS char* lds = (LAS char*)smem;
  if (blockIdx.x < 256) role_b(lds, x, wqkv, wo, out, ws);
  else if (blockIdx.x < 480) role_a(lds, u, logA, Aim, Cre, Cim, logdt, Dsk, out, ws);
  else {
    const u64 tag = next_tag(__hip_atomic_load((const GAS u64*)ws, RLX_AGENT));
    const int t = threadIdx.x;
    wait_flags(ws, t, tag);
    wait_flags(ws, t < 224 ? 256 + t : t, tag);
    __syncthreads();
    if (t == 0) __hip_atomic_store((GAS u64*)ws, tag, RLX_AGENT);
  }
}

extern "C" void kernel_launch(void* const* d_in, const int* in_sizes, int n_in, void* d_out, int out_size, void* d_ws,
                              size_t ws_size, hipStream_t stream) {
  fused_kernel<<<481, 256, 0, stream>>>((const char*)d_in[0], (const u32*)d_in[1], (const u32*)d_in[2], (const u32*)d_in[3],
                                        (const u32*)d_in[4], (const u16*)d_in[5], (const char*)d_in[6], (const char*)d_in[7],
                                        (const char*)d_in[8], (const char*)d_in[9], (char*)d_out, (char*)d_ws);
}
```

```cpp
#include <hip/hip_runtime.h>
#include <stdint.h>

typedef unsigned short u16;
typedef unsigned u32;
typedef unsigned long long u64;
typedef short s16x4 __attribute__((ext_vector_type(4)));
typedef __bf16 bf16x8 __attribute__((ext_vector_type(8)));
typedef __bf16 bf16x2 __attribute__((ext_vector_type(2)));
typedef _Float16 f16x8 __attribute__((ext_vector_type(8)));
typedef _Float16 f16x2 __attribute__((ext_vector_type(2)));
typedef float f32x16 __attribute__((ext_vector_type(16)));
typedef float f32x4 __attribute__((ext_vector_type(4)));
typedef float f32x2 __attribute__((ext_vector_type(2)));
typedef u32 u32x4 __attribute__((ext_vector_type(4)));
typedef u32 u32x2 __attribute__((ext_vector_type(2)));
#define LAS __attribute__((address_space(3)))
#define GAS __attribute__((address_space(1)))
#define DI __device__ __forceinline__
#define RLX_AGENT __ATOMIC_RELAXED, __HIP_MEMORY_SCOPE_AGENT

#define BMAP 0
#define PREFETCH 0
#define CODE_BEFORE 0
#define CODE_LINES 180
#define BPRIO 2

constexpr u32 WS_FLAG = 4096;
constexpr u32 WS_UCK = 73728;
constexpr u32 WS_YCK = WS_UCK + 28672;
constexpr u32 WS_KVP = 1u << 17;
constexpr u32 WS_OB = WS_KVP + (1u << 20);
constexpr u32 WS_UX = WS_OB + (2u << 20);
constexpr u32 WS_YX = WS_UX + (2u << 20);
constexpr u32 WS_END = WS_YX + (2u << 20);
constexpr u32 WS_DBG = WS_END;
constexpr int SPIN_CAP = 1 << 15;

#define STAMP(i) do {} while (0)
#define STAMP_DECL
#define STAMP_FLUSH() do {} while (0)

DI float bflo(u32 v) { return __uint_as_float(v << 16); }
DI float bfhi(u32 v) { return __uint_as_float(v & 0xffff0000u); }
DI u32 pkbf(float a, float b) { f32x2 p = {a, b}; return __builtin_bit_cast(u32, __builtin_convertvector(p, bf16x2)); }
DI u32 pkh(f32x2 p) { return __builtin_bit_cast(u32, __builtin_convertvector(p, f16x2)); }
DI float rbf(float a) { return bflo(pkbf(a, a)); }
DI u32 umax3(u32 a, u32 b, u32 c) { u32 m = a > b ? a : b; return m > c ? m : c; }
DI u32 sum4(u32 m, u32x4 v) { return (m + v.x + v.y) + v.z + v.w; }
DI u32 row_sum(u32 v) {
  v += __builtin_amdgcn_update_dpp(0u, v, 0x111, 0xf, 0xf, true); v += __builtin_amdgcn_update_dpp(0u, v, 0x112, 0xf, 0xf, true);
  v += __builtin_amdgcn_update_dpp(0u, v, 0x114, 0xf, 0xf, true); v += __builtin_amdgcn_update_dpp(0u, v, 0x118, 0xf, 0xf, true);
  return v;
}
DI u32 wave_sum(u32 v) {
  v = row_sum(v);
  return (__builtin_amdgcn_readlane(v, 15) + __builtin_amdgcn_readlane(v, 31)) + (__builtin_amdgcn_readlane(v, 47) + __builtin_amdgcn_readlane(v, 63));
}
DI u32 load_tag(const char* ws) { u32 t = *(const __attribute__((address_space(4))) u32*)ws * 1664525u + 1013904223u; asm volatile("" : "+s"(t)); return t; }
DI u64 flag_of(u32 tag) { return ((u64)(tag * 0x9E3779B1u + 1u) << 32) | tag; }
DI void put_flag(char* ws, u32 tag) { if (threadIdx.x == 0) __hip_atomic_store((GAS u64*)(ws + WS_FLAG) + blockIdx.x * 16, flag_of(tag), RLX_AGENT); }
#define SBAR() __builtin_amdgcn_sched_barrier(0)
#define PIN(x) asm volatile("" : "+v"(x))
DI f32x16 mfma32(u32x4 a, u32x4 b, f32x16 c) { return __builtin_amdgcn_mfma_f32_32x32x16_bf16(__builtin_bit_cast(bf16x8, a), __builtin_bit_cast(bf16x8, b), c, 0, 0, 0); }
DI f32x16 mfma32h(u32x4 a, u32x4 b, f32x16 c) { return __builtin_amdgcn_mfma_f32_32x32x16_f16(__builtin_bit_cast(f16x8, a), __builtin_bit_cast(f16x8, b), c, 0, 0, 0); }
DI f32x4 mfma16(u32x4 a, u32x4 b, f32x4 c) { return __builtin_amdgcn_mfma_f32_16x16x32_bf16(__builtin_bit_cast(bf16x8, a), __builtin_bit_cast(bf16x8, b), c, 0, 0, 0); }
DI u32x2 tr_read(LAS char* p) { return __builtin_bit_cast(u32x2, __builtin_amdgcn_ds_read_tr16_b64_v4i16((LAS s16x4*)p)); }
DI void dma16(const void* src, LAS char* dst) { __builtin_amdgcn_global_load_lds((const GAS void*)src, (LAS void*)dst, 16, 0, 0); }
#define VMCNT(n) asm volatile("s_waitcnt vmcnt(" #n ")" ::: "memory")
#define LGKM0() asm volatile("s_waitcnt lgkmcnt(0)" ::: "memory")
#define BAR() do { asm volatile("s_waitcnt lgkmcnt(0)" ::: "memory"); __builtin_amdgcn_s_barrier(); asm volatile("" ::: "memory"); } while (0)
#define ST_SC1(v, rs, off) __builtin_amdgcn_raw_buffer_store_b128((v), (rs), (off), 0, 16)
#define LD_SC1(rs, off) __builtin_amdgcn_raw_buffer_load_b128((rs), (off), 0, 16)
#define ST1_SC1(v, rs, off) __builtin_amdgcn_raw_buffer_store_b32((v), (rs), (off), 0, 16)
#define LD1_SC1(rs, off) __builtin_amdgcn_raw_buffer_load_b32((rs), (off), 0, 16)

DI u32 phi2(float a, float b) {
  const u32 t = pkbf(a, b);
  const f32x2 x = {bflo(t), bfhi(t)};
  const f32x2 y = x * 1.4426950408889634f;
  const float e0 = __builtin_amdgcn_fmed3f(__builtin_amdgcn_exp2f(y.x), 0.f, 1.f);
  const float e1 = __builtin_amdgcn_fmed3f(__builtin_amdgcn_exp2f(y.y), 0.f, 1.f);
  const f32x2 x1 = x + 1.0f;
  const u32 r = pkbf(__builtin_fmaxf(x1.x, e0), __builtin_fmaxf(x1.y, e1));
  const f32x2 z = (f32x2){bflo(r), bfhi(r)} + 1e-6f;
  return pkbf(z.x, z.y);
}

DI void wait_flags(const char* ws, int idx, u64 tag) {
  const GAS u64* f = (const GAS u64*)(ws + WS_FLAG) + idx * 16;
#pragma nounroll
  for (int it = 0; it < SPIN_CAP; ++it) {
    const u64 v = __hip_atomic_load(f, RLX_AGENT);
    if (__all(v == tag)) break;
    __builtin_amdgcn_s_sleep(16);
  }
}

DI void role_b(LAS char* lds, const char* __restrict__ x, const char* __restrict__ wqkv, const char* __restrict__ wo,
               char* __restrict__ out, char* ws) {
  const int b = blockIdx.x - 256, tid = threadIdx.x, lane = tid & 63;
  const int w = __builtin_amdgcn_readfirstlane(tid >> 6);
  STAMP_DECL;
  STAMP(0);
  const int h = 2 * (b & 7) + ((b >> 3) & 1), rb = b >> 4;
  const int start = (rb + 3 * (h & 1)) % 7;
  const __amdgpu_buffer_rsrc_t rs = __builtin_amdgcn_make_buffer_rsrc(ws, 0, WS_END, 0x00020000);
  const u32 kvp_off = WS_KVP + (h * 16 + rb) * 4096 + tid * 16;
  const u32 ob_off = WS_OB + ((rb * 4 + w) * 16 + h) * 2048 + lane * 16;

  constexpr int WR = 49152, KVI = 45056, WOE = 16384;
#define XS(i) ((i) == 0 ? 16384 : (i) == 1 ? 0 : 32768)
#define QS(cc) ((cc) < 2 ? WR + 24576 + (cc) * 4096 : (cc) < 6 ? XS(1) + ((cc) - 2) * 1024 : WR)
#define QD(cc) ((cc) >= 2 && (cc) < 6 ? 4096 : 1024)
  const int wsb = (56 * h) & ~15;
  const int coff = (h & 1) * 4;
  const int wone = (h & 1) ? 0 : 28;
  const char* xs0 = x + (u32)((128 * rb + 32 * w + (lane >> 3)) * 896 + (((lane & 7) ^ (lane >> 4)) << 4));
  const char* xs1 = x + (u32)((128 * rb + 32 * w + 8 + (lane >> 3)) * 896 + (((lane & 7) ^ (4 + (lane >> 4))) << 4));
  const char* wsrc = wqkv + (u32)((16 * w + (lane >> 2)) * 2688 + wsb + (lane & 3) * 16);
  auto chunk = [&](int cc) { int c = cc + start; return c >= 7 ? c - 7 : c; };
  auto issue_x = [&](int cc, int i) { dma16(((i & 1) ? xs1 : xs0) + chunk(cc) * 128 + (i >> 1) * (16 * 896), lds + XS(cc % 3) + w * 4096 + i * 1024); };
  auto issue_w = [&](int cc, int i) { dma16(wsrc + chunk(cc) * (64 * 2688) + 896 * (i + 1), lds + WR + (cc & 3) * 8192 + i * 4096 + w * 1024); };
  auto issue_q = [&](int cc) { dma16(wsrc + chunk(cc) * (64 * 2688), lds + QS(cc) + w * QD(cc)); };
#pragma unroll
  for (int cc = 0; cc < 4; ++cc) {
    issue_w(cc, 0); issue_w(cc, 1);
    if (cc < 3) { issue_x(cc, 0); issue_x(cc, 1); issue_x(cc, 2); issue_x(cc, 3); }
  }
  __builtin_amdgcn_s_setprio(BPRIO);
  const u32 tag = load_tag(ws);

  const int r32 = lane & 31, half = lane >> 5;
  const int xrd = w * 4096 + r32 * 128 + ((half ^ ((r32 >> 1) & 7)) << 4);
  const int wrd = (8 * half + ((lane >> 2) & 3)) * 64 + ((lane >> 4) & 1) * 32 + (lane & 3) * 8;
  LAS char* wlo = lds + wrd; LAS char* whi = lds + WR + wrd;
  PIN(whi);
  u32x4 xf[28], wf[2][8];
  f32x16 ak = {}, av = {};
#pragma unroll
  for (int cc = 0; cc < 8; ++cc) {
    if (cc < 7) {
      if (cc == 0) VMCNT(14); else if (cc < 6) VMCNT(8); else VMCNT(7);
      BAR();
      if (cc == 0) { STAMP(1); }
#pragma unroll
      for (int s = 0; s < 4; ++s) {
        xf[cc * 4 + s] = *(LAS u32x4*)(lds + XS(cc % 3) + (xrd ^ (s << 5)));
        LAS char* p = whi + (cc & 3) * 8192 + s * 1024;
        const u32x2 k0 = tr_read(p), k1 = tr_read(p + 256), v0 = tr_read(p + 4096), v1 = tr_read(p + 4096 + 256);
        wf[cc & 1][2 * s] = (u32x4){k0.x, k0.y, k1.x, k1.y}; wf[cc & 1][2 * s + 1] = (u32x4){v0.x, v0.y, v1.x, v1.y};
      }
      SBAR();
    }
    if (cc > 0) {
#pragma unroll
      for (int i = 0; i < 8; ++i) {
        if (i & 1) av = mfma32(xf[(cc - 1) * 4 + (i >> 1)], wf[(cc - 1) & 1][i], av);
        else ak = mfma32(xf[(cc - 1) * 4 + (i >> 1)], wf[(cc - 1) & 1][i], ak);
        if (cc <= 4 && i < 4) issue_x(cc + 2, i);
        else if (cc <= 3 && i < 6) issue_w(cc + 3, i - 4);
        else if (cc == 4 && i < 6) issue_q(i - 4);
        else if (cc == 5 && i < 5) issue_q(i + 2);
        SBAR();
      }
    }
    if (cc < 7) {
#pragma unroll
      for (int s = 0; s < 4; ++s) { PIN(xf[cc * 4 + s]); PIN(wf[cc & 1][2 * s]); PIN(wf[cc & 1][2 * s + 1]); }
    }
  }
  STAMP(2);
  {
    u32x4 pk[2], pv[2];
#pragma unroll
    for (int s = 0; s < 2; ++s)
#pragma unroll
      for (int j = 0; j < 4; ++j) {
        pk[s][j] = phi2(ak[8 * s + 2 * j], ak[8 * s + 2 * j + 1]);
        const u32 v = pkbf(av[8 * s + 2 * j], av[8 * s + 2 * j + 1]);
        pv[s][j] = r32 == wone ? 0x3F803F80u : v;
      }
    f32x16 kv = {};
    kv = mfma32(pk[0], pv[0], kv); kv = mfma32(pk[1], pv[1], kv);
#pragma unroll
    for (int g = 0; g < 4; ++g) *(LAS f32x4*)(lds + XS(2) + w * 4096 + g * 1024 + lane * 16) = (f32x4){kv[4 * g], kv[4 * g + 1], kv[4 * g + 2], kv[4 * g + 3]};
    BAR();
    f32x4 s4 = *(LAS f32x4*)(lds + XS(2) + tid * 16);
#pragma unroll
    for (int ww = 1; ww < 4; ++ww) s4 += *(LAS f32x4*)(lds + XS(2) + ww * 4096 + tid * 16);
    u32x4 v = __builtin_bit_cast(u32x4, s4);
    const bool ck = lane == wone + 1;
    if (ck) v = (u32x4){0u, tag, 0u, 0u};
    const u32 c = tag - wave_sum(sum4(0u, v));
    if (ck) v.x = c;
    ST_SC1(v, rs, kvp_off);
    put_flag(ws, tag);
  }
  STAMP(3);
  const int rt = h & 3, ct = h >> 2;
  const int sg = (lane & 15) ^ (4 * (lane >> 4));
  const bool live = ct < 3 || sg < 8;
  const char* wosrc = wo + (u32)((28 * w + (lane >> 4)) * 896 + ct * 256 + sg * 16);
  auto issue_wo1 = [&](int pc, int ii) {
    if (live) dma16(wosrc + (112 * pc + 4 * ii) * 896, lds + ((pc & 1) ? WR : WOE) + (w * 7 + ii) * 1024);
  };
  auto issue_wo = [&](int pc) {
    if (live) {
#pragma unroll
      for (int ii = 0; ii < 7; ++ii) dma16(wosrc + (112 * pc + 4 * ii) * 896, lds + ((pc & 1) ? WR : WOE) + (w * 7 + ii) * 1024);
    }
  };
  const int woq = (lane >> 2) & 3, gam = 4 * ((lane >> 4) & 1) + (lane & 3), ng = 4 * (gam & 1) + (gam >> 1);
  LAS char* wob = lds + WOE + (14 * half + woq) * 256 + (w ^ ((2 * half + woq) & 3)) * 64 + 8 * ng;
  PIN(wob);
  u32x4 wof[32];
  auto read_wo = [&](int pc) {
#pragma unroll
    for (int hl = 0; hl < 4; ++hl) {
      LAS char* p = wob + ((pc & 1) ? WR - WOE : 0) + 28 * hl * 256;
      const u32x2 r0 = tr_read(p), r1 = tr_read(p + 1024), r2 = tr_read(p + 2048), r3 = tr_read(p + 3072);
      wof[pc * 8 + 2 * hl] = (u32x4){r0.x, r0.y, r1.x, r1.y};
      wof[pc * 8 + 2 * hl + 1] = (u32x4){r2.x, r2.y, r3.x, 0u};
    }
    SBAR();
  };
  auto pin_wo = [&](int pc) {
#pragma unroll
    for (int i = 0; i < 8; ++i) PIN(wof[pc * 8 + i]);
  };
  if (w == 0) VMCNT(2); else VMCNT(1);
  BAR();
  u32x4 pq[2];
  {
    f32x16 aq = {};
    u32x4 qf[2][4];
    auto read_q = [&](int cc) {
#pragma unroll
      for (int s = 0; s < 4; ++s) {
        LAS char* p = (QS(cc) >= WR ? whi + (QS(cc) - WR) : wlo + QS(cc)) + s * QD(cc);
        const u32x2 lo = tr_read(p), hi = tr_read(p + 256);
        qf[cc & 1][s] = (u32x4){lo.x, lo.y, hi.x, hi.y};
      }
      SBAR();
    };
    read_q(0);
#pragma unroll
    for (int cc = 0; cc < 7; ++cc) {
#pragma unroll
      for (int s = 0; s < 4; ++s) PIN(qf[cc & 1][s]);
      if (cc < 6) read_q(cc + 1);
#pragma unroll
      for (int s = 0; s < 4; ++s) {
        aq = mfma32(qf[cc & 1][s], xf[cc * 4 + s], aq);
        if (s == 0) issue_wo1(0, cc);
        SBAR();
      }
    }
#pragma unroll
    for (int s = 0; s < 2; ++s) {
#pragma unroll
      for (int j = 0; j < 4; ++j) pq[s][j] = phi2(aq[8 * s + 2 * j], aq[8 * s + 2 * j + 1]);
      PIN(pq[s]);
    }
  }
  STAMP(4);
  VMCNT(0);
  BAR();
  read_wo(0);
  issue_wo(1);
  pin_wo(0);
  STAMP(5);
  {
    u32x4 d[16];
#pragma nounroll
    for (int it = 0; it < SPIN_CAP; ++it) {
      asm volatile("" ::: "memory");
#pragma unroll
      for (int p = 0; p < 16; ++p) d[p] = LD_SC1(rs, WS_KVP + h * 65536 + p * 4096 + tid * 16);
      u32 m = 0;
#pragma unroll
      for (int p = 0; p < 16; ++p) m = sum4(m, d[p]);
      if (wave_sum(m) == 16u * tag) break;
    }
    f32x4 s4 = __builtin_bit_cast(f32x4, d[0]);
#pragma unroll
    for (int p = 1; p < 16; ++p) s4 += __builtin_bit_cast(f32x4, d[p]);
    const int dw = 8 * w + 4 * half - coff;
    u32x2 o2 = {pkbf(s4.x, s4.y), pkbf(s4.z, s4.w)};
    if (dw < 0 || dw >= 28) o2 = (u32x2){0u, 0u};
    *(LAS u32x2*)(lds + KVI + lane * 32 + w * 8) = o2;
  }
  STAMP(6);
  VMCNT(0);
  BAR();
  {
    const int i = (r32 & 3) + 4 * (r32 >> 3), hh = (r32 >> 2) & 1;
    const int ew = i < 14 ? 14 * hh + i + coff : wone;
    LAS char* p = lds + KVI + (ew + 32 * half) * 32;
    const u32x4 a0 = *(LAS u32x4*)p, a1 = *(LAS u32x4*)(p + 16);
    f32x16 nm = {};
    nm = mfma32(a0, pq[0], nm); nm = mfma32(a1, pq[1], nm);
    const float den = rbf(rbf(nm[14]) + 1e-6f);
    const float rc = __builtin_amdgcn_rcpf(den);
    u32 o[8];
#pragma unroll
    for (int j = 0; j < 7; ++j) { const u32 t = pkbf(nm[2 * j], nm[2 * j + 1]); o[j] = pkbf(bflo(t) * rc, bfhi(t) * rc); }
    o[7] = tag - ((o[0] + o[1] + o[2]) + (o[3] + o[4] + o[5]) + o[6]);
    ST_SC1(((u32x4){o[0], o[1], o[2], o[3]}), rs, ob_off);
    ST_SC1(((u32x4){o[4], o[5], o[6], o[7]}), rs, ob_off + 1024);
  }
  STAMP(7);
  read_wo(1);
  issue_wo(2);
  pin_wo(1);
  BAR();
  issue_wo(3);
  VMCNT(7); BAR();
  read_wo(2);
  STAMP(8);
  {
    u32x4 d[8];
#pragma nounroll
    for (int it = 0; it < SPIN_CAP; ++it) {
      asm volatile("" ::: "memory");
#pragma unroll
      for (int p = 0; p < 8; ++p) d[p] = LD_SC1(rs, WS_OB + ((rb * 4 + rt) * 16 + 4 * w) * 2048 + p * 1024 + lane * 16);
      u32 m = 0;
#pragma unroll
      for (int p = 0; p < 8; ++p) m = sum4(m, d[p]);
      if (__all(m == 4u * tag)) break;
    }
#pragma unroll
    for (int p = 1; p < 8; p += 2) d[p].w = 0u;
    STAMP(9);
    pin_wo(2);
    VMCNT(0);
    BAR();
#pragma unroll
    for (int p = 0; p < 8; ++p) *(LAS u32x4*)(lds + WOE + (8 * w + p) * 1024 + lane * 16) = d[p];
    read_wo(3);
    pin_wo(3);
    BAR();
  }
  if (ct < 3 || w < 2) {
    f32x16 acc = {};
#pragma unroll
    for (int k = 0; k < 32; ++k) acc = mfma32(wof[k], *(LAS u32x4*)(lds + WOE + k * 1024 + lane * 16), acc);
    char* dst = out + (u32)((128 * rb + 32 * rt + r32) * 1792 + 896 + ct * 256 + w * 64 + half * 32);
    *(u32x4*)dst = (u32x4){pkbf(acc[0], acc[1]), pkbf(acc[2], acc[3]), pkbf(acc[4], acc[5]), pkbf(acc[6], acc[7])};
    *(u32x4*)(dst + 16) = (u32x4){pkbf(acc[8], acc[9]), pkbf(acc[10], acc[11]), pkbf(acc[12], acc[13]), pkbf(acc[14], acc[15])};
  }
  STAMP(10);
  STAMP_FLUSH();
}

DI f32x2 rot(f32x2 b) { return (f32x2){-b.y, b.x}; }
DI f32x2 cm1(f32x2 a, f32x2 b) { return (f32x2){a.x, a.x} * b; }
DI f32x2 cm2(f32x2 a, f32x2 br, f32x2 t) { return __builtin_elementwise_fma((f32x2){a.y, a.y}, br, t); }
DI f32x2 cexp_p(float a2, float f, float p) {
  const float m = __builtin_amdgcn_exp2f(a2 * p), ph = __builtin_amdgcn_fractf(f * p);
  return (f32x2){m, m} * (f32x2){__builtin_amdgcn_cosf(ph), __builtin_amdgcn_sinf(ph)};
}
DI u32 wave_umax(u32 v) {
  u32 t;
  t = __builtin_amdgcn_update_dpp(0u, v, 0x111, 0xf, 0xf, false); v = v > t ? v : t;
  t = __builtin_amdgcn_update_dpp(0u, v, 0x112, 0xf, 0xf, false); v = v > t ? v : t;
  t = __builtin_amdgcn_update_dpp(0u, v, 0x114, 0xf, 0xf, false); v = v > t ? v : t;
  t = __builtin_amdgcn_update_dpp(0u, v, 0x118, 0xf, 0xf, false); v = v > t ? v : t;
  const u32 a = __builtin_amdgcn_readlane(v, 15), b = __builtin_amdgcn_readlane(v, 31), c = __builtin_amdgcn_readlane(v, 47), d = __builtin_amdgcn_readlane(v, 63);
  const u32 ab = a > b ? a : b, cd = c > d ? c : d;
  return ab > cd ? ab : cd;
}

DI void role_a(LAS char* lds, const char* __restrict__ u, const u32* __restrict__ logA, const u32* __restrict__ Aim,
               const u32* __restrict__ Cre, const u32* __restrict__ Cim, const u16* __restrict__ logdt,
               const char* __restrict__ Dsk, const char* __restrict__ wo, char* __restrict__ out, char* ws) {
  const int a = blockIdx.x, cb = a >> 5, jp = a & 31, tid = threadIdx.x, lane = tid & 63;
  const int w = __builtin_amdgcn_readfirstlane(tid >> 6);
  STAMP_DECL;
  STAMP(0);
  const __amdgpu_buffer_rsrc_t rs = __builtin_amdgcn_make_buffer_rsrc(ws, 0, WS_END, 0x00020000);
  const int ch = w >> 1, hf = w & 1, d = 2 * a + ch;
  const int pi = d * 128 + hf * 64 + lane;
  u32x4 ub[4];
  if (w < 2) {
#pragma unroll
    for (int i = 0; i < 4; ++i) ub[i] = *(const u32x4*)(u + (u32)((64 * jp + 4 * (tid & 15) + i) * 896 + cb * 128 + (tid >> 4) * 16));
  }
  const u32 pA = logA[pi], pI = Aim[pi], pR = Cre[pi], pC = Cim[pi];
  const float ldt = bflo(logdt[d]);
  u32x4 us[2];
#pragma unroll
  for (int i = 0; i < 2; ++i) us[i] = *(const u32x4*)(u + (u32)((64 * jp + 32 * i + (tid >> 3)) * 896 + cb * 128 + (tid & 7) * 16));
  const u32x4 dk = *(const u32x4*)(Dsk + cb * 128 + (tid & 7) * 16);
  u32 dummy = 0;
  const u32 tag = load_tag(ws);
  int tix[8];
#pragma unroll
  for (int j = 0; j < 8; ++j) {
    const int p = (int)rbf((float)(2047 - 8 * tid - j));
    const int low = p & 7, blk = p >> 8;
    const int k4 = (blk < 4 ? blk : 4) * 4;
    const int sh = (0x32210 >> k4) & 15;
    const int base = blk + ((0xCBA70 >> k4) & 15);
    tix[j] = ((base + (low >> sh)) * 32 + ((p >> 3) & 31)) * 4;
    PIN(tix[j]);
  }
  if (w < 2) {
    u32x4 ck;
#pragma unroll
    for (int k = 0; k < 4; ++k) {
      const u32x4 v = {ub[0][k], ub[1][k], ub[2][k], ub[3][k]};
      ST_SC1(v, rs, WS_UX + (cb * 32 + 4 * (tid >> 4) + k) * 8192 + jp * 256 + (tid & 15) * 16);
      ck[k] = tag - row_sum(sum4(0u, v));
    }
    if ((tid & 15) == 15) ST_SC1(ck, rs, WS_UCK + a * 128 + (tid >> 4) * 16);
  }
  put_flag(ws, tag);
  STAMP(1);
  constexpr int SROW = 272, STG = 53 * SROW, TBL = 4 * STG, UC = 0, UCS = 4160, RV = 8 * UCS, RVS = 4608, EX = RV + 2 * RVS;
  const float dt = rbf(expf(ldt));
  const u32 mx = wave_umax(umax3(umax3(pR & 0x7fffu, (pR >> 16) & 0x7fffu, pC & 0x7fffu), (pC >> 16) & 0x7fffu, 0u));
  int ex = mx >> 7; ex = ex < 13 ? 13 : ex > 254 ? 254 : ex;
  const float sc = __uint_as_float((u32)(266 - ex) << 23), isc = __uint_as_float((u32)(ex - 12) << 23);
  f32x16 kacc = {};
  LAS char* stg = lds + w * STG;
  const int r32 = lane & 31, half = lane >> 5;
  LAS char* px = stg + r32 * SROW + half * 16;
  LAS char* py = stg + (32 + (r32 < 20 ? r32 : 20)) * SROW + half * 16;
  u32x4 fx[8], fy[8];
  u32x4 ud[3]; u32 uck;
  const __amdgpu_buffer_rsrc_t ru = __builtin_amdgcn_make_buffer_rsrc(ws + WS_UX + a * 8192, 0, 8192, 0x00020000);
  const int npc = w < 3 ? 9 : 8;
  auto load_u = [&]() {
    ud[0] = LD_SC1(ru, w * 2048 + lane * 16); ud[1] = LD_SC1(ru, w * 2048 + 1024 + lane * 16);
    ud[2] = LD_SC1(ru, lane < 16 ? w * 2048 + 2048 + lane * 16 : 8192);
    uck = LD1_SC1(rs, lane < npc ? WS_UCK + (cb * 32 + 8 * w + lane) * 128 + jp * 4 : WS_END);
  };
#pragma unroll
  for (int bt = 0; bt < 2; ++bt) {
    const float la = bt ? bfhi(pA) : bflo(pA), ai = bt ? bfhi(pI) : bflo(pI), cr = bt ? bfhi(pR) : bflo(pR), ci = bt ? bfhi(pC) : bflo(pC);
    const float a2 = -rbf(expf(la)) * dt * 1.4426950408889634f, f = ai * dt * 0.15915494309189535f;
    LAS u32* sw = (LAS u32*)(stg + lane * 4);
#define STEP(p0, b0, b0r, r0, p1, b1, b1r, r1) do { \
      const f32x2 t0_ = cm1(p0, b0), t1_ = cm1(p1, b1); const u32 c0_ = pkh(p0), c1_ = pkh(p1); SBAR(); \
      p0 = cm2(p0, b0r, t0_); p1 = cm2(p1, b1r, t1_); sw[(r0) * (SROW / 4)] = c0_; sw[(r1) * (SROW / 4)] = c1_; SBAR(); } while (0)
    const f32x2 z8 = cexp_p(a2, f, 8.f), y1 = cexp_p(a2, -f, 1.f), yq = cexp_p(a2, -f, 256.f);
    const f32x2 z8r = rot(z8), y1r = rot(y1), yqr = rot(yq);
    f32x2 x0 = (f32x2){cr, ci} * sc;
    f32x2 x1 = cm2(x0, z8r, cm1(x0, z8));
    const f32x2 z16 = cm2(z8, z8r, cm1(z8, z8)), z16r = rot(z16);
    SBAR();
#pragma unroll
    for (int r = 0; r < 30; r += 2) {
      STEP(x0, z16, z16r, r, x1, z16, z16r, r + 1);
      if (bt == 1 && r < 16) { kacc = mfma32h(fy[r >> 1], fx[r >> 1], kacc); SBAR(); }
    }
    sw[30 * (SROW / 4)] = pkh(x0); sw[31 * (SROW / 4)] = pkh(x1);
    if (bt == 1) { STAMP(2); load_u(); SBAR(); }
    {
      f32x2 p = y1, q = yq, y2, y4, y6, v;
      sw[32 * (SROW / 4)] = 0x00003C00u;
      STEP(p, y1, y1r, 33, q, yq, yqr, 40); y2 = p;
      STEP(p, y1, y1r, 34, q, yq, yqr, 44);
      STEP(p, y1, y1r, 35, q, yq, yqr, 46); y4 = p;
      STEP(p, y1, y1r, 36, q, yq, yqr, 48);
      STEP(p, y1, y1r, 37, q, yq, yqr, 49); y6 = p;
      STEP(p, y1, y1r, 38, q, yq, yqr, 50);
      v = cm2(y4, yqr, cm1(y4, yq));
      sw[39 * (SROW / 4)] = pkh(p);
      STEP(v, yq, yqr, 42, q, yq, yqr, 51);
      y2 = cm2(y2, yqr, cm1(y2, yq)); y6 = cm2(y6, yqr, cm1(y6, yq));
      sw[52 * (SROW / 4)] = pkh(q); sw[45 * (SROW / 4)] = pkh(v);
      v = cm2(v, yqr, cm1(v, yq));
      sw[41 * (SROW / 4)] = pkh(y2); sw[43 * (SROW / 4)] = pkh(y6); sw[47 * (SROW / 4)] = pkh(v);
    }
#undef STEP
#pragma unroll
    for (int s = 0; s < 8; ++s) { fy[s] = *(LAS u32x4*)(py + 32 * s); fx[s] = *(LAS u32x4*)(px + 32 * s); }
    SBAR();
  }
  STAMP(3);
#pragma unroll
  for (int s = 0; s < 8; ++s) kacc = mfma32h(fy[s], fx[s], kacc);
  u32 sk[8];
#pragma unroll
  for (int i = 0; i < 2; ++i)
#pragma unroll
    for (int k = 0; k < 4; ++k) { sk[4 * i + k] = pkbf(bflo(us[i][k]) * bflo(dk[k]), bfhi(us[i][k]) * bfhi(dk[k])); PIN(sk[4 * i + k]); }
  {
    const float fac = 2.f * dt * isc;
#pragma unroll
    for (int i = 0; i < 12; ++i) *(LAS float*)(lds + TBL + w * 3072 + (((i & 3) + 8 * (i >> 2) + 4 * half) * 32 + r32) * 4) = kacc[i] * fac;
  }
  STAMP(4);
  BAR();
#pragma unroll
  for (int c = 0; c < 2; ++c) {
    float kk[8];
#pragma unroll
    for (int j = 0; j < 8; ++j) kk[j] = *(LAS float*)(lds + TBL + c * 6144 + tix[j]) + *(LAS float*)(lds + TBL + c * 6144 + 3072 + tix[j]);
    *(LAS u32x4*)(lds + RV + c * RVS + tid * 16) = (u32x4){pkbf(kk[0], kk[1]), pkbf(kk[2], kk[3]), pkbf(kk[4], kk[5]), pkbf(kk[6], kk[7])};
  }
  if (tid < 64) *(LAS u32x4*)(lds + RV + half * RVS + 4096 + r32 * 16) = (u32x4){0u, 0u, 0u, 0u};
  SBAR();
#pragma nounroll
  for (int it = 0; it < SPIN_CAP; ++it) {
    if (wave_sum(sum4(sum4(sum4(uck, ud[0]), ud[1]), ud[2])) == npc * tag) break;
    asm volatile("" ::: "memory");
    load_u();
  }
  asm volatile("" :: "v"(dummy));
#pragma unroll
  for (int i = 0; i < 2; ++i) {
    const u32 nx = __builtin_amdgcn_update_dpp(__builtin_amdgcn_readlane(ud[i + 1].x, 0), ud[i].x, 0x130, 0xf, 0xf, false);
    const u32 dw[5] = {ud[i].x, ud[i].y, ud[i].z, ud[i].w, nx};
    LAS char* p = lds + UC + (128 * w + 64 * i + lane + 4) * 8;
#pragma unroll
    for (int c = 0; c < 2; ++c) {
      u32 pr[4];
#pragma unroll
      for (int k = 0; k < 4; ++k) pr[k] = __builtin_amdgcn_perm(dw[k + 1], dw[k], c ? 0x07060302u : 0x05040100u);
      *(LAS u32x2*)(p + (4 * c) * UCS) = (u32x2){pr[0], pr[2]};
      *(LAS u32x2*)(p + (4 * c + 1) * UCS) = (u32x2){pr[1], pr[3]};
      *(LAS u32*)(p + (4 * c + 2) * UCS - 4) = pr[0]; *(LAS u32*)(p + (4 * c + 2) * UCS) = pr[2];
      *(LAS u32*)(p + (4 * c + 3) * UCS - 4) = pr[1]; *(LAS u32*)(p + (4 * c + 3) * UCS) = pr[3];
    }
  }
  if (w == 0) {
    const u32 u0 = __builtin_amdgcn_readlane(ud[0].x, 0);
    const int cs = lane >> 3, ix = lane & 7, last = (cs & 2) ? 6 : 7;
    const u32 v = ((cs & 1) && ix == last) ? (cs & 4 ? u0 & 0xffff0000u : u0 << 16) : 0u;
    if (ix <= last) *(LAS u32*)(lds + UC + cs * UCS + ix * 4) = v;
  }
  BAR();
  STAMP(5);
  {
    const int c16 = lane & 15, hq = lane >> 4;
    LAS char* pa = lds + RV + ch * RVS + 2 * (2032 - 256 * 7 + 128 * hf - 16 * c16 + 8 * hq);
    LAS char* pb = lds + UC + (4 * ch + ((c16 + 1) & 3)) * UCS + 2 * (128 * hf + 8 * hq + 4 * ((c16 + 1) >> 2));
    f32x4 acc[8] = {};
    u32x4 fa[2][8], fb[2][8];
    auto ldf = [&](int rho, int m) {
      fa[rho & 1][m] = *(LAS u32x4*)(pa + 64 * rho + 512 * (7 - m));
      const u32x2 lo = *(volatile LAS u32x2*)(pb + 64 * rho + 512 * m), hi = *(volatile LAS u32x2*)(pb + 64 * rho + 512 * m + 8);
      fb[rho & 1][m] = (u32x4){lo.x, lo.y, hi.x, hi.y};
    };
#pragma unroll
    for (int m = 0; m < 8; ++m) ldf(0, m);
#pragma unroll
    for (int rho = 0; rho < 4; ++rho) {
      SBAR();
#pragma unroll
      for (int g = 0; g < 8; ++g) {
#pragma unroll
        for (int aa = 0; aa <= g; ++aa) {
          if (aa + g < 8) acc[aa + g] = mfma16(fa[rho & 1][g], fb[rho & 1][aa], acc[aa + g]);
          if (aa < g && aa + g < 8) acc[aa + g] = mfma16(fa[rho & 1][aa], fb[rho & 1][g], acc[aa + g]);
        }
        if (rho < 3) { ldf(rho + 1, g); SBAR(); }
      }
    }
    STAMP(6);
#pragma unroll
    for (int t = 0; t < 8; ++t) *(LAS f32x4*)(lds + EX + w * 8192 + t * 1024 + lane * 16) = acc[t];
    BAR();
#pragma unroll
    for (int i = 0; i < 2; ++i) {
      LAS char* p = lds + EX + (2 * w + i) * 1024 + lane * 16;
      const f32x4 y0 = *(LAS f32x4*)p + *(LAS f32x4*)(p + 8192), y1 = *(LAS f32x4*)(p + 16384) + *(LAS f32x4*)(p + 24576);
      const u32x4 v = {pkbf(y0.x, y1.x), pkbf(y0.y, y1.y), pkbf(y0.z, y1.z), pkbf(y0.w, y1.w)};
      ST_SC1(v, rs, WS_YX + (cb * 32 + 4 * (2 * w + i) + hq) * 8192 + jp * 256 + c16 * 16);
      const u32 ck = tag - row_sum(sum4(0u, v));
      if (c16 == 15) ST1_SC1(ck, rs, WS_YCK + a * 128 + (4 * (2 * w + i) + hq) * 4);
    }
  }
  STAMP(7);
  {
    u32x4 y[2];
    __builtin_amdgcn_s_sleep(8);
#pragma nounroll
    for (int it = 0; it < SPIN_CAP; ++it) {
      asm volatile("" ::: "memory");
      y[0] = LD_SC1(rs, WS_YX + a * 8192 + tid * 16); y[1] = LD_SC1(rs, WS_YX + a * 8192 + 4096 + tid * 16);
      const u32 ck = LD1_SC1(rs, lane < 8 ? WS_YCK + (cb * 32 + 16 * (lane >> 2) + 4 * w + (lane & 3)) * 128 + jp * 4 : WS_END);
      if (wave_sum(sum4(sum4(ck, y[0]), y[1])) == 8u * tag) break;
      __builtin_amdgcn_s_sleep(2);
    }
    STAMP(8);
#pragma unroll
    for (int i = 0; i < 2; ++i)
#pragma unroll
      for (int g = 0; g < 4; ++g) *(LAS u32*)(lds + (16 * g + (tid & 15)) * 144 + (16 * i + (tid >> 4)) * 4) = y[i][g];
    BAR();
#pragma unroll
    for (int i = 0; i < 2; ++i) {
      const u32x4 v = *(LAS u32x4*)(lds + (32 * i + (tid >> 3)) * 144 + (tid & 7) * 16);
      u32x4 o;
#pragma unroll
      for (int k = 0; k < 4; ++k) o[k] = pkbf(bflo(v[k]) + bflo(sk[4 * i + k]), bfhi(v[k]) + bfhi(sk[4 * i + k]));
      *(u32x4*)(out + (u32)((64 * jp + 32 * i + (tid >> 3)) * 1792 + cb * 128 + (tid & 7) * 16)) = o;
    }
  }
  STAMP(9);
  STAMP_FLUSH();
}

__global__ void __launch_bounds__(256, 2)
fused_kernel(const char* __restrict__ u, const u32* __restrict__ logA, const u32* __restrict__ Aim, const u32* __restrict__ Cre,
             const u32* __restrict__ Cim, const u16* __restrict__ logdt, const char* __restrict__ Dsk, const char* __restrict__ x,
             const char* __restrict__ wqkv, const char* __restrict__ wo, char* __restrict__ out, char* ws) {
  __shared__ __attribute__((aligned(16))) char smem[81920];
  LAS char* lds = (LAS char*)smem;
  asm volatile("" :: "s"(u), "s"(logA), "s"(Aim), "s"(Cre), "s"(Cim), "s"(logdt), "s"(Dsk), "s"(x), "s"(wqkv), "s"(wo), "s"(out), "s"(ws));
  if (blockIdx.x >= 256) role_b(lds, x, wqkv, wo, out, ws);
  else if (blockIdx.x < 224) role_a(lds, u, logA, Aim, Cre, Cim, logdt, Dsk, wo, out, ws);
  else {
    const int t = threadIdx.x;
    if (blockIdx.x == 224) {
      const u32 tag = load_tag(ws);
      __builtin_amdgcn_s_sleep(127);
      wait_flags(ws, 256 + t, flag_of(tag));
      wait_flags(ws, t < 224 ? t : 0, flag_of(tag));
      __syncthreads();
      if (t == 0) __hip_atomic_store((GAS u32*)ws, tag, RLX_AGENT);
    }
  }
}

extern "C" void kernel_launch(void* const* d_in, const int* in_sizes, int n_in, void* d_out, int out_size, void* d_ws,
                              size_t ws_size, hipStream_t stream) {
  fused_kernel<<<512, 256, 0, stream>>>((const char*)d_in[0], (const u32*)d_in[1], (const u32*)d_in[2], (const u32*)d_in[3],
                                        (const u32*)d_in[4], (const u16*)d_in[5], (const char*)d_in[6], (const char*)d_in[7],
                                        (const char*)d_in[8], (const char*)d_in[9], (char*)d_out, (char*)d_ws);
}
```

```cpp
#define STAMPS
#define READOUT 0x30
#include <hip/hip_runtime.h>
#include <stdint.h>

typedef unsigned short u16;
typedef unsigned u32;
typedef unsigned long long u64;
typedef short s16x4 __attribute__((ext_vector_type(4)));
typedef __bf16 bf16x8 __attribute__((ext_vector_type(8)));
typedef __bf16 bf16x2 __attribute__((ext_vector_type(2)));
typedef _Float16 f16x8 __attribute__((ext_vector_type(8)));
typedef _Float16 f16x2 __attribute__((ext_vector_type(2)));
typedef float f32x16 __attribute__((ext_vector_type(16)));
typedef float f32x4 __attribute__((ext_vector_type(4)));
typedef float f32x2 __attribute__((ext_vector_type(2)));
typedef u32 u32x4 __attribute__((ext_vector_type(4)));
typedef u32 u32x2 __attribute__((ext_vector_type(2)));
#define LAS __attribute__((address_space(3)))
#define GAS __attribute__((address_space(1)))
#define DI __device__ __forceinline__
#define RLX_AGENT __ATOMIC_RELAXED, __HIP_MEMORY_SCOPE_AGENT

#define BMAP 0
#define PREFETCH 0
#define CODE_BEFORE 0
#define CODE_LINES 180
#define BPRIO 2

constexpr u32 WS_FLAG = 4096;
constexpr u32 WS_UCK = 73728;
constexpr u32 WS_YCK = WS_UCK + 28672;
constexpr u32 WS_KVP = 1u << 17;
constexpr u32 WS_OB = WS_KVP + (1u << 20);
constexpr u32 WS_UX = WS_OB + (2u << 20);
constexpr u32 WS_YX = WS_UX + (2u << 20);
constexpr u32 WS_END = WS_YX + (2u << 20);
constexpr u32 WS_DBG = WS_END;
constexpr int SPIN_CAP = 1 << 15;

#define STAMP(i) do { st_[i] = __builtin_amdgcn_s_memrealtime(); } while (0)
#define STAMP_DECL u64 st_[16] = {}
#define STAMP_FLUSH() do { if (lane == 0) { GAS u64* p_ = (GAS u64*)(ws + WS_DBG) + (blockIdx.x * 4 + w) * 16; _Pragma("unroll") for (int i_ = 0; i_ < 16; ++i_) __hip_atomic_store(p_ + i_, st_[i_], RLX_AGENT); } \
   asm volatile("s_waitcnt vmcnt(0)" ::: "memory"); __syncthreads(); if (threadIdx.x == 0) __hip_atomic_store((GAS u64*)(ws + WS_FLAG) + blockIdx.x * 16 + 8, flag_of(tag), RLX_AGENT); } while (0)

DI float bflo(u32 v) { return __uint_as_float(v << 16); }
DI float bfhi(u32 v) { return __uint_as_float(v & 0xffff0000u); }
DI u32 pkbf(float a, float b) { f32x2 p = {a, b}; return __builtin_bit_cast(u32, __builtin_convertvector(p, bf16x2)); }
DI u32 pkh(f32x2 p) { return __builtin_bit_cast(u32, __builtin_convertvector(p, f16x2)); }
DI float rbf(float a) { return bflo(pkbf(a, a)); }
DI u32 umax3(u32 a, u32 b, u32 c) { u32 m = a > b ? a : b; return m > c ? m : c; }
DI u32 sum4(u32 m, u32x4 v) { return (m + v.x + v.y) + v.z + v.w; }
DI u32 row_sum(u32 v) {
  v += __builtin_amdgcn_update_dpp(0u, v, 0x111, 0xf, 0xf, true); v += __builtin_amdgcn_update_dpp(0u, v, 0x112, 0xf, 0xf, true);
  v += __builtin_amdgcn_update_dpp(0u, v, 0x114, 0xf, 0xf, true); v += __builtin_amdgcn_update_dpp(0u, v, 0x118, 0xf, 0xf, true);
  return v;
}
DI u32 wave_sum(u32 v) {
  v = row_sum(v);
  return (__builtin_amdgcn_readlane(v, 15) + __builtin_amdgcn_readlane(v, 31)) + (__builtin_amdgcn_readlane(v, 47) + __builtin_amdgcn_readlane(v, 63));
}
DI u32 load_tag(const char* ws) { u32 t = *(const __attribute__((address_space(4))) u32*)ws * 1664525u + 1013904223u; asm volatile("" : "+s"(t)); return t; }
DI u64 flag_of(u32 tag) { return ((u64)(tag * 0x9E3779B1u + 1u) << 32) | tag; }
DI void put_flag(char* ws, u32 tag) { if (threadIdx.x == 0) __hip_atomic_store((GAS u64*)(ws + WS_FLAG) + blockIdx.x * 16, flag_of(tag), RLX_AGENT); }
#define SBAR() __builtin_amdgcn_sched_barrier(0)
#define PIN(x) asm volatile("" : "+v"(x))
DI f32x16 mfma32(u32x4 a, u32x4 b, f32x16 c) { return __builtin_amdgcn_mfma_f32_32x32x16_bf16(__builtin_bit_cast(bf16x8, a), __builtin_bit_cast(bf16x8, b), c, 0, 0, 0); }
DI f32x16 mfma32h(u32x4 a, u32x4 b, f32x16 c) { return __builtin_amdgcn_mfma_f32_32x32x16_f16(__builtin_bit_cast(f16x8, a), __builtin_bit_cast(f16x8, b), c, 0, 0, 0); }
DI f32x4 mfma16(u32x4 a, u32x4 b, f32x4 c) { return __builtin_amdgcn_mfma_f32_16x16x32_bf16(__builtin_bit_cast(bf16x8, a), __builtin_bit_cast(bf16x8, b), c, 0, 0, 0); }
DI u32x2 tr_read(LAS char* p) { return __builtin_bit_cast(u32x2, __builtin_amdgcn_ds_read_tr16_b64_v4i16((LAS s16x4*)p)); }
DI void dma16(const void* src, LAS char* dst) { __builtin_amdgcn_global_load_lds((const GAS void*)src, (LAS void*)dst, 16, 0, 0); }
#define VMCNT(n) asm volatile("s_waitcnt vmcnt(" #n ")" ::: "memory")
#define LGKM0() asm volatile("s_waitcnt lgkmcnt(0)" ::: "memory")
#define BAR() do { asm volatile("s_waitcnt lgkmcnt(0)" ::: "memory"); __builtin_amdgcn_s_barrier(); asm volatile("" ::: "memory"); } while (0)
#define ST_SC1(v, rs, off) __builtin_amdgcn_raw_buffer_store_b128((v), (rs), (off), 0, 16)
#define LD_SC1(rs, off) __builtin_amdgcn_raw_buffer_load_b128((rs), (off), 0, 16)
#define ST1_SC1(v, rs, off) __builtin_amdgcn_raw_buffer_store_b32((v), (rs), (off), 0, 16)
#define LD1_SC1(rs, off) __builtin_amdgcn_raw_buffer_load_b32((rs), (off), 0, 16)

DI u32 phi2(float a, float b) {
  const u32 t = pkbf(a, b);
  const f32x2 x = {bflo(t), bfhi(t)};
  const f32x2 y = x * 1.4426950408889634f;
  const float e0 = __builtin_amdgcn_fmed3f(__builtin_amdgcn_exp2f(y.x), 0.f, 1.f);
  const float e1 = __builtin_amdgcn_fmed3f(__builtin_amdgcn_exp2f(y.y), 0.f, 1.f);
  const f32x2 x1 = x + 1.0f;
  const u32 r = pkbf(__builtin_fmaxf(x1.x, e0), __builtin_fmaxf(x1.y, e1));
  const f32x2 z = (f32x2){bflo(r), bfhi(r)} + 1e-6f;
  return pkbf(z.x, z.y);
}

DI void wait_flags(const char* ws, int idx, u64 tag) {
  const GAS u64* f = (const GAS u64*)(ws + WS_FLAG) + idx * 16;
#pragma nounroll
  for (int it = 0; it < SPIN_CAP; ++it) {
    const u64 v = __hip_atomic_load(f, RLX_AGENT);
    if (__all(v == tag)) break;
    __builtin_amdgcn_s_sleep(16);
  }
}

DI void role_b(LAS char* lds, const char* __restrict__ x, const char* __restrict__ wqkv, const char* __restrict__ wo,
               char* __restrict__ out, char* ws) {
  const int b = blockIdx.x - 256, tid = threadIdx.x, lane = tid & 63;
  const int w = __builtin_amdgcn_readfirstlane(tid >> 6);
  STAMP_DECL;
  STAMP(0);
  const int h = 2 * (b & 7) + ((b >> 3) & 1), rb = b >> 4;
  const int start = (rb + 3 * (h & 1)) % 7;
  const __amdgpu_buffer_rsrc_t rs = __builtin_amdgcn_make_buffer_rsrc(ws, 0, WS_END, 0x00020000);
  const u32 kvp_off = WS_KVP + (h * 16 + rb) * 4096 + tid * 16;
  const u32 ob_off = WS_OB + ((rb * 4 + w) * 16 + h) * 2048 + lane * 16;

  constexpr int WR = 49152, KVI = 45056, WOE = 16384;
#define XS(i) ((i) == 0 ? 16384 : (i) == 1 ? 0 : 32768)
#define QS(cc) ((cc) < 2 ? WR + 24576 + (cc) * 4096 : (cc) < 6 ? XS(1) + ((cc) - 2) * 1024 : WR)
#define QD(cc) ((cc) >= 2 && (cc) < 6 ? 4096 : 1024)
  const int wsb = (56 * h) & ~15;
  const int coff = (h & 1) * 4;
  const int wone = (h & 1) ? 0 : 28;
  const char* xs0 = x + (u32)((128 * rb + 32 * w + (lane >> 3)) * 896 + (((lane & 7) ^ (lane >> 4)) << 4));
  const char* xs1 = x + (u32)((128 * rb + 32 * w + 8 + (lane >> 3)) * 896 + (((lane & 7) ^ (4 + (lane >> 4))) << 4));
  const char* wsrc = wqkv + (u32)((16 * w + (lane >> 2)) * 2688 + wsb + (lane & 3) * 16);
  auto chunk = [&](int cc) { int c = cc + start; return c >= 7 ? c - 7 : c; };
  auto issue_x = [&](int cc, int i) { dma16(((i & 1) ? xs1 : xs0) + chunk(cc) * 128 + (i >> 1) * (16 * 896), lds + XS(cc % 3) + w * 4096 + i * 1024); };
  auto issue_w = [&](int cc, int i) { dma16(wsrc + chunk(cc) * (64 * 2688) + 896 * (i + 1), lds + WR + (cc & 3) * 8192 + i * 4096 + w * 1024); };
  auto issue_q = [&](int cc) { dma16(wsrc + chunk(cc) * (64 * 2688), lds + QS(cc) + w * QD(cc)); };
#pragma unroll
  for (int cc = 0; cc < 4; ++cc) {
    issue_w(cc, 0); issue_w(cc, 1);
    if (cc < 3) { issue_x(cc, 0); issue_x(cc, 1); issue_x(cc, 2); issue_x(cc, 3); }
  }
  __builtin_amdgcn_s_setprio(BPRIO);
  const u32 tag = load_tag(ws);

  const int r32 = lane & 31, half = lane >> 5;
  const int xrd = w * 4096 + r32 * 128 + ((half ^ ((r32 >> 1) & 7)) << 4);
  const int wrd = (8 * half + ((lane >> 2) & 3)) * 64 + ((lane >> 4) & 1) * 32 + (lane & 3) * 8;
  LAS char* wlo = lds + wrd; LAS char* whi = lds + WR + wrd;
  PIN(whi);
  u32x4 xf[28], wf[2][8];
  f32x16 ak = {}, av = {};
#pragma unroll
  for (int cc = 0; cc < 8; ++cc) {
    if (cc < 7) {
      if (cc == 0) VMCNT(14); else if (cc < 6) VMCNT(8); else VMCNT(7);
      BAR();
      if (cc == 0) { STAMP(1); }
#pragma unroll
      for (int s = 0; s < 4; ++s) {
        xf[cc * 4 + s] = *(LAS u32x4*)(lds + XS(cc % 3) + (xrd ^ (s << 5)));
        LAS char* p = whi + (cc & 3) * 8192 + s * 1024;
        const u32x2 k0 = tr_read(p), k1 = tr_read(p + 256), v0 = tr_read(p + 4096), v1 = tr_read(p + 4096 + 256);
        wf[cc & 1][2 * s] = (u32x4){k0.x, k0.y, k1.x, k1.y}; wf[cc & 1][2 * s + 1] = (u32x4){v0.x, v0.y, v1.x, v1.y};
      }
      SBAR();
    }
    if (cc > 0) {
#pragma unroll
      for (int i = 0; i < 8; ++i) {
        if (i & 1) av = mfma32(xf[(cc - 1) * 4 + (i >> 1)], wf[(cc - 1) & 1][i], av);
        else ak = mfma32(xf[(cc - 1) * 4 + (i >> 1)], wf[(cc - 1) & 1][i], ak);
        if (cc <= 4 && i < 4) issue_x(cc + 2, i);
        else if (cc <= 3 && i < 6) issue_w(cc + 3, i - 4);
        else if (cc == 4 && i < 6) issue_q(i - 4);
        else if (cc == 5 && i < 5) issue_q(i + 2);
        SBAR();
      }
    }
    if (cc < 7) {
#pragma unroll
      for (int s = 0; s < 4; ++s) { PIN(xf[cc * 4 + s]); PIN(wf[cc & 1][2 * s]); PIN(wf[cc & 1][2 * s + 1]); }
    }
  }
  STAMP(2);
  {
    u32x4 pk[2], pv[2];
#pragma unroll
    for (int s = 0; s < 2; ++s)
#pragma unroll
      for (int j = 0; j < 4; ++j) {
        pk[s][j] = phi2(ak[8 * s + 2 * j], ak[8 * s + 2 * j + 1]);
        const u32 v = pkbf(av[8 * s + 2 * j], av[8 * s + 2 * j + 1]);
        pv[s][j] = r32 == wone ? 0x3F803F80u : v;
      }
    f32x16 kv = {};
    kv = mfma32(pk[0], pv[0], kv); kv = mfma32(pk[1], pv[1], kv);
#pragma unroll
    for (int g = 0; g < 4; ++g) *(LAS f32x4*)(lds + XS(2) + w * 4096 + g * 1024 + lane * 16) = (f32x4){kv[4 * g], kv[4 * g + 1], kv[4 * g + 2], kv[4 * g + 3]};
    BAR();
    f32x4 s4 = *(LAS f32x4*)(lds + XS(2) + tid * 16);
#pragma unroll
    for (int ww = 1; ww < 4; ++ww) s4 += *(LAS f32x4*)(lds + XS(2) + ww * 4096 + tid * 16);
    u32x4 v = __builtin_bit_cast(u32x4, s4);
    const bool ck = lane == wone + 1;
    if (ck) v = (u32x4){0u, tag, 0u, 0u};
    const u32 c = tag - wave_sum(sum4(0u, v));
    if (ck) v.x = c;
    ST_SC1(v, rs, kvp_off);
    put_flag(ws, tag);
  }
  STAMP(3);
  const int rt = h & 3, ct = h >> 2;
  const int sg = (lane & 15) ^ (4 * (lane >> 4));
  const bool live = ct < 3 || sg < 8;
  const char* wosrc = wo + (u32)((28 * w + (lane >> 4)) * 896 + ct * 256 + sg * 16);
  auto issue_wo1 = [&](int pc, int ii) {
    if (live) dma16(wosrc + (112 * pc + 4 * ii) * 896, lds + ((pc & 1) ? WR : WOE) + (w * 7 + ii) * 1024);
  };
  auto issue_wo = [&](int pc) {
    if (live) {
#pragma unroll
      for (int ii = 0; ii < 7; ++ii) dma16(wosrc + (112 * pc + 4 * ii) * 896, lds + ((pc & 1) ? WR : WOE) + (w * 7 + ii) * 1024);
    }
  };
  const int woq = (lane >> 2) & 3, gam = 4 * ((lane >> 4) & 1) + (lane & 3), ng = 4 * (gam & 1) + (gam >> 1);
  LAS char* wob = lds + WOE + (14 * half + woq) * 256 + (w ^ ((2 * half + woq) & 3)) * 64 + 8 * ng;
  PIN(wob);
  u32x4 wof[32];
  auto read_wo = [&](int pc) {
#pragma unroll
    for (int hl = 0; hl < 4; ++hl) {
      LAS char* p = wob + ((pc & 1) ? WR - WOE : 0) + 28 * hl * 256;
      const u32x2 r0 = tr_read(p), r1 = tr_read(p + 1024), r2 = tr_read(p + 2048), r3 = tr_read(p + 3072);
      wof[pc * 8 + 2 * hl] = (u32x4){r0.x, r0.y, r1.x, r1.y};
      wof[pc * 8 + 2 * hl + 1] = (u32x4){r2.x, r2.y, r3.x, 0u};
    }
    SBAR();
  };
  auto pin_wo = [&](int pc) {
#pragma unroll
    for (int i = 0; i < 8; ++i) PIN(wof[pc * 8 + i]);
  };
  if (w == 0) VMCNT(2); else VMCNT(1);
  BAR();
  u32x4 pq[2];
  {
    f32x16 aq = {};
    u32x4 qf[2][4];
    auto read_q = [&](int cc) {
#pragma unroll
      for (int s = 0; s < 4; ++s) {
        LAS char* p = (QS(cc) >= WR ? whi + (QS(cc) - WR) : wlo + QS(cc)) + s * QD(cc);
        const u32x2 lo = tr_read(p), hi = tr_read(p + 256);
        qf[cc & 1][s] = (u32x4){lo.x, lo.y, hi.x, hi.y};
      }
      SBAR();
    };
    read_q(0);
#pragma unroll
    for (int cc = 0; cc < 7; ++cc) {
#pragma unroll
      for (int s = 0; s < 4; ++s) PIN(qf[cc & 1][s]);
      if (cc < 6) read_q(cc + 1);
#pragma unroll
      for (int s = 0; s < 4; ++s) {
        aq = mfma32(qf[cc & 1][s], xf[cc * 4 + s], aq);
        if (s == 0) issue_wo1(0, cc);
        SBAR();
      }
    }
#pragma unroll
    for (int s = 0; s < 2; ++s) {
#pragma unroll
      for (int j = 0; j < 4; ++j) pq[s][j] = phi2(aq[8 * s + 2 * j], aq[8 * s + 2 * j + 1]);
      PIN(pq[s]);
    }
  }
  STAMP(4);
  VMCNT(0);
  BAR();
  read_wo(0);
  issue_wo(1);
  pin_wo(0);
  STAMP(5);
  {
    u32x4 d[16];
#pragma nounroll
    for (int it = 0; it < SPIN_CAP; ++it) {
      asm volatile("" ::: "memory");
#pragma unroll
      for (int p = 0; p < 16; ++p) d[p] = LD_SC1(rs, WS_KVP + h * 65536 + p * 4096 + tid * 16);
      u32 m = 0;
#pragma unroll
      for (int p = 0; p < 16; ++p) m = sum4(m, d[p]);
      if (wave_sum(m) == 16u * tag) break;
    }
    f32x4 s4 = __builtin_bit_cast(f32x4, d[0]);
#pragma unroll
    for (int p = 1; p < 16; ++p) s4 += __builtin_bit_cast(f32x4, d[p]);
    const int dw = 8 * w + 4 * half - coff;
    u32x2 o2 = {pkbf(s4.x, s4.y), pkbf(s4.z, s4.w)};
    if (dw < 0 || dw >= 28) o2 = (u32x2){0u, 0u};
    *(LAS u32x2*)(lds + KVI + lane * 32 + w * 8) = o2;
  }
  STAMP(6);
  VMCNT(0);
  BAR();
  {
    const int i = (r32 & 3) + 4 * (r32 >> 3), hh = (r32 >> 2) & 1;
    const int ew = i < 14 ? 14 * hh + i + coff : wone;
    LAS char* p = lds + KVI + (ew + 32 * half) * 32;
    const u32x4 a0 = *(LAS u32x4*)p, a1 = *(LAS u32x4*)(p + 16);
    f32x16 nm = {};
    nm = mfma32(a0, pq[0], nm); nm = mfma32(a1, pq[1], nm);
    const float den = rbf(rbf(nm[14]) + 1e-6f);
    const float rc = __builtin_amdgcn_rcpf(den);
    u32 o[8];
#pragma unroll
    for (int j = 0; j < 7; ++j) { const u32 t = pkbf(nm[2 * j], nm[2 * j + 1]); o[j] = pkbf(bflo(t) * rc, bfhi(t) * rc); }
    o[7] = tag - ((o[0] + o[1] + o[2]) + (o[3] + o[4] + o[5]) + o[6]);
    ST_SC1(((u32x4){o[0], o[1], o[2], o[3]}), rs, ob_off);
    ST_SC1(((u32x4){o[4], o[5], o[6], o[7]}), rs, ob_off + 1024);
  }
  STAMP(7);
  read_wo(1);
  issue_wo(2);
  pin_wo(1);
  BAR();
  issue_wo(3);
  VMCNT(7); BAR();
  read_wo(2);
  STAMP(8);
  {
    u32x4 d[8];
#pragma nounroll
    for (int it = 0; it < SPIN_CAP; ++it) {
      asm volatile("" ::: "memory");
#pragma unroll
      for (int p = 0; p < 8; ++p) d[p] = LD_SC1(rs, WS_OB + ((rb * 4 + rt) * 16 + 4 * w) * 2048 + p * 1024 + lane * 16);
      u32 m = 0;
#pragma unroll
      for (int p = 0; p < 8; ++p) m = sum4(m, d[p]);
      if (__all(m == 4u * tag)) break;
    }
#pragma unroll
    for (int p = 1; p < 8; p += 2) d[p].w = 0u;
    STAMP(9);
    pin_wo(2);
    VMCNT(0);
    BAR();
#pragma unroll
    for (int p = 0; p < 8; ++p) *(LAS u32x4*)(lds + WOE + (8 * w + p) * 1024 + lane * 16) = d[p];
    read_wo(3);
    pin_wo(3);
    BAR();
  }
  if (ct < 3 || w < 2) {
    f32x16 acc = {};
#pragma unroll
    for (int k = 0; k < 32; ++k) acc = mfma32(wof[k], *(LAS u32x4*)(lds + WOE + k * 1024 + lane * 16), acc);
    char* dst = out + (u32)((128 * rb + 32 * rt + r32) * 1792 + 896 + ct * 256 + w * 64 + half * 32);
    *(u32x4*)dst = (u32x4){pkbf(acc[0], acc[1]), pkbf(acc[2], acc[3]), pkbf(acc[4], acc[5]), pkbf(acc[6], acc[7])};
    *(u32x4*)(dst + 16) = (u32x4){pkbf(acc[8], acc[9]), pkbf(acc[10], acc[11]), pkbf(acc[12], acc[13]), pkbf(acc[14], acc[15])};
  }
  STAMP(10);
  STAMP_FLUSH();
}

DI f32x2 rot(f32x2 b) { return (f32x2){-b.y, b.x}; }
DI f32x2 cm1(f32x2 a, f32x2 b) { return (f32x2){a.x, a.x} * b; }
DI f32x2 cm2(f32x2 a, f32x2 br, f32x2 t) { return __builtin_elementwise_fma((f32x2){a.y, a.y}, br, t); }
DI f32x2 cexp_p(float a2, float f, float p) {
  const float m = __builtin_amdgcn_exp2f(a2 * p), ph = __builtin_amdgcn_fractf(f * p);
  return (f32x2){m, m} * (f32x2){__builtin_amdgcn_cosf(ph), __builtin_amdgcn_sinf(ph)};
}
DI u32 wave_umax(u32 v) {
  u32 t;
  t = __builtin_amdgcn_update_dpp(0u, v, 0x111, 0xf, 0xf, false); v = v > t ? v : t;
  t = __builtin_amdgcn_update_dpp(0u, v, 0x112, 0xf, 0xf, false); v = v > t ? v : t;
  t = __builtin_amdgcn_update_dpp(0u, v, 0x114, 0xf, 0xf, false); v = v > t ? v : t;
  t = __builtin_amdgcn_update_dpp(0u, v, 0x118, 0xf, 0xf, false); v = v > t ? v : t;
  const u32 a = __builtin_amdgcn_readlane(v, 15), b = __builtin_amdgcn_readlane(v, 31), c = __builtin_amdgcn_readlane(v, 47), d = __builtin_amdgcn_readlane(v, 63);
  const u32 ab = a > b ? a : b, cd = c > d ? c : d;
  return ab > cd ? ab : cd;
}

DI void role_a(LAS char* lds, const char* __restrict__ u, const u32* __restrict__ logA, const u32* __restrict__ Aim,
               const u32* __restrict__ Cre, const u32* __restrict__ Cim, const u16* __restrict__ logdt,
               const char* __restrict__ Dsk, const char* __restrict__ wo, char* __restrict__ out, char* ws) {
  const int a = blockIdx.x, cb = a >> 5, jp = a & 31, tid = threadIdx.x, lane = tid & 63;
  const int w = __builtin_amdgcn_readfirstlane(tid >> 6);
  STAMP_DECL;
  STAMP(0);
  const __amdgpu_buffer_rsrc_t rs = __builtin_amdgcn_make_buffer_rsrc(ws, 0, WS_END, 0x00020000);
  const int ch = w >> 1, hf = w & 1, d = 2 * a + ch;
  const int pi = d * 128 + hf * 64 + lane;
  u32x4 ub[4];
  if (w < 2) {
#pragma unroll
    for (int i = 0; i < 4; ++i) ub[i] = *(const u32x4*)(u + (u32)((64 * jp + 4 * (tid & 15) + i) * 896 + cb * 128 + (tid >> 4) * 16));
  }
  const u32 pA = logA[pi], pI = Aim[pi], pR = Cre[pi], pC = Cim[pi];
  const float ldt = bflo(logdt[d]);
  u32x4 us[2];
#pragma unroll
  for (int i = 0; i < 2; ++i) us[i] = *(const u32x4*)(u + (u32)((64 * jp + 32 * i + (tid >> 3)) * 896 + cb * 128 + (tid & 7) * 16));
  const u32x4 dk = *(const u32x4*)(Dsk + cb * 128 + (tid & 7) * 16);
  u32 dummy = 0;
  const u32 tag = load_tag(ws);
  int tix[8];
#pragma unroll
  for (int j = 0; j < 8; ++j) {
    const int p = (int)rbf((float)(2047 - 8 * tid - j));
    const int low = p & 7, blk = p >> 8;
    const int k4 = (blk < 4 ? blk : 4) * 4;
    const int sh = (0x32210 >> k4) & 15;
    const int base = blk + ((0xCBA70 >> k4) & 15);
    tix[j] = ((base + (low >> sh)) * 32 + ((p >> 3) & 31)) * 4;
    PIN(tix[j]);
  }
  if (w < 2) {
    u32x4 ck;
#pragma unroll
    for (int k = 0; k < 4; ++k) {
      const u32x4 v = {ub[0][k], ub[1][k], ub[2][k], ub[3][k]};
      ST_SC1(v, rs, WS_UX + (cb * 32 + 4 * (tid >> 4) + k) * 8192 + jp * 256 + (tid & 15) * 16);
      ck[k] = tag - row_sum(sum4(0u, v));
    }
    if ((tid & 15) == 15) ST_SC1(ck, rs, WS_UCK + a * 128 + (tid >> 4) * 16);
  }
  put_flag(ws, tag);
  STAMP(1);
  constexpr int SROW = 272, STG = 53 * SROW, TBL = 4 * STG, UC = 0, UCS = 4160, RV = 8 * UCS, RVS = 4608, EX = RV + 2 * RVS;
  const float dt = rbf(expf(ldt));
  const u32 mx = wave_umax(umax3(umax3(pR & 0x7fffu, (pR >> 16) & 0x7fffu, pC & 0x7fffu), (pC >> 16) & 0x7fffu, 0u));
  int ex = mx >> 7; ex = ex < 13 ? 13 : ex > 254 ? 254 : ex;
  const float sc = __uint_as_float((u32)(266 - ex) << 23), isc = __uint_as_float((u32)(ex - 12) << 23);
  f32x16 kacc = {};
  LAS char* stg = lds + w * STG;
  const int r32 = lane & 31, half = lane >> 5;
  LAS char* px = stg + r32 * SROW + half * 16;
  LAS char* py = stg + (32 + (r32 < 20 ? r32 : 20)) * SROW + half * 16;
  u32x4 fx[8], fy[8];
  u32x4 ud[3]; u32 uck;
  const __amdgpu_buffer_rsrc_t ru = __builtin_amdgcn_make_buffer_rsrc(ws + WS_UX + a * 8192, 0, 8192, 0x00020000);
  const int npc = w < 3 ? 9 : 8;
  auto load_u = [&]() {
    ud[0] = LD_SC1(ru, w * 2048 + lane * 16); ud[1] = LD_SC1(ru, w * 2048 + 1024 + lane * 16);
    ud[2] = LD_SC1(ru, lane < 16 ? w * 2048 + 2048 + lane * 16 : 8192);
    uck = LD1_SC1(rs, lane < npc ? WS_UCK + (cb * 32 + 8 * w + lane) * 128 + jp * 4 : WS_END);
  };
#pragma unroll
  for (int bt = 0; bt < 2; ++bt) {
    const float la = bt ? bfhi(pA) : bflo(pA), ai = bt ? bfhi(pI) : bflo(pI), cr = bt ? bfhi(pR) : bflo(pR), ci = bt ? bfhi(pC) : bflo(pC);
    const float a2 = -rbf(expf(la)) * dt * 1.4426950408889634f, f = ai * dt * 0.15915494309189535f;
    LAS u32* sw = (LAS u32*)(stg + lane * 4);
#define STEP(p0, b0, b0r, r0, p1, b1, b1r, r1) do { \
      const f32x2 t0_ = cm1(p0, b0), t1_ = cm1(p1, b1); const u32 c0_ = pkh(p0), c1_ = pkh(p1); SBAR(); \
      p0 = cm2(p0, b0r, t0_); p1 = cm2(p1, b1r, t1_); sw[(r0) * (SROW / 4)] = c0_; sw[(r1) * (SROW / 4)] = c1_; SBAR(); } while (0)
    const f32x2 z8 = cexp_p(a2, f, 8.f), y1 = cexp_p(a2, -f, 1.f), yq = cexp_p(a2, -f, 256.f);
    const f32x2 z8r = rot(z8), y1r = rot(y1), yqr = rot(yq);
    f32x2 x0 = (f32x2){cr, ci} * sc;
    f32x2 x1 = cm2(x0, z8r, cm1(x0, z8));
    const f32x2 z16 = cm2(z8, z8r, cm1(z8, z8)), z16r = rot(z16);
    SBAR();
#pragma unroll
    for (int r = 0; r < 30; r += 2) {
      STEP(x0, z16, z16r, r, x1, z16, z16r, r + 1);
      if (bt == 1 && r < 16) { kacc = mfma32h(fy[r >> 1], fx[r >> 1], kacc); SBAR(); }
    }
    sw[30 * (SROW / 4)] = pkh(x0); sw[31 * (SROW / 4)] = pkh(x1);
    if (bt == 1) { STAMP(2); load_u(); SBAR(); }
    {
      f32x2 p = y1, q = yq, y2, y4, y6, v;
      sw[32 * (SROW / 4)] = 0x00003C00u;
      STEP(p, y1, y1r, 33, q, yq, yqr, 40); y2 = p;
      STEP(p, y1, y1r, 34, q, yq, yqr, 44);
      STEP(p, y1, y1r, 35, q, yq, yqr, 46); y4 = p;
      STEP(p, y1, y1r, 36, q, yq, yqr, 48);
      STEP(p, y1, y1r, 37, q, yq, yqr, 49); y6 = p;
      STEP(p, y1, y1r, 38, q, yq, yqr, 50);
      v = cm2(y4, yqr, cm1(y4, yq));
      sw[39 * (SROW / 4)] = pkh(p);
      STEP(v, yq, yqr, 42, q, yq, yqr, 51);
      y2 = cm2(y2, yqr, cm1(y2, yq)); y6 = cm2(y6, yqr, cm1(y6, yq));
      sw[52 * (SROW / 4)] = pkh(q); sw[45 * (SROW / 4)] = pkh(v);
      v = cm2(v, yqr, cm1(v, yq));
      sw[41 * (SROW / 4)] = pkh(y2); sw[43 * (SROW / 4)] = pkh(y6); sw[47 * (SROW / 4)] = pkh(v);
    }
#undef STEP
#pragma unroll
    for (int s = 0; s < 8; ++s) { fy[s] = *(LAS u32x4*)(py + 32 * s); fx[s] = *(LAS u32x4*)(px + 32 * s); }
    SBAR();
  }
  STAMP(3);
#pragma unroll
  for (int s = 0; s < 8; ++s) kacc = mfma32h(fy[s], fx[s], kacc);
  u32 sk[8];
#pragma unroll
  for (int i = 0; i < 2; ++i)
#pragma unroll
    for (int k = 0; k < 4; ++k) { sk[4 * i + k] = pkbf(bflo(us[i][k]) * bflo(dk[k]), bfhi(us[i][k]) * bfhi(dk[k])); PIN(sk[4 * i + k]); }
  {
    const float fac = 2.f * dt * isc;
#pragma unroll
    for (int i = 0; i < 12; ++i) *(LAS float*)(lds + TBL + w * 3072 + (((i & 3) + 8 * (i >> 2) + 4 * half) * 32 + r32) * 4) = kacc[i] * fac;
  }
  STAMP(4);
  BAR();
#pragma unroll
  for (int c = 0; c < 2; ++c) {
    float kk[8];
#pragma unroll
    for (int j = 0; j < 8; ++j) kk[j] = *(LAS float*)(lds + TBL + c * 6144 + tix[j]) + *(LAS float*)(lds + TBL + c * 6144 + 3072 + tix[j]);
    *(LAS u32x4*)(lds + RV + c * RVS + tid * 16) = (u32x4){pkbf(kk[0], kk[1]), pkbf(kk[2], kk[3]), pkbf(kk[4], kk[5]), pkbf(kk[6], kk[7])};
  }
  if (tid < 64) *(LAS u32x4*)(lds + RV + half * RVS + 4096 + r32 * 16) = (u32x4){0u, 0u, 0u, 0u};
  SBAR();
#pragma nounroll
  for (int it = 0; it < SPIN_CAP; ++it) {
    if (wave_sum(sum4(sum4(sum4(uck, ud[0]), ud[1]), ud[2])) == npc * tag) break;
    asm volatile("" ::: "memory");
    load_u();
  }
  asm volatile("" :: "v"(dummy));
#pragma unroll
  for (int i = 0; i < 2; ++i) {
    const u32 nx = __builtin_amdgcn_update_dpp(__builtin_amdgcn_readlane(ud[i + 1].x, 0), ud[i].x, 0x130, 0xf, 0xf, false);
    const u32 dw[5] = {ud[i].x, ud[i].y, ud[i].z, ud[i].w, nx};
    LAS char* p = lds + UC + (128 * w + 64 * i + lane + 4) * 8;
#pragma unroll
    for (int c = 0; c < 2; ++c) {
      u32 pr[4];
#pragma unroll
      for (int k = 0; k < 4; ++k) pr[k] = __builtin_amdgcn_perm(dw[k + 1], dw[k], c ? 0x07060302u : 0x05040100u);
      *(LAS u32x2*)(p + (4 * c) * UCS) = (u32x2){pr[0], pr[2]};
      *(LAS u32x2*)(p + (4 * c + 1) * UCS) = (u32x2){pr[1], pr[3]};
      *(LAS u32*)(p + (4 * c + 2) * UCS - 4) = pr[0]; *(LAS u32*)(p + (4 * c + 2) * UCS) = pr[2];
      *(LAS u32*)(p + (4 * c + 3) * UCS - 4) = pr[1]; *(LAS u32*)(p + (4 * c + 3) * UCS) = pr[3];
    }
  }
  if (w == 0) {
    const u32 u0 = __builtin_amdgcn_readlane(ud[0].x, 0);
    const int cs = lane >> 3, ix = lane & 7, last = (cs & 2) ? 6 : 7;
    const u32 v = ((cs & 1) && ix == last) ? (cs & 4 ? u0 & 0xffff0000u : u0 << 16) : 0u;
    if (ix <= last) *(LAS u32*)(lds + UC + cs * UCS + ix * 4) = v;
  }
  BAR();
  STAMP(5);
  {
    const int c16 = lane & 15, hq = lane >> 4;
    LAS char* pa = lds + RV + ch * RVS + 2 * (2032 - 256 * 7 + 128 * hf - 16 * c16 + 8 * hq);
    LAS char* pb = lds + UC + (4 * ch + ((c16 + 1) & 3)) * UCS + 2 * (128 * hf + 8 * hq + 4 * ((c16 + 1) >> 2));
    f32x4 acc[8] = {};
    u32x4 fa[2][8], fb[2][8];
    auto ldf = [&](int rho, int m) {
      fa[rho & 1][m] = *(LAS u32x4*)(pa + 64 * rho + 512 * (7 - m));
      const u32x2 lo = *(volatile LAS u32x2*)(pb + 64 * rho + 512 * m), hi = *(volatile LAS u32x2*)(pb + 64 * rho + 512 * m + 8);
      fb[rho & 1][m] = (u32x4){lo.x, lo.y, hi.x, hi.y};
    };
#pragma unroll
    for (int m = 0; m < 8; ++m) ldf(0, m);
#pragma unroll
    for (int rho = 0; rho < 4; ++rho) {
      SBAR();
#pragma unroll
      for (int g = 0; g < 8; ++g) {
#pragma unroll
        for (int aa = 0; aa <= g; ++aa) {
          if (aa + g < 8) acc[aa + g] = mfma16(fa[rho & 1][g], fb[rho & 1][aa], acc[aa + g]);
          if (aa < g && aa + g < 8) acc[aa + g] = mfma16(fa[rho & 1][aa], fb[rho & 1][g], acc[aa + g]);
        }
        if (rho < 3) { ldf(rho + 1, g); SBAR(); }
      }
    }
    STAMP(6);
#pragma unroll
    for (int t = 0; t < 8; ++t) *(LAS f32x4*)(lds + EX + w * 8192 + t * 1024 + lane * 16) = acc[t];
    BAR();
#pragma unroll
    for (int i = 0; i < 2; ++i) {
      LAS char* p = lds + EX + (2 * w + i) * 1024 + lane * 16;
      const f32x4 y0 = *(LAS f32x4*)p + *(LAS f32x4*)(p + 8192), y1 = *(LAS f32x4*)(p + 16384) + *(LAS f32x4*)(p + 24576);
      const u32x4 v = {pkbf(y0.x, y1.x), pkbf(y0.y, y1.y), pkbf(y0.z, y1.z), pkbf(y0.w, y1.w)};
      ST_SC1(v, rs, WS_YX + (cb * 32 + 4 * (2 * w + i) + hq) * 8192 + jp * 256 + c16 * 16);
      const u32 ck = tag - row_sum(sum4(0u, v));
      if (c16 == 15) ST1_SC1(ck, rs, WS_YCK + a * 128 + (4 * (2 * w + i) + hq) * 4);
    }
  }
  STAMP(7);
  {
    u32x4 y[2];
    __builtin_amdgcn_s_sleep(8);
#pragma nounroll
    for (int it = 0; it < SPIN_CAP; ++it) {
      asm volatile("" ::: "memory");
      y[0] = LD_SC1(rs, WS_YX + a * 8192 + tid * 16); y[1] = LD_SC1(rs, WS_YX + a * 8192 + 4096 + tid * 16);
      const u32 ck = LD1_SC1(rs, lane < 8 ? WS_YCK + (cb * 32 + 16 * (lane >> 2) + 4 * w + (lane & 3)) * 128 + jp * 4 : WS_END);
      if (wave_sum(sum4(sum4(ck, y[0]), y[1])) == 8u * tag) break;
      __builtin_amdgcn_s_sleep(2);
    }
    STAMP(8);
#pragma unroll
    for (int i = 0; i < 2; ++i)
#pragma unroll
      for (int g = 0; g < 4; ++g) *(LAS u32*)(lds + (16 * g + (tid & 15)) * 144 + (16 * i + (tid >> 4)) * 4) = y[i][g];
    BAR();
#pragma unroll
    for (int i = 0; i < 2; ++i) {
      const u32x4 v = *(LAS u32x4*)(lds + (32 * i + (tid >> 3)) * 144 + (tid & 7) * 16);
      u32x4 o;
#pragma unroll
      for (int k = 0; k < 4; ++k) o[k] = pkbf(bflo(v[k]) + bflo(sk[4 * i + k]), bfhi(v[k]) + bfhi(sk[4 * i + k]));
      *(u32x4*)(out + (u32)((64 * jp + 32 * i + (tid >> 3)) * 1792 + cb * 128 + (tid & 7) * 16)) = o;
    }
  }
  STAMP(9);
  STAMP_FLUSH();
}

__global__ void __launch_bounds__(256, 2)
fused_kernel(const char* __restrict__ u, const u32* __restrict__ logA, const u32* __restrict__ Aim, const u32* __restrict__ Cre,
             const u32* __restrict__ Cim, const u16* __restrict__ logdt, const char* __restrict__ Dsk, const char* __restrict__ x,
             const char* __restrict__ wqkv, const char* __restrict__ wo, char* __restrict__ out, char* ws) {
  __shared__ __attribute__((aligned(16))) char smem[81920];
  LAS char* lds = (LAS char*)smem;
  asm volatile("" :: "s"(u), "s"(logA), "s"(Aim), "s"(Cre), "s"(Cim), "s"(logdt), "s"(Dsk), "s"(x), "s"(wqkv), "s"(wo), "s"(out), "s"(ws));
  if (blockIdx.x >= 256) role_b(lds, x, wqkv, wo, out, ws);
  else if (blockIdx.x < 224) role_a(lds, u, logA, Aim, Cre, Cim, logdt, Dsk, wo, out, ws);
  else {
    const int t = threadIdx.x;
    if (blockIdx.x == 224) {
      const u32 tag = load_tag(ws);
      __builtin_amdgcn_s_sleep(127);
      wait_flags(ws, 256 + t, flag_of(tag));
      wait_flags(ws, t < 224 ? t : 0, flag_of(tag));
      __syncthreads();
      if (t == 0) __hip_atomic_store((GAS u32*)ws, tag, RLX_AGENT);
      wait_flags(ws + 64, 256 + t, flag_of(tag));
      wait_flags(ws + 64, t < 224 ? t : 0, flag_of(tag));
      __syncthreads();
      u64 mn = ~0ull, mxv = 0;
      for (int i = 0; i < 8; ++i) {
        const int wv = t + 256 * i, blk = wv >> 2;
        if (blk >= 224 && blk < 256) continue;
        const GAS u64* p = (const GAS u64*)(ws + WS_DBG) + wv * 16;
        const u64 s0 = __hip_atomic_load(p, RLX_AGENT), sk = __hip_atomic_load(p + (READOUT & 15), RLX_AGENT);
        mn = s0 < mn ? s0 : mn;
        if ((READOUT >> 4) == 2 || ((READOUT >> 4) == 1) == (blk >= 256)) mxv = sk > mxv ? sk : mxv;
      }
      LAS u64* l = (LAS u64*)lds;
      l[t] = mn; l[256 + t] = mxv;
      __syncthreads();
      for (int i = 0; i < 256; ++i) { mn = l[i] < mn ? l[i] : mn; mxv = l[256 + i] > mxv ? l[256 + i] : mxv; }
      const u64 until = mn + 6000 + ((READOUT >> 4) == 3 ? 0 : mxv - mn);
      for (int it = 0; it < 1000000 && __builtin_amdgcn_s_memrealtime() < until; ++it) __builtin_amdgcn_s_sleep(1);
    }
  }
}

extern "C" void kernel_launch(void* const* d_in, const int* in_sizes, int n_in, void* d_out, int out_size, void* d_ws,
                              size_t ws_size, hipStream_t stream) {
  fused_kernel<<<512, 256, 0, stream>>>((const char*)d_in[0], (const u32*)d_in[1], (const u32*)d_in[2], (const u32*)d_in[3],
                                        (const u32*)d_in[4], (const u16*)d_in[5], (const char*)d_in[6], (const char*)d_in[7],
                                        (const char*)d_in[8], (const char*)d_in[9], (char*)d_out, (char*)d_ws);
}
```

```cpp
#define OUTAUX 16
#include <hip/hip_runtime.h>
#include <stdint.h>

typedef unsigned short u16;
typedef unsigned u32;
typedef unsigned long long u64;
typedef short s16x4 __attribute__((ext_vector_type(4)));
typedef __bf16 bf16x8 __attribute__((ext_vector_type(8)));
typedef __bf16 bf16x2 __attribute__((ext_vector_type(2)));
typedef _Float16 f16x8 __attribute__((ext_vector_type(8)));
typedef _Float16 f16x2 __attribute__((ext_vector_type(2)));
typedef float f32x16 __attribute__((ext_vector_type(16)));
typedef float f32x4 __attribute__((ext_vector_type(4)));
typedef float f32x2 __attribute__((ext_vector_type(2)));
typedef u32 u32x4 __attribute__((ext_vector_type(4)));
typedef u32 u32x2 __attribute__((ext_vector_type(2)));
#define LAS __attribute__((address_space(3)))
#define GAS __attribute__((address_space(1)))
#define DI __device__ __forceinline__
#define RLX_AGENT __ATOMIC_RELAXED, __HIP_MEMORY_SCOPE_AGENT

#define BMAP 0
#define PREFETCH 0
#define CODE_BEFORE 0
#define CODE_LINES 180
#define BPRIO 2

constexpr u32 WS_FLAG = 4096;
constexpr u32 WS_UCK = 73728;
constexpr u32 WS_YCK = WS_UCK + 28672;
constexpr u32 WS_KVP = 1u << 17;
constexpr u32 WS_OB = WS_KVP + (1u << 20);
constexpr u32 WS_UX = WS_OB + (2u << 20);
constexpr u32 WS_YX = WS_UX + (2u << 20);
constexpr u32 WS_END = WS_YX + (2u << 20);
constexpr u32 WS_DBG = WS_END;
constexpr int SPIN_CAP = 1 << 15;

#define STAMP(i) do {} while (0)
#define STAMP_DECL
#define STAMP_FLUSH() do {} while (0)

DI float bflo(u32 v) { return __uint_as_float(v << 16); }
DI float bfhi(u32 v) { return __uint_as_float(v & 0xffff0000u); }
DI u32 pkbf(float a, float b) { f32x2 p = {a, b}; return __builtin_bit_cast(u32, __builtin_convertvector(p, bf16x2)); }
DI u32 pkh(f32x2 p) { return __builtin_bit_cast(u32, __builtin_convertvector(p, f16x2)); }
DI float rbf(float a) { return bflo(pkbf(a, a)); }
DI u32 umax3(u32 a, u32 b, u32 c) { u32 m = a > b ? a : b; return m > c ? m : c; }
DI u32 sum4(u32 m, u32x4 v) { return (m + v.x + v.y) + v.z + v.w; }
DI u32 row_sum(u32 v) {
  v += __builtin_amdgcn_update_dpp(0u, v, 0x111, 0xf, 0xf, true); v += __builtin_amdgcn_update_dpp(0u, v, 0x112, 0xf, 0xf, true);
  v += __builtin_amdgcn_update_dpp(0u, v, 0x114, 0xf, 0xf, true); v += __builtin_amdgcn_update_dpp(0u, v, 0x118, 0xf, 0xf, true);
  return v;
}
DI u32 wave_sum(u32 v) {
  v = row_sum(v);
  return (__builtin_amdgcn_readlane(v, 15) + __builtin_amdgcn_readlane(v, 31)) + (__builtin_amdgcn_readlane(v, 47) + __builtin_amdgcn_readlane(v, 63));
}
DI u32 load_tag(const char* ws) { u32 t = *(const __attribute__((address_space(4))) u32*)ws * 1664525u + 1013904223u; asm volatile("" : "+s"(t)); return t; }
DI u64 flag_of(u32 tag) { return ((u64)(tag * 0x9E3779B1u + 1u) << 32) | tag; }
DI void put_flag(char* ws, u32 tag) { if (threadIdx.x == 0) __hip_atomic_store((GAS u64*)(ws + WS_FLAG) + blockIdx.x * 16, flag_of(tag), RLX_AGENT); }
#define SBAR() __builtin_amdgcn_sched_barrier(0)
#define PIN(x) asm volatile("" : "+v"(x))
DI f32x16 mfma32(u32x4 a, u32x4 b, f32x16 c) { return __builtin_amdgcn_mfma_f32_32x32x16_bf16(__builtin_bit_cast(bf16x8, a), __builtin_bit_cast(bf16x8, b), c, 0, 0, 0); }
DI f32x16 mfma32h(u32x4 a, u32x4 b, f32x16 c) { return __builtin_amdgcn_mfma_f32_32x32x16_f16(__builtin_bit_cast(f16x8, a), __builtin_bit_cast(f16x8, b), c, 0, 0, 0); }
DI f32x4 mfma16(u32x4 a, u32x4 b, f32x4 c) { return __builtin_amdgcn_mfma_f32_16x16x32_bf16(__builtin_bit_cast(bf16x8, a), __builtin_bit_cast(bf16x8, b), c, 0, 0, 0); }
DI u32x2 tr_read(LAS char* p) { return __builtin_bit_cast(u32x2, __builtin_amdgcn_ds_read_tr16_b64_v4i16((LAS s16x4*)p)); }
DI void dma16(const void* src, LAS char* dst) { __builtin_amdgcn_global_load_lds((const GAS void*)src, (LAS void*)dst, 16, 0, 0); }
#define VMCNT(n) asm volatile("s_waitcnt vmcnt(" #n ")" ::: "memory")
#define LGKM0() asm volatile("s_waitcnt lgkmcnt(0)" ::: "memory")
#define BAR() do { asm volatile("s_waitcnt lgkmcnt(0)" ::: "memory"); __builtin_amdgcn_s_barrier(); asm volatile("" ::: "memory"); } while (0)
#define ST_OUT(v, off) __builtin_amdgcn_raw_buffer_store_b128((v), __builtin_amdgcn_make_buffer_rsrc(out, 0, 2048 * 1792, 0x00020000), (off), 0, OUTAUX)
#define ST_SC1(v, rs, off) __builtin_amdgcn_raw_buffer_store_b128((v), (rs), (off), 0, 16)
#define LD_SC1(rs, off) __builtin_amdgcn_raw_buffer_load_b128((rs), (off), 0, 16)
#define ST1_SC1(v, rs, off) __builtin_amdgcn_raw_buffer_store_b32((v), (rs), (off), 0, 16)
#define LD1_SC1(rs, off) __builtin_amdgcn_raw_buffer_load_b32((rs), (off), 0, 16)

DI u32 phi2(float a, float b) {
  const u32 t = pkbf(a, b);
  const f32x2 x = {bflo(t), bfhi(t)};
  const f32x2 y = x * 1.4426950408889634f;
  const float e0 = __builtin_amdgcn_fmed3f(__builtin_amdgcn_exp2f(y.x), 0.f, 1.f);
  const float e1 = __builtin_amdgcn_fmed3f(__builtin_amdgcn_exp2f(y.y), 0.f, 1.f);
  const f32x2 x1 = x + 1.0f;
  const u32 r = pkbf(__builtin_fmaxf(x1.x, e0), __builtin_fmaxf(x1.y, e1));
  const f32x2 z = (f32x2){bflo(r), bfhi(r)} + 1e-6f;
  return pkbf(z.x, z.y);
}

DI void wait_flags(const char* ws, int idx, u64 tag) {
  const GAS u64* f = (const GAS u64*)(ws + WS_FLAG) + idx * 16;
#pragma nounroll
  for (int it = 0; it < SPIN_CAP; ++it) {
    const u64 v = __hip_atomic_load(f, RLX_AGENT);
    if (__all(v == tag)) break;
    __builtin_amdgcn_s_sleep(16);
  }
}

DI void role_b(LAS char* lds, const char* __restrict__ x, const char* __restrict__ wqkv, const char* __restrict__ wo,
               char* __restrict__ out, char* ws, u64 t_first) {
  const int b = blockIdx.x - 256, tid = threadIdx.x, lane = tid & 63;
  const int w = __builtin_amdgcn_readfirstlane(tid >> 6);
  STAMP_DECL;
  STAMP(0);
  const int h = 2 * (b & 7) + ((b >> 3) & 1), rb = b >> 4;
  const int start = (rb + 3 * (h & 1)) % 7;
  const __amdgpu_buffer_rsrc_t rs = __builtin_amdgcn_make_buffer_rsrc(ws, 0, WS_END, 0x00020000);
  const u32 kvp_off = WS_KVP + (h * 16 + rb) * 4096 + tid * 16;
  const u32 ob_off = WS_OB + ((rb * 4 + w) * 16 + h) * 2048 + lane * 16;

  constexpr int WR = 49152, KVI = 45056, WOE = 16384;
#define XS(i) ((i) == 0 ? 16384 : (i) == 1 ? 0 : 32768)
#define QS(cc) ((cc) < 2 ? WR + 24576 + (cc) * 4096 : (cc) < 6 ? XS(1) + ((cc) - 2) * 1024 : WR)
#define QD(cc) ((cc) >= 2 && (cc) < 6 ? 4096 : 1024)
  const int wsb = (56 * h) & ~15;
  const int coff = (h & 1) * 4;
  const int wone = (h & 1) ? 0 : 28;
  const char* xs0 = x + (u32)((128 * rb + 32 * w + (lane >> 3)) * 896 + (((lane & 7) ^ (lane >> 4)) << 4));
  const char* xs1 = x + (u32)((128 * rb + 32 * w + 8 + (lane >> 3)) * 896 + (((lane & 7) ^ (4 + (lane >> 4))) << 4));
  const char* wsrc = wqkv + (u32)((16 * w + (lane >> 2)) * 2688 + wsb + (lane & 3) * 16);
  auto chunk = [&](int cc) { int c = cc + start; return c >= 7 ? c - 7 : c; };
  auto issue_x = [&](int cc, int i) { dma16(((i & 1) ? xs1 : xs0) + chunk(cc) * 128 + (i >> 1) * (16 * 896), lds + XS(cc % 3) + w * 4096 + i * 1024); };
  auto issue_w = [&](int cc, int i) { dma16(wsrc + chunk(cc) * (64 * 2688) + 896 * (i + 1), lds + WR + (cc & 3) * 8192 + i * 4096 + w * 1024); };
  auto issue_q = [&](int cc) { dma16(wsrc + chunk(cc) * (64 * 2688), lds + QS(cc) + w * QD(cc)); };
#pragma unroll
  for (int cc = 0; cc < 4; ++cc) {
    issue_w(cc, 0); issue_w(cc, 1);
    if (cc < 3) { issue_x(cc, 0); issue_x(cc, 1); issue_x(cc, 2); issue_x(cc, 3); }
  }
  __builtin_amdgcn_s_setprio(BPRIO);
  const u32 tag = load_tag(ws);

  const int r32 = lane & 31, half = lane >> 5;
  const int xrd = w * 4096 + r32 * 128 + ((half ^ ((r32 >> 1) & 7)) << 4);
  const int wrd = (8 * half + ((lane >> 2) & 3)) * 64 + ((lane >> 4) & 1) * 32 + (lane & 3) * 8;
  LAS char* wlo = lds + wrd; LAS char* whi = lds + WR + wrd;
  PIN(whi);
  u32x4 xf[28], wf[2][8];
  f32x16 ak = {}, av = {};
#pragma unroll
  for (int cc = 0; cc < 8; ++cc) {
    if (cc < 7) {
      if (cc == 0) VMCNT(14); else if (cc < 6) VMCNT(8); else VMCNT(7);
      BAR();
      if (cc == 0) { STAMP(1); }
#pragma unroll
      for (int s = 0; s < 4; ++s) {
        xf[cc * 4 + s] = *(LAS u32x4*)(lds + XS(cc % 3) + (xrd ^ (s << 5)));
        LAS char* p = whi + (cc & 3) * 8192 + s * 1024;
        const u32x2 k0 = tr_read(p), k1 = tr_read(p + 256), v0 = tr_read(p + 4096), v1 = tr_read(p + 4096 + 256);
        wf[cc & 1][2 * s] = (u32x4){k0.x, k0.y, k1.x, k1.y}; wf[cc & 1][2 * s + 1] = (u32x4){v0.x, v0.y, v1.x, v1.y};
      }
      SBAR();
    }
    if (cc > 0) {
#pragma unroll
      for (int i = 0; i < 8; ++i) {
        if (i & 1) av = mfma32(xf[(cc - 1) * 4 + (i >> 1)], wf[(cc - 1) & 1][i], av);
        else ak = mfma32(xf[(cc - 1) * 4 + (i >> 1)], wf[(cc - 1) & 1][i], ak);
        if (cc <= 4 && i < 4) issue_x(cc + 2, i);
        else if (cc <= 3 && i < 6) issue_w(cc + 3, i - 4);
        else if (cc == 4 && i < 6) issue_q(i - 4);
        else if (cc == 5 && i < 5) issue_q(i + 2);
        SBAR();
      }
    }
    if (cc < 7) {
#pragma unroll
      for (int s = 0; s < 4; ++s) { PIN(xf[cc * 4 + s]); PIN(wf[cc & 1][2 * s]); PIN(wf[cc & 1][2 * s + 1]); }
    }
  }
  STAMP(2);
  {
    u32x4 pk[2], pv[2];
#pragma unroll
    for (int s = 0; s < 2; ++s)
#pragma unroll
      for (int j = 0; j < 4; ++j) {
        pk[s][j] = phi2(ak[8 * s + 2 * j], ak[8 * s + 2 * j + 1]);
        const u32 v = pkbf(av[8 * s + 2 * j], av[8 * s + 2 * j + 1]);
        pv[s][j] = r32 == wone ? 0x3F803F80u : v;
      }
    f32x16 kv = {};
    kv = mfma32(pk[0], pv[0], kv); kv = mfma32(pk[1], pv[1], kv);
#pragma unroll
    for (int g = 0; g < 4; ++g) *(LAS f32x4*)(lds + XS(2) + w * 4096 + g * 1024 + lane * 16) = (f32x4){kv[4 * g], kv[4 * g + 1], kv[4 * g + 2], kv[4 * g + 3]};
    BAR();
    f32x4 s4 = *(LAS f32x4*)(lds + XS(2) + tid * 16);
#pragma unroll
    for (int ww = 1; ww < 4; ++ww) s4 += *(LAS f32x4*)(lds + XS(2) + ww * 4096 + tid * 16);
    u32x4 v = __builtin_bit_cast(u32x4, s4);
    const bool ck = lane == wone + 1;
    if (ck) v = (u32x4){0u, tag, 0u, 0u};
    const u32 c = tag - wave_sum(sum4(0u, v));
    if (ck) v.x = c;
    ST_SC1(v, rs, kvp_off);
    put_flag(ws, tag);
  }
  STAMP(3);
  const int rt = h & 3, ct = h >> 2;
  const int sg = (lane & 15) ^ (4 * (lane >> 4));
  const bool live = ct < 3 || sg < 8;
  const char* wosrc = wo + (u32)((28 * w + (lane >> 4)) * 896 + ct * 256 + sg * 16);
  auto issue_wo1 = [&](int pc, int ii) {
    if (live) dma16(wosrc + (112 * pc + 4 * ii) * 896, lds + ((pc & 1) ? WR : WOE) + (w * 7 + ii) * 1024);
  };
  auto issue_wo = [&](int pc) {
    if (live) {
#pragma unroll
      for (int ii = 0; ii < 7; ++ii) dma16(wosrc + (112 * pc + 4 * ii) * 896, lds + ((pc & 1) ? WR : WOE) + (w * 7 + ii) * 1024);
    }
  };
  const int woq = (lane >> 2) & 3, gam = 4 * ((lane >> 4) & 1) + (lane & 3), ng = 4 * (gam & 1) + (gam >> 1);
  LAS char* wob = lds + WOE + (14 * half + woq) * 256 + (w ^ ((2 * half + woq) & 3)) * 64 + 8 * ng;
  PIN(wob);
  u32x4 wof[32];
  auto read_wo = [&](int pc) {
#pragma unroll
    for (int hl = 0; hl < 4; ++hl) {
      LAS char* p = wob + ((pc & 1) ? WR - WOE : 0) + 28 * hl * 256;
      const u32x2 r0 = tr_read(p), r1 = tr_read(p + 1024), r2 = tr_read(p + 2048), r3 = tr_read(p + 3072);
      wof[pc * 8 + 2 * hl] = (u32x4){r0.x, r0.y, r1.x, r1.y};
      wof[pc * 8 + 2 * hl + 1] = (u32x4){r2.x, r2.y, r3.x, 0u};
    }
    SBAR();
  };
  auto pin_wo = [&](int pc) {
#pragma unroll
    for (int i = 0; i < 8; ++i) PIN(wof[pc * 8 + i]);
  };
  if (w == 0) VMCNT(2); else VMCNT(1);
  BAR();
  u32x4 pq[2];
  {
    f32x16 aq = {};
    u32x4 qf[2][4];
    auto read_q = [&](int cc) {
#pragma unroll
      for (int s = 0; s < 4; ++s) {
        LAS char* p = (QS(cc) >= WR ? whi + (QS(cc) - WR) : wlo + QS(cc)) + s * QD(cc);
        const u32x2 lo = tr_read(p), hi = tr_read(p + 256);
        qf[cc & 1][s] = (u32x4){lo.x, lo.y, hi.x, hi.y};
      }
      SBAR();
    };
    read_q(0);
#pragma unroll
    for (int cc = 0; cc < 7; ++cc) {
#pragma unroll
      for (int s = 0; s < 4; ++s) PIN(qf[cc & 1][s]);
      if (cc < 6) read_q(cc + 1);
#pragma unroll
      for (int s = 0; s < 4; ++s) {
        aq = mfma32(qf[cc & 1][s], xf[cc * 4 + s], aq);
        if (s == 0) issue_wo1(0, cc);
        SBAR();
      }
    }
#pragma unroll
    for (int s = 0; s < 2; ++s) {
#pragma unroll
      for (int j = 0; j < 4; ++j) pq[s][j] = phi2(aq[8 * s + 2 * j], aq[8 * s + 2 * j + 1]);
      PIN(pq[s]);
    }
  }
  STAMP(4);
  VMCNT(0);
  BAR();
  read_wo(0);
  issue_wo(1);
  pin_wo(0);
  STAMP(5);
  {
    u32x4 d[16];
#pragma nounroll
    for (int it = 0; it < SPIN_CAP; ++it) {
      asm volatile("" ::: "memory");
#pragma unroll
      for (int p = 0; p < 16; ++p) d[p] = LD_SC1(rs, WS_KVP + h * 65536 + p * 4096 + tid * 16);
      u32 m = 0;
#pragma unroll
      for (int p = 0; p < 16; ++p) m = sum4(m, d[p]);
      if (wave_sum(m) == 16u * tag) break;
    }
    f32x4 s4 = __builtin_bit_cast(f32x4, d[0]);
#pragma unroll
    for (int p = 1; p < 16; ++p) s4 += __builtin_bit_cast(f32x4, d[p]);
    const int dw = 8 * w + 4 * half - coff;
    u32x2 o2 = {pkbf(s4.x, s4.y), pkbf(s4.z, s4.w)};
    if (dw < 0 || dw >= 28) o2 = (u32x2){0u, 0u};
    *(LAS u32x2*)(lds + KVI + lane * 32 + w * 8) = o2;
  }
  STAMP(6);
  VMCNT(0);
  BAR();
  {
    const int i = (r32 & 3) + 4 * (r32 >> 3), hh = (r32 >> 2) & 1;
    const int ew = i < 14 ? 14 * hh + i + coff : wone;
    LAS char* p = lds + KVI + (ew + 32 * half) * 32;
    const u32x4 a0 = *(LAS u32x4*)p, a1 = *(LAS u32x4*)(p + 16);
    f32x16 nm = {};
    nm = mfma32(a0, pq[0], nm); nm = mfma32(a1, pq[1], nm);
    const float den = rbf(rbf(nm[14]) + 1e-6f);
    const float rc = __builtin_amdgcn_rcpf(den);
    u32 o[8];
#pragma unroll
    for (int j = 0; j < 7; ++j) { const u32 t = pkbf(nm[2 * j], nm[2 * j + 1]); o[j] = pkbf(bflo(t) * rc, bfhi(t) * rc); }
    o[7] = tag - ((o[0] + o[1] + o[2]) + (o[3] + o[4] + o[5]) + o[6]);
    ST_SC1(((u32x4){o[0], o[1], o[2], o[3]}), rs, ob_off);
    ST_SC1(((u32x4){o[4], o[5], o[6], o[7]}), rs, ob_off + 1024);
  }
  STAMP(7);
  read_wo(1);
  issue_wo(2);
  pin_wo(1);
  BAR();
  issue_wo(3);
  VMCNT(7); BAR();
  read_wo(2);
  STAMP(8);
  {
    u32x4 d[8];
#pragma nounroll
    for (int it = 0; it < SPIN_CAP; ++it) {
      asm volatile("" ::: "memory");
#pragma unroll
      for (int p = 0; p < 8; ++p) d[p] = LD_SC1(rs, WS_OB + ((rb * 4 + rt) * 16 + 4 * w) * 2048 + p * 1024 + lane * 16);
      u32 m = 0;
#pragma unroll
      for (int p = 0; p < 8; ++p) m = sum4(m, d[p]);
      if (__all(m == 4u * tag)) break;
    }
#pragma unroll
    for (int p = 1; p < 8; p += 2) d[p].w = 0u;
    STAMP(9);
    pin_wo(2);
    VMCNT(0);
    BAR();
#pragma unroll
    for (int p = 0; p < 8; ++p) *(LAS u32x4*)(lds + WOE + (8 * w + p) * 1024 + lane * 16) = d[p];
    read_wo(3);
    pin_wo(3);
    BAR();
  }
  if (ct < 3 || w < 2) {
    f32x16 acc = {};
#pragma unroll
    for (int k = 0; k < 32; ++k) acc = mfma32(wof[k], *(LAS u32x4*)(lds + WOE + k * 1024 + lane * 16), acc);
    const u32 dst = (u32)((128 * rb + 32 * rt + r32) * 1792 + 896 + ct * 256 + w * 64 + half * 32);
    ST_OUT(((u32x4){pkbf(acc[0], acc[1]), pkbf(acc[2], acc[3]), pkbf(acc[4], acc[5]), pkbf(acc[6], acc[7])}), dst);
    ST_OUT(((u32x4){pkbf(acc[8], acc[9]), pkbf(acc[10], acc[11]), pkbf(acc[12], acc[13]), pkbf(acc[14], acc[15])}), dst + 16);
  }
  STAMP(10);
  STAMP_FLUSH();
}

DI f32x2 rot(f32x2 b) { return (f32x2){-b.y, b.x}; }
DI f32x2 cm1(f32x2 a, f32x2 b) { return (f32x2){a.x, a.x} * b; }
DI f32x2 cm2(f32x2 a, f32x2 br, f32x2 t) { return __builtin_elementwise_fma((f32x2){a.y, a.y}, br, t); }
DI f32x2 cexp_p(float a2, float f, float p) {
  const float m = __builtin_amdgcn_exp2f(a2 * p), ph = __builtin_amdgcn_fractf(f * p);
  return (f32x2){m, m} * (f32x2){__builtin_amdgcn_cosf(ph), __builtin_amdgcn_sinf(ph)};
}
DI u32 wave_umax(u32 v) {
  u32 t;
  t = __builtin_amdgcn_update_dpp(0u, v, 0x111, 0xf, 0xf, false); v = v > t ? v : t;
  t = __builtin_amdgcn_update_dpp(0u, v, 0x112, 0xf, 0xf, false); v = v > t ? v : t;
  t = __builtin_amdgcn_update_dpp(0u, v, 0x114, 0xf, 0xf, false); v = v > t ? v : t;
  t = __builtin_amdgcn_update_dpp(0u, v, 0x118, 0xf, 0xf, false); v = v > t ? v : t;
  const u32 a = __builtin_amdgcn_readlane(v, 15), b = __builtin_amdgcn_readlane(v, 31), c = __builtin_amdgcn_readlane(v, 47), d = __builtin_amdgcn_readlane(v, 63);
  const u32 ab = a > b ? a : b, cd = c > d ? c : d;
  return ab > cd ? ab : cd;
}

DI void role_a(LAS char* lds, const char* __restrict__ u, const u32* __restrict__ logA, const u32* __restrict__ Aim,
               const u32* __restrict__ Cre, const u32* __restrict__ Cim, const u16* __restrict__ logdt,
               const char* __restrict__ Dsk, const char* __restrict__ wo, char* __restrict__ out, char* ws, u64 t_first) {
  const int a = blockIdx.x, cb = a >> 5, jp = a & 31, tid = threadIdx.x, lane = tid & 63;
  const int w = __builtin_amdgcn_readfirstlane(tid >> 6);
  STAMP_DECL;
  STAMP(0);
  const __amdgpu_buffer_rsrc_t rs = __builtin_amdgcn_make_buffer_rsrc(ws, 0, WS_END, 0x00020000);
  const int ch = w >> 1, hf = w & 1, d = 2 * a + ch;
  const int pi = d * 128 + hf * 64 + lane;
  u32x4 ub[4];
  if (w < 2) {
#pragma unroll
    for (int i = 0; i < 4; ++i) ub[i] = *(const u32x4*)(u + (u32)((64 * jp + 4 * (tid & 15) + i) * 896 + cb * 128 + (tid >> 4) * 16));
  }
  const u32 pA = logA[pi], pI = Aim[pi], pR = Cre[pi], pC = Cim[pi];
  const float ldt = bflo(logdt[d]);
  u32x4 us[2];
#pragma unroll
  for (int i = 0; i < 2; ++i) us[i] = *(const u32x4*)(u + (u32)((64 * jp + 32 * i + (tid >> 3)) * 896 + cb * 128 + (tid & 7) * 16));
  const u32x4 dk = *(const u32x4*)(Dsk + cb * 128 + (tid & 7) * 16);
  u32 dummy = 0;
  const u32 tag = load_tag(ws);
  int tix[8];
#pragma unroll
  for (int j = 0; j < 8; ++j) {
    const int p = (int)rbf((float)(2047 - 8 * tid - j));
    const int low = p & 7, blk = p >> 8;
    const int k4 = (blk < 4 ? blk : 4) * 4;
    const int sh = (0x32210 >> k4) & 15;
    const int base = blk + ((0xCBA70 >> k4) & 15);
    tix[j] = ((base + (low >> sh)) * 32 + ((p >> 3) & 31)) * 4;
    PIN(tix[j]);
  }
  if (w < 2) {
    u32x4 ck;
#pragma unroll
    for (int k = 0; k < 4; ++k) {
      const u32x4 v = {ub[0][k], ub[1][k], ub[2][k], ub[3][k]};
      ST_SC1(v, rs, WS_UX + (cb * 32 + 4 * (tid >> 4) + k) * 8192 + jp * 256 + (tid & 15) * 16);
      ck[k] = tag - row_sum(sum4(0u, v));
    }
    if ((tid & 15) == 15) ST_SC1(ck, rs, WS_UCK + a * 128 + (tid >> 4) * 16);
  }
  put_flag(ws, tag);
  STAMP(1);
  constexpr int SROW = 272, STG = 53 * SROW, TBL = 4 * STG, UC = 0, UCS = 4160, RV = 8 * UCS, RVS = 4608, EX = RV + 2 * RVS;
  const float dt = rbf(expf(ldt));
  const u32 mx = wave_umax(umax3(umax3(pR & 0x7fffu, (pR >> 16) & 0x7fffu, pC & 0x7fffu), (pC >> 16) & 0x7fffu, 0u));
  int ex = mx >> 7; ex = ex < 13 ? 13 : ex > 254 ? 254 : ex;
  const float sc = __uint_as_float((u32)(266 - ex) << 23), isc = __uint_as_float((u32)(ex - 12) << 23);
  f32x16 kacc = {};
  LAS char* stg = lds + w * STG;
  const int r32 = lane & 31, half = lane >> 5;
  LAS char* px = stg + r32 * SROW + half * 16;
  LAS char* py = stg + (32 + (r32 < 20 ? r32 : 20)) * SROW + half * 16;
  u32x4 fx[8], fy[8];
  u32x4 ud[3]; u32 uck;
  const __amdgpu_buffer_rsrc_t ru = __builtin_amdgcn_make_buffer_rsrc(ws + WS_UX + a * 8192, 0, 8192, 0x00020000);
  const int npc = w < 3 ? 9 : 8;
  auto load_u = [&]() {
    ud[0] = LD_SC1(ru, w * 2048 + lane * 16); ud[1] = LD_SC1(ru, w * 2048 + 1024 + lane * 16);
    ud[2] = LD_SC1(ru, lane < 16 ? w * 2048 + 2048 + lane * 16 : 8192);
    uck = LD1_SC1(rs, lane < npc ? WS_UCK + (cb * 32 + 8 * w + lane) * 128 + jp * 4 : WS_END);
  };
#pragma unroll
  for (int bt = 0; bt < 2; ++bt) {
    const float la = bt ? bfhi(pA) : bflo(pA), ai = bt ? bfhi(pI) : bflo(pI), cr = bt ? bfhi(pR) : bflo(pR), ci = bt ? bfhi(pC) : bflo(pC);
    const float a2 = -rbf(expf(la)) * dt * 1.4426950408889634f, f = ai * dt * 0.15915494309189535f;
    LAS u32* sw = (LAS u32*)(stg + lane * 4);
#define STEP(p0, b0, b0r, r0, p1, b1, b1r, r1) do { \
      const f32x2 t0_ = cm1(p0, b0), t1_ = cm1(p1, b1); const u32 c0_ = pkh(p0), c1_ = pkh(p1); SBAR(); \
      p0 = cm2(p0, b0r, t0_); p1 = cm2(p1, b1r, t1_); sw[(r0) * (SROW / 4)] = c0_; sw[(r1) * (SROW / 4)] = c1_; SBAR(); } while (0)
    const f32x2 z8 = cexp_p(a2, f, 8.f), y1 = cexp_p(a2, -f, 1.f), yq = cexp_p(a2, -f, 256.f);
    const f32x2 z8r = rot(z8), y1r = rot(y1), yqr = rot(yq);
    f32x2 x0 = (f32x2){cr, ci} * sc;
    f32x2 x1 = cm2(x0, z8r, cm1(x0, z8));
    const f32x2 z16 = cm2(z8, z8r, cm1(z8, z8)), z16r = rot(z16);
    SBAR();
#pragma unroll
    for (int r = 0; r < 30; r += 2) {
      STEP(x0, z16, z16r, r, x1, z16, z16r, r + 1);
      if (bt == 1 && r < 16) { kacc = mfma32h(fy[r >> 1], fx[r >> 1], kacc); SBAR(); }
    }
    sw[30 * (SROW / 4)] = pkh(x0); sw[31 * (SROW / 4)] = pkh(x1);
    if (bt == 1) { STAMP(2); load_u(); SBAR(); }
    {
      f32x2 p = y1, q = yq, y2, y4, y6, v;
      sw[32 * (SROW / 4)] = 0x00003C00u;
      STEP(p, y1, y1r, 33, q, yq, yqr, 40); y2 = p;
      STEP(p, y1, y1r, 34, q, yq, yqr, 44);
      STEP(p, y1, y1r, 35, q, yq, yqr, 46); y4 = p;
      STEP(p, y1, y1r, 36, q, yq, yqr, 48);
      STEP(p, y1, y1r, 37, q, yq, yqr, 49); y6 = p;
      STEP(p, y1, y1r, 38, q, yq, yqr, 50);
      v = cm2(y4, yqr, cm1(y4, yq));
      sw[39 * (SROW / 4)] = pkh(p);
      STEP(v, yq, yqr, 42, q, yq, yqr, 51);
      y2 = cm2(y2, yqr, cm1(y2, yq)); y6 = cm2(y6, yqr, cm1(y6, yq));
      sw[52 * (SROW / 4)] = pkh(q); sw[45 * (SROW / 4)] = pkh(v);
      v = cm2(v, yqr, cm1(v, yq));
      sw[41 * (SROW / 4)] = pkh(y2); sw[43 * (SROW / 4)] = pkh(y6); sw[47 * (SROW / 4)] = pkh(v);
    }
#undef STEP
#pragma unroll
    for (int s = 0; s < 8; ++s) { fy[s] = *(LAS u32x4*)(py + 32 * s); fx[s] = *(LAS u32x4*)(px + 32 * s); }
    SBAR();
  }
  STAMP(3);
#pragma unroll
  for (int s = 0; s < 8; ++s) kacc = mfma32h(fy[s], fx[s], kacc);
  u32 sk[8];
#pragma unroll
  for (int i = 0; i < 2; ++i)
#pragma unroll
    for (int k = 0; k < 4; ++k) { sk[4 * i + k] = pkbf(bflo(us[i][k]) * bflo(dk[k]), bfhi(us[i][k]) * bfhi(dk[k])); PIN(sk[4 * i + k]); }
  {
    const float fac = 2.f * dt * isc;
#pragma unroll
    for (int i = 0; i < 12; ++i) *(LAS float*)(lds + TBL + w * 3072 + (((i & 3) + 8 * (i >> 2) + 4 * half) * 32 + r32) * 4) = kacc[i] * fac;
  }
  STAMP(4);
  BAR();
#pragma unroll
  for (int c = 0; c < 2; ++c) {
    float kk[8];
#pragma unroll
    for (int j = 0; j < 8; ++j) kk[j] = *(LAS float*)(lds + TBL + c * 6144 + tix[j]) + *(LAS float*)(lds + TBL + c * 6144 + 3072 + tix[j]);
    *(LAS u32x4*)(lds + RV + c * RVS + tid * 16) = (u32x4){pkbf(kk[0], kk[1]), pkbf(kk[2], kk[3]), pkbf(kk[4], kk[5]), pkbf(kk[6], kk[7])};
  }
  if (tid < 64) *(LAS u32x4*)(lds + RV + half * RVS + 4096 + r32 * 16) = (u32x4){0u, 0u, 0u, 0u};
  SBAR();
#pragma nounroll
  for (int it = 0; it < SPIN_CAP; ++it) {
    if (wave_sum(sum4(sum4(sum4(uck, ud[0]), ud[1]), ud[2])) == npc * tag) break;
    asm volatile("" ::: "memory");
    load_u();
  }
  asm volatile("" :: "v"(dummy));
#pragma unroll
  for (int i = 0; i < 2; ++i) {
    const u32 nx = __builtin_amdgcn_update_dpp(__builtin_amdgcn_readlane(ud[i + 1].x, 0), ud[i].x, 0x130, 0xf, 0xf, false);
    const u32 dw[5] = {ud[i].x, ud[i].y, ud[i].z, ud[i].w, nx};
    LAS char* p = lds + UC + (128 * w + 64 * i + lane + 4) * 8;
#pragma unroll
    for (int c = 0; c < 2; ++c) {
      u32 pr[4];
#pragma unroll
      for (int k = 0; k < 4; ++k) pr[k] = __builtin_amdgcn_perm(dw[k + 1], dw[k], c ? 0x07060302u : 0x05040100u);
      *(LAS u32x2*)(p + (4 * c) * UCS) = (u32x2){pr[0], pr[2]};
      *(LAS u32x2*)(p + (4 * c + 1) * UCS) = (u32x2){pr[1], pr[3]};
      *(LAS u32*)(p + (4 * c + 2) * UCS - 4) = pr[0]; *(LAS u32*)(p + (4 * c + 2) * UCS) = pr[2];
      *(LAS u32*)(p + (4 * c + 3) * UCS - 4) = pr[1]; *(LAS u32*)(p + (4 * c + 3) * UCS) = pr[3];
    }
  }
  if (w == 0) {
    const u32 u0 = __builtin_amdgcn_readlane(ud[0].x, 0);
    const int cs = lane >> 3, ix = lane & 7, last = (cs & 2) ? 6 : 7;
    const u32 v = ((cs & 1) && ix == last) ? (cs & 4 ? u0 & 0xffff0000u : u0 << 16) : 0u;
    if (ix <= last) *(LAS u32*)(lds + UC + cs * UCS + ix * 4) = v;
  }
  BAR();
  STAMP(5);
  {
    const int c16 = lane & 15, hq = lane >> 4;
    LAS char* pa = lds + RV + ch * RVS + 2 * (2032 - 256 * 7 + 128 * hf - 16 * c16 + 8 * hq);
    LAS char* pb = lds + UC + (4 * ch + ((c16 + 1) & 3)) * UCS + 2 * (128 * hf + 8 * hq + 4 * ((c16 + 1) >> 2));
    f32x4 acc[8] = {};
    u32x4 fa[2][8], fb[2][8];
    auto ldf = [&](int rho, int m) {
      fa[rho & 1][m] = *(LAS u32x4*)(pa + 64 * rho + 512 * (7 - m));
      const u32x2 lo = *(volatile LAS u32x2*)(pb + 64 * rho + 512 * m), hi = *(volatile LAS u32x2*)(pb + 64 * rho + 512 * m + 8);
      fb[rho & 1][m] = (u32x4){lo.x, lo.y, hi.x, hi.y};
    };
#pragma unroll
    for (int m = 0; m < 8; ++m) ldf(0, m);
#pragma unroll
    for (int rho = 0; rho < 4; ++rho) {
      SBAR();
#pragma unroll
      for (int g = 0; g < 8; ++g) {
#pragma unroll
        for (int aa = 0; aa <= g; ++aa) {
          if (aa + g < 8) acc[aa + g] = mfma16(fa[rho & 1][g], fb[rho & 1][aa], acc[aa + g]);
          if (aa < g && aa + g < 8) acc[aa + g] = mfma16(fa[rho & 1][aa], fb[rho & 1][g], acc[aa + g]);
        }
        if (rho < 3) { ldf(rho + 1, g); SBAR(); }
      }
    }
    STAMP(6);
#pragma unroll
    for (int t = 0; t < 8; ++t) *(LAS f32x4*)(lds + EX + w * 8192 + t * 1024 + lane * 16) = acc[t];
    BAR();
#pragma unroll
    for (int i = 0; i < 2; ++i) {
      LAS char* p = lds + EX + (2 * w + i) * 1024 + lane * 16;
      const f32x4 y0 = *(LAS f32x4*)p + *(LAS f32x4*)(p + 8192), y1 = *(LAS f32x4*)(p + 16384) + *(LAS f32x4*)(p + 24576);
      const u32x4 v = {pkbf(y0.x, y1.x), pkbf(y0.y, y1.y), pkbf(y0.z, y1.z), pkbf(y0.w, y1.w)};
      ST_SC1(v, rs, WS_YX + (cb * 32 + 4 * (2 * w + i) + hq) * 8192 + jp * 256 + c16 * 16);
      const u32 ck = tag - row_sum(sum4(0u, v));
      if (c16 == 15) ST1_SC1(ck, rs, WS_YCK + a * 128 + (4 * (2 * w + i) + hq) * 4);
    }
  }
  STAMP(7);
  {
    u32x4 y[2];
    __builtin_amdgcn_s_sleep(8);
#pragma nounroll
    for (int it = 0; it < SPIN_CAP; ++it) {
      asm volatile("" ::: "memory");
      y[0] = LD_SC1(rs, WS_YX + a * 8192 + tid * 16); y[1] = LD_SC1(rs, WS_YX + a * 8192 + 4096 + tid * 16);
      const u32 ck = LD1_SC1(rs, lane < 8 ? WS_YCK + (cb * 32 + 16 * (lane >> 2) + 4 * w + (lane & 3)) * 128 + jp * 4 : WS_END);
      if (wave_sum(sum4(sum4(ck, y[0]), y[1])) == 8u * tag) break;
      __builtin_amdgcn_s_sleep(2);
    }
    STAMP(8);
#pragma unroll
    for (int i = 0; i < 2; ++i)
#pragma unroll
      for (int g = 0; g < 4; ++g) *(LAS u32*)(lds + (16 * g + (tid & 15)) * 144 + (16 * i + (tid >> 4)) * 4) = y[i][g];
    BAR();
#pragma unroll
    for (int i = 0; i < 2; ++i) {
      const u32x4 v = *(LAS u32x4*)(lds + (32 * i + (tid >> 3)) * 144 + (tid & 7) * 16);
      u32x4 o;
#pragma unroll
      for (int k = 0; k < 4; ++k) o[k] = pkbf(bflo(v[k]) + bflo(sk[4 * i + k]), bfhi(v[k]) + bfhi(sk[4 * i + k]));
      ST_OUT(o, (u32)((64 * jp + 32 * i + (tid >> 3)) * 1792 + cb * 128 + (tid & 7) * 16));
    }
  }
  STAMP(9);
  STAMP_FLUSH();
}

__global__ void __launch_bounds__(256, 2)
fused_kernel(const char* __restrict__ u, const u32* __restrict__ logA, const u32* __restrict__ Aim, const u32* __restrict__ Cre,
             const u32* __restrict__ Cim, const u16* __restrict__ logdt, const char* __restrict__ Dsk, const char* __restrict__ x,
             const char* __restrict__ wqkv, const char* __restrict__ wo, char* __restrict__ out, char* ws) {
  __shared__ __attribute__((aligned(16))) char smem[81920];
  LAS char* lds = (LAS char*)smem;
  u64 t_first = 0;
  asm volatile("" :: "s"(u), "s"(logA), "s"(Aim), "s"(Cre), "s"(Cim), "s"(logdt), "s"(Dsk), "s"(x), "s"(wqkv), "s"(wo), "s"(out), "s"(ws));
  if (blockIdx.x >= 256) role_b(lds, x, wqkv, wo, out, ws, t_first);
  else if (blockIdx.x < 224) role_a(lds, u, logA, Aim, Cre, Cim, logdt, Dsk, wo, out, ws, t_first);
  else {
    const int t = threadIdx.x;
    if (blockIdx.x == 224) {
      const u32 tag = load_tag(ws);
      __builtin_amdgcn_s_sleep(127);
      wait_flags(ws, 256 + t, flag_of(tag));
      wait_flags(ws, t < 224 ? t : 0, flag_of(tag));
      __syncthreads();
      if (t == 0) __hip_atomic_store((GAS u32*)ws, tag, RLX_AGENT);
    }
  }
}

extern "C" void kernel_launch(void* const* d_in, const int* in_sizes, int n_in, void* d_out, int out_size, void* d_ws,
                              size_t ws_size, hipStream_t stream) {
  fused_kernel<<<512, 256, 0, stream>>>((const char*)d_in[0], (const u32*)d_in[1], (const u32*)d_in[2], (const u32*)d_in[3],
                                        (const u32*)d_in[4], (const u16*)d_in[5], (const char*)d_in[6], (const char*)d_in[7],
                                        (const char*)d_in[8], (const char*)d_in[9], (char*)d_out, (char*)d_ws);
}
```

```cpp
#define STAMPS
#define READOUT 0x30
#define EARLY0
#define SPINT 3000
#include <hip/hip_runtime.h>
#include <stdint.h>

typedef unsigned short u16;
typedef unsigned u32;
typedef unsigned long long u64;
typedef short s16x4 __attribute__((ext_vector_type(4)));
typedef __bf16 bf16x8 __attribute__((ext_vector_type(8)));
typedef __bf16 bf16x2 __attribute__((ext_vector_type(2)));
typedef _Float16 f16x8 __attribute__((ext_vector_type(8)));
typedef _Float16 f16x2 __attribute__((ext_vector_type(2)));
typedef float f32x16 __attribute__((ext_vector_type(16)));
typedef float f32x4 __attribute__((ext_vector_type(4)));
typedef float f32x2 __attribute__((ext_vector_type(2)));
typedef u32 u32x4 __attribute__((ext_vector_type(4)));
typedef u32 u32x2 __attribute__((ext_vector_type(2)));
#define LAS __attribute__((address_space(3)))
#define GAS __attribute__((address_space(1)))
#define DI __device__ __forceinline__
#define RLX_AGENT __ATOMIC_RELAXED, __HIP_MEMORY_SCOPE_AGENT

#define BMAP 0
#define PREFETCH 0
#define CODE_BEFORE 0
#define CODE_LINES 180
#define BPRIO 2

constexpr u32 WS_FLAG = 4096;
constexpr u32 WS_UCK = 73728;
constexpr u32 WS_YCK = WS_UCK + 28672;
constexpr u32 WS_KVP = 1u << 17;
constexpr u32 WS_OB = WS_KVP + (1u << 20);
constexpr u32 WS_UX = WS_OB + (2u << 20);
constexpr u32 WS_YX = WS_UX + (2u << 20);
constexpr u32 WS_END = WS_YX + (2u << 20);
constexpr u32 WS_DBG = WS_END;
constexpr int SPIN_CAP = 1 << 15;

#define STAMP(i) do { st_[i] = __builtin_amdgcn_s_memrealtime(); } while (0)
#define STAMP_DECL u64 st_[16] = {}
#define STAMP_FLUSH() do { if (lane == 0) { GAS u64* p_ = (GAS u64*)(ws + WS_DBG) + (blockIdx.x * 4 + w) * 16; _Pragma("unroll") for (int i_ = 0; i_ < 16; ++i_) __hip_atomic_store(p_ + i_, st_[i_], RLX_AGENT); } \
   asm volatile("s_waitcnt vmcnt(0)" ::: "memory"); __syncthreads(); if (threadIdx.x == 0) __hip_atomic_store((GAS u64*)(ws + WS_FLAG) + blockIdx.x * 16 + 8, flag_of(tag), RLX_AGENT); } while (0)

DI float bflo(u32 v) { return __uint_as_float(v << 16); }
DI float bfhi(u32 v) { return __uint_as_float(v & 0xffff0000u); }
DI u32 pkbf(float a, float b) { f32x2 p = {a, b}; return __builtin_bit_cast(u32, __builtin_convertvector(p, bf16x2)); }
DI u32 pkh(f32x2 p) { return __builtin_bit_cast(u32, __builtin_convertvector(p, f16x2)); }
DI float rbf(float a) { return bflo(pkbf(a, a)); }
DI u32 umax3(u32 a, u32 b, u32 c) { u32 m = a > b ? a : b; return m > c ? m : c; }
DI u32 sum4(u32 m, u32x4 v) { return (m + v.x + v.y) + v.z + v.w; }
DI u32 row_sum(u32 v) {
  v += __builtin_amdgcn_update_dpp(0u, v, 0x111, 0xf, 0xf, true); v += __builtin_amdgcn_update_dpp(0u, v, 0x112, 0xf, 0xf, true);
  v += __builtin_amdgcn_update_dpp(0u, v, 0x114, 0xf, 0xf, true); v += __builtin_amdgcn_update_dpp(0u, v, 0x118, 0xf, 0xf, true);
  return v;
}
DI u32 wave_sum(u32 v) {
  v = row_sum(v);
  return (__builtin_amdgcn_readlane(v, 15) + __builtin_amdgcn_readlane(v, 31)) + (__builtin_amdgcn_readlane(v, 47) + __builtin_amdgcn_readlane(v, 63));
}
DI u32 load_tag(const char* ws) { u32 t = *(const __attribute__((address_space(4))) u32*)ws * 1664525u + 1013904223u; asm volatile("" : "+s"(t)); return t; }
DI u64 flag_of(u32 tag) { return ((u64)(tag * 0x9E3779B1u + 1u) << 32) | tag; }
DI void put_flag(char* ws, u32 tag) { if (threadIdx.x == 0) __hip_atomic_store((GAS u64*)(ws + WS_FLAG) + blockIdx.x * 16, flag_of(tag), RLX_AGENT); }
#define SBAR() __builtin_amdgcn_sched_barrier(0)
#define PIN(x) asm volatile("" : "+v"(x))
DI f32x16 mfma32(u32x4 a, u32x4 b, f32x16 c) { return __builtin_amdgcn_mfma_f32_32x32x16_bf16(__builtin_bit_cast(bf16x8, a), __builtin_bit_cast(bf16x8, b), c, 0, 0, 0); }
DI f32x16 mfma32h(u32x4 a, u32x4 b, f32x16 c) { return __builtin_amdgcn_mfma_f32_32x32x16_f16(__builtin_bit_cast(f16x8, a), __builtin_bit_cast(f16x8, b), c, 0, 0, 0); }
DI f32x4 mfma16(u32x4 a, u32x4 b, f32x4 c) { return __builtin_amdgcn_mfma_f32_16x16x32_bf16(__builtin_bit_cast(bf16x8, a), __builtin_bit_cast(bf16x8, b), c, 0, 0, 0); }
DI u32x2 tr_read(LAS char* p) { return __builtin_bit_cast(u32x2, __builtin_amdgcn_ds_read_tr16_b64_v4i16((LAS s16x4*)p)); }
DI void dma16(const void* src, LAS char* dst) { __builtin_amdgcn_global_load_lds((const GAS void*)src, (LAS void*)dst, 16, 0, 0); }
#define VMCNT(n) asm volatile("s_waitcnt vmcnt(" #n ")" ::: "memory")
#define LGKM0() asm volatile("s_waitcnt lgkmcnt(0)" ::: "memory")
#define BAR() do { asm volatile("s_waitcnt lgkmcnt(0)" ::: "memory"); __builtin_amdgcn_s_barrier(); asm volatile("" ::: "memory"); } while (0)
#define OUTAUX 0
#define ST_OUT(v, off) __builtin_amdgcn_raw_buffer_store_b128((v), __builtin_amdgcn_make_buffer_rsrc(out, 0, 2048 * 1792, 0x00020000), (off), 0, OUTAUX)
#define ST_SC1(v, rs, off) __builtin_amdgcn_raw_buffer_store_b128((v), (rs), (off), 0, 16)
#define LD_SC1(rs, off) __builtin_amdgcn_raw_buffer_load_b128((rs), (off), 0, 16)
#define ST1_SC1(v, rs, off) __builtin_amdgcn_raw_buffer_store_b32((v), (rs), (off), 0, 16)
#define LD1_SC1(rs, off) __builtin_amdgcn_raw_buffer_load_b32((rs), (off), 0, 16)

DI u32 phi2(float a, float b) {
  const u32 t = pkbf(a, b);
  const f32x2 x = {bflo(t), bfhi(t)};
  const f32x2 y = x * 1.4426950408889634f;
  const float e0 = __builtin_amdgcn_fmed3f(__builtin_amdgcn_exp2f(y.x), 0.f, 1.f);
  const float e1 = __builtin_amdgcn_fmed3f(__builtin_amdgcn_exp2f(y.y), 0.f, 1.f);
  const f32x2 x1 = x + 1.0f;
  const u32 r = pkbf(__builtin_fmaxf(x1.x, e0), __builtin_fmaxf(x1.y, e1));
  const f32x2 z = (f32x2){bflo(r), bfhi(r)} + 1e-6f;
  return pkbf(z.x, z.y);
}

DI void wait_flags(const char* ws, int idx, u64 tag) {
  const GAS u64* f = (const GAS u64*)(ws + WS_FLAG) + idx * 16;
#pragma nounroll
  for (int it = 0; it < SPIN_CAP; ++it) {
    const u64 v = __hip_atomic_load(f, RLX_AGENT);
    if (__all(v == tag)) break;
    __builtin_amdgcn_s_sleep(16);
  }
}

DI void role_b(LAS char* lds, const char* __restrict__ x, const char* __restrict__ wqkv, const char* __restrict__ wo,
               char* __restrict__ out, char* ws, u64 t_first) {
  const int b = blockIdx.x - 256, tid = threadIdx.x, lane = tid & 63;
  const int w = __builtin_amdgcn_readfirstlane(tid >> 6);
  STAMP_DECL;
  STAMP(0);
  st_[15] = st_[0]; st_[0] = t_first;
  const int h = 2 * (b & 7) + ((b >> 3) & 1), rb = b >> 4;
  const int start = (rb + 3 * (h & 1)) % 7;
  const __amdgpu_buffer_rsrc_t rs = __builtin_amdgcn_make_buffer_rsrc(ws, 0, WS_END, 0x00020000);
  const u32 kvp_off = WS_KVP + (h * 16 + rb) * 4096 + tid * 16;
  const u32 ob_off = WS_OB + ((rb * 4 + w) * 16 + h) * 2048 + lane * 16;

  constexpr int WR = 49152, KVI = 45056, WOE = 16384;
#define XS(i) ((i) == 0 ? 16384 : (i) == 1 ? 0 : 32768)
#define QS(cc) ((cc) < 2 ? WR + 24576 + (cc) * 4096 : (cc) < 6 ? XS(1) + ((cc) - 2) * 1024 : WR)
#define QD(cc) ((cc) >= 2 && (cc) < 6 ? 4096 : 1024)
  const int wsb = (56 * h) & ~15;
  const int coff = (h & 1) * 4;
  const int wone = (h & 1) ? 0 : 28;
  const char* xs0 = x + (u32)((128 * rb + 32 * w + (lane >> 3)) * 896 + (((lane & 7) ^ (lane >> 4)) << 4));
  const char* xs1 = x + (u32)((128 * rb + 32 * w + 8 + (lane >> 3)) * 896 + (((lane & 7) ^ (4 + (lane >> 4))) << 4));
  const char* wsrc = wqkv + (u32)((16 * w + (lane >> 2)) * 2688 + wsb + (lane & 3) * 16);
  auto chunk = [&](int cc) { int c = cc + start; return c >= 7 ? c - 7 : c; };
  auto issue_x = [&](int cc, int i) { dma16(((i & 1) ? xs1 : xs0) + chunk(cc) * 128 + (i >> 1) * (16 * 896), lds + XS(cc % 3) + w * 4096 + i * 1024); };
  auto issue_w = [&](int cc, int i) { dma16(wsrc + chunk(cc) * (64 * 2688) + 896 * (i + 1), lds + WR + (cc & 3) * 8192 + i * 4096 + w * 1024); };
  auto issue_q = [&](int cc) { dma16(wsrc + chunk(cc) * (64 * 2688), lds + QS(cc) + w * QD(cc)); };
#pragma unroll
  for (int cc = 0; cc < 4; ++cc) {
    issue_w(cc, 0); issue_w(cc, 1);
    if (cc < 3) { issue_x(cc, 0); issue_x(cc, 1); issue_x(cc, 2); issue_x(cc, 3); }
  }
  __builtin_amdgcn_s_setprio(BPRIO);
  const u32 tag = load_tag(ws);

  const int r32 = lane & 31, half = lane >> 5;
  const int xrd = w * 4096 + r32 * 128 + ((half ^ ((r32 >> 1) & 7)) << 4);
  const int wrd = (8 * half + ((lane >> 2) & 3)) * 64 + ((lane >> 4) & 1) * 32 + (lane & 3) * 8;
  LAS char* wlo = lds + wrd; LAS char* whi = lds + WR + wrd;
  PIN(whi);
  u32x4 xf[28], wf[2][8];
  f32x16 ak = {}, av = {};
#pragma unroll
  for (int cc = 0; cc < 8; ++cc) {
    if (cc < 7) {
      if (cc == 0) VMCNT(14); else if (cc < 6) VMCNT(8); else VMCNT(7);
      BAR();
      if (cc == 0) { STAMP(1); }
#pragma unroll
      for (int s = 0; s < 4; ++s) {
        xf[cc * 4 + s] = *(LAS u32x4*)(lds + XS(cc % 3) + (xrd ^ (s << 5)));
        LAS char* p = whi + (cc & 3) * 8192 + s * 1024;
        const u32x2 k0 = tr_read(p), k1 = tr_read(p + 256), v0 = tr_read(p + 4096), v1 = tr_read(p + 4096 + 256);
        wf[cc & 1][2 * s] = (u32x4){k0.x, k0.y, k1.x, k1.y}; wf[cc & 1][2 * s + 1] = (u32x4){v0.x, v0.y, v1.x, v1.y};
      }
      SBAR();
    }
    if (cc > 0) {
#pragma unroll
      for (int i = 0; i < 8; ++i) {
        if (i & 1) av = mfma32(xf[(cc - 1) * 4 + (i >> 1)], wf[(cc - 1) & 1][i], av);
        else ak = mfma32(xf[(cc - 1) * 4 + (i >> 1)], wf[(cc - 1) & 1][i], ak);
        if (cc <= 4 && i < 4) issue_x(cc + 2, i);
        else if (cc <= 3 && i < 6) issue_w(cc + 3, i - 4);
        else if (cc == 4 && i < 6) issue_q(i - 4);
        else if (cc == 5 && i < 5) issue_q(i + 2);
        SBAR();
      }
    }
    if (cc < 7) {
#pragma unroll
      for (int s = 0; s < 4; ++s) { PIN(xf[cc * 4 + s]); PIN(wf[cc & 1][2 * s]); PIN(wf[cc & 1][2 * s + 1]); }
    }
  }
  STAMP(2);
  {
    u32x4 pk[2], pv[2];
#pragma unroll
    for (int s = 0; s < 2; ++s)
#pragma unroll
      for (int j = 0; j < 4; ++j) {
        pk[s][j] = phi2(ak[8 * s + 2 * j], ak[8 * s + 2 * j + 1]);
        const u32 v = pkbf(av[8 * s + 2 * j], av[8 * s + 2 * j + 1]);
        pv[s][j] = r32 == wone ? 0x3F803F80u : v;
      }
    f32x16 kv = {};
    kv = mfma32(pk[0], pv[0], kv); kv = mfma32(pk[1], pv[1], kv);
#pragma unroll
    for (int g = 0; g < 4; ++g) *(LAS f32x4*)(lds + XS(2) + w * 4096 + g * 1024 + lane * 16) = (f32x4){kv[4 * g], kv[4 * g + 1], kv[4 * g + 2], kv[4 * g + 3]};
    BAR();
    f32x4 s4 = *(LAS f32x4*)(lds + XS(2) + tid * 16);
#pragma unroll
    for (int ww = 1; ww < 4; ++ww) s4 += *(LAS f32x4*)(lds + XS(2) + ww * 4096 + tid * 16);
    u32x4 v = __builtin_bit_cast(u32x4, s4);
    const bool ck = lane == wone + 1;
    if (ck) v = (u32x4){0u, tag, 0u, 0u};
    const u32 c = tag - wave_sum(sum4(0u, v));
    if (ck) v.x = c;
    ST_SC1(v, rs, kvp_off);
    put_flag(ws, tag);
  }
  STAMP(3);
  const int rt = h & 3, ct = h >> 2;
  const int sg = (lane & 15) ^ (4 * (lane >> 4));
  const bool live = ct < 3 || sg < 8;
  const char* wosrc = wo + (u32)((28 * w + (lane >> 4)) * 896 + ct * 256 + sg * 16);
  auto issue_wo1 = [&](int pc, int ii) {
    if (live) dma16(wosrc + (112 * pc + 4 * ii) * 896, lds + ((pc & 1) ? WR : WOE) + (w * 7 + ii) * 1024);
  };
  auto issue_wo = [&](int pc) {
    if (live) {
#pragma unroll
      for (int ii = 0; ii < 7; ++ii) dma16(wosrc + (112 * pc + 4 * ii) * 896, lds + ((pc & 1) ? WR : WOE) + (w * 7 + ii) * 1024);
    }
  };
  const int woq = (lane >> 2) & 3, gam = 4 * ((lane >> 4) & 1) + (lane & 3), ng = 4 * (gam & 1) + (gam >> 1);
  LAS char* wob = lds + WOE + (14 * half + woq) * 256 + (w ^ ((2 * half + woq) & 3)) * 64 + 8 * ng;
  PIN(wob);
  u32x4 wof[32];
  auto read_wo = [&](int pc) {
#pragma unroll
    for (int hl = 0; hl < 4; ++hl) {
      LAS char* p = wob + ((pc & 1) ? WR - WOE : 0) + 28 * hl * 256;
      const u32x2 r0 = tr_read(p), r1 = tr_read(p + 1024), r2 = tr_read(p + 2048), r3 = tr_read(p + 3072);
      wof[pc * 8 + 2 * hl] = (u32x4){r0.x, r0.y, r1.x, r1.y};
      wof[pc * 8 + 2 * hl + 1] = (u32x4){r2.x, r2.y, r3.x, 0u};
    }
    SBAR();
  };
  auto pin_wo = [&](int pc) {
#pragma unroll
    for (int i = 0; i < 8; ++i) PIN(wof[pc * 8 + i]);
  };
  if (w == 0) VMCNT(2); else VMCNT(1);
  BAR();
  u32x4 pq[2];
  {
    f32x16 aq = {};
    u32x4 qf[2][4];
    auto read_q = [&](int cc) {
#pragma unroll
      for (int s = 0; s < 4; ++s) {
        LAS char* p = (QS(cc) >= WR ? whi + (QS(cc) - WR) : wlo + QS(cc)) + s * QD(cc);
        const u32x2 lo = tr_read(p), hi = tr_read(p + 256);
        qf[cc & 1][s] = (u32x4){lo.x, lo.y, hi.x, hi.y};
      }
      SBAR();
    };
    read_q(0);
#pragma unroll
    for (int cc = 0; cc < 7; ++cc) {
#pragma unroll
      for (int s = 0; s < 4; ++s) PIN(qf[cc & 1][s]);
      if (cc < 6) read_q(cc + 1);
#pragma unroll
      for (int s = 0; s < 4; ++s) {
        aq = mfma32(qf[cc & 1][s], xf[cc * 4 + s], aq);
        if (s == 0) issue_wo1(0, cc);
        SBAR();
      }
    }
#pragma unroll
    for (int s = 0; s < 2; ++s) {
#pragma unroll
      for (int j = 0; j < 4; ++j) pq[s][j] = phi2(aq[8 * s + 2 * j], aq[8 * s + 2 * j + 1]);
      PIN(pq[s]);
    }
  }
  STAMP(4);
  VMCNT(0);
  BAR();
  read_wo(0);
  issue_wo(1);
  pin_wo(0);
  STAMP(5);
  {
    u32x4 d[16];
#pragma nounroll
    for (int it = 0; it < SPIN_CAP; ++it) {
      asm volatile("" ::: "memory");
#pragma unroll
      for (int p = 0; p < 16; ++p) d[p] = LD_SC1(rs, WS_KVP + h * 65536 + p * 4096 + tid * 16);
      u32 m = 0;
#pragma unroll
      for (int p = 0; p < 16; ++p) m = sum4(m, d[p]);
      if (wave_sum(m) == 16u * tag) break;
    }
    f32x4 s4 = __builtin_bit_cast(f32x4, d[0]);
#pragma unroll
    for (int p = 1; p < 16; ++p) s4 += __builtin_bit_cast(f32x4, d[p]);
    const int dw = 8 * w + 4 * half - coff;
    u32x2 o2 = {pkbf(s4.x, s4.y), pkbf(s4.z, s4.w)};
    if (dw < 0 || dw >= 28) o2 = (u32x2){0u, 0u};
    *(LAS u32x2*)(lds + KVI + lane * 32 + w * 8) = o2;
  }
  STAMP(6);
  VMCNT(0);
  BAR();
  {
    const int i = (r32 & 3) + 4 * (r32 >> 3), hh = (r32 >> 2) & 1;
    const int ew = i < 14 ? 14 * hh + i + coff : wone;
    LAS char* p = lds + KVI + (ew + 32 * half) * 32;
    const u32x4 a0 = *(LAS u32x4*)p, a1 = *(LAS u32x4*)(p + 16);
    f32x16 nm = {};
    nm = mfma32(a0, pq[0], nm); nm = mfma32(a1, pq[1], nm);
    const float den = rbf(rbf(nm[14]) + 1e-6f);
    const float rc = __builtin_amdgcn_rcpf(den);
    u32 o[8];
#pragma unroll
    for (int j = 0; j < 7; ++j) { const u32 t = pkbf(nm[2 * j], nm[2 * j + 1]); o[j] = pkbf(bflo(t) * rc, bfhi(t) * rc); }
    o[7] = tag - ((o[0] + o[1] + o[2]) + (o[3] + o[4] + o[5]) + o[6]);
    ST_SC1(((u32x4){o[0], o[1], o[2], o[3]}), rs, ob_off);
    ST_SC1(((u32x4){o[4], o[5], o[6], o[7]}), rs, ob_off + 1024);
  }
  STAMP(7);
  read_wo(1);
  issue_wo(2);
  pin_wo(1);
  BAR();
  issue_wo(3);
  VMCNT(7); BAR();
  read_wo(2);
  STAMP(8);
  {
    u32x4 d[8];
#pragma nounroll
    for (int it = 0; it < SPIN_CAP; ++it) {
      asm volatile("" ::: "memory");
#pragma unroll
      for (int p = 0; p < 8; ++p) d[p] = LD_SC1(rs, WS_OB + ((rb * 4 + rt) * 16 + 4 * w) * 2048 + p * 1024 + lane * 16);
      u32 m = 0;
#pragma unroll
      for (int p = 0; p < 8; ++p) m = sum4(m, d[p]);
      if (__all(m == 4u * tag)) break;
    }
#pragma unroll
    for (int p = 1; p < 8; p += 2) d[p].w = 0u;
    STAMP(9);
    pin_wo(2);
    VMCNT(0);
    BAR();
#pragma unroll
    for (int p = 0; p < 8; ++p) *(LAS u32x4*)(lds + WOE + (8 * w + p) * 1024 + lane * 16) = d[p];
    read_wo(3);
    pin_wo(3);
    BAR();
  }
  if (ct < 3 || w < 2) {
    f32x16 acc = {};
#pragma unroll
    for (int k = 0; k < 32; ++k) acc = mfma32(wof[k], *(LAS u32x4*)(lds + WOE + k * 1024 + lane * 16), acc);
    const u32 dst = (u32)((128 * rb + 32 * rt + r32) * 1792 + 896 + ct * 256 + w * 64 + half * 32);
    ST_OUT(((u32x4){pkbf(acc[0], acc[1]), pkbf(acc[2], acc[3]), pkbf(acc[4], acc[5]), pkbf(acc[6], acc[7])}), dst);
    ST_OUT(((u32x4){pkbf(acc[8], acc[9]), pkbf(acc[10], acc[11]), pkbf(acc[12], acc[13]), pkbf(acc[14], acc[15])}), dst + 16);
  }
  STAMP(10);
  STAMP_FLUSH();
}

DI f32x2 rot(f32x2 b) { return (f32x2){-b.y, b.x}; }
DI f32x2 cm1(f32x2 a, f32x2 b) { return (f32x2){a.x, a.x} * b; }
DI f32x2 cm2(f32x2 a, f32x2 br, f32x2 t) { return __builtin_elementwise_fma((f32x2){a.y, a.y}, br, t); }
DI f32x2 cexp_p(float a2, float f, float p) {
  const float m = __builtin_amdgcn_exp2f(a2 * p), ph = __builtin_amdgcn_fractf(f * p);
  return (f32x2){m, m} * (f32x2){__builtin_amdgcn_cosf(ph), __builtin_amdgcn_sinf(ph)};
}
DI u32 wave_umax(u32 v) {
  u32 t;
  t = __builtin_amdgcn_update_dpp(0u, v, 0x111, 0xf, 0xf, false); v = v > t ? v : t;
  t = __builtin_amdgcn_update_dpp(0u, v, 0x112, 0xf, 0xf, false); v = v > t ? v : t;
  t = __builtin_amdgcn_update_dpp(0u, v, 0x114, 0xf, 0xf, false); v = v > t ? v : t;
  t = __builtin_amdgcn_update_dpp(0u, v, 0x118, 0xf, 0xf, false); v = v > t ? v : t;
  const u32 a = __builtin_amdgcn_readlane(v, 15), b = __builtin_amdgcn_readlane(v, 31), c = __builtin_amdgcn_readlane(v, 47), d = __builtin_amdgcn_readlane(v, 63);
  const u32 ab = a > b ? a : b, cd = c > d ? c : d;
  return ab > cd ? ab : cd;
}

DI void role_a(LAS char* lds, const char* __restrict__ u, const u32* __restrict__ logA, const u32* __restrict__ Aim,
               const u32* __restrict__ Cre, const u32* __restrict__ Cim, const u16* __restrict__ logdt,
               const char* __restrict__ Dsk, const char* __restrict__ wo, char* __restrict__ out, char* ws, u64 t_first) {
  const int a = blockIdx.x, cb = a >> 5, jp = a & 31, tid = threadIdx.x, lane = tid & 63;
  const int w = __builtin_amdgcn_readfirstlane(tid >> 6);
  STAMP_DECL;
  STAMP(0);
  st_[15] = st_[0]; st_[0] = t_first;
  const __amdgpu_buffer_rsrc_t rs = __builtin_amdgcn_make_buffer_rsrc(ws, 0, WS_END, 0x00020000);
  const int ch = w >> 1, hf = w & 1, d = 2 * a + ch;
  const int pi = d * 128 + hf * 64 + lane;
  u32x4 ub[4];
  if (w < 2) {
#pragma unroll
    for (int i = 0; i < 4; ++i) ub[i] = *(const u32x4*)(u + (u32)((64 * jp + 4 * (tid & 15) + i) * 896 + cb * 128 + (tid >> 4) * 16));
  }
  const u32 pA = logA[pi], pI = Aim[pi], pR = Cre[pi], pC = Cim[pi];
  const float ldt = bflo(logdt[d]);
  u32x4 us[2];
#pragma unroll
  for (int i = 0; i < 2; ++i) us[i] = *(const u32x4*)(u + (u32)((64 * jp + 32 * i + (tid >> 3)) * 896 + cb * 128 + (tid & 7) * 16));
  const u32x4 dk = *(const u32x4*)(Dsk + cb * 128 + (tid & 7) * 16);
  u32 dummy = 0;
  const u32 tag = load_tag(ws);
  int tix[8];
#pragma unroll
  for (int j = 0; j < 8; ++j) {
    const int p = (int)rbf((float)(2047 - 8 * tid - j));
    const int low = p & 7, blk = p >> 8;
    const int k4 = (blk < 4 ? blk : 4) * 4;
    const int sh = (0x32210 >> k4) & 15;
    const int base = blk + ((0xCBA70 >> k4) & 15);
    tix[j] = ((base + (low >> sh)) * 32 + ((p >> 3) & 31)) * 4;
    PIN(tix[j]);
  }
  if (w < 2) {
    u32x4 ck;
#pragma unroll
    for (int k = 0; k < 4; ++k) {
      const u32x4 v = {ub[0][k], ub[1][k], ub[2][k], ub[3][k]};
      ST_SC1(v, rs, WS_UX + (cb * 32 + 4 * (tid >> 4) + k) * 8192 + jp * 256 + (tid & 15) * 16);
      ck[k] = tag - row_sum(sum4(0u, v));
    }
    if ((tid & 15) == 15) ST_SC1(ck, rs, WS_UCK + a * 128 + (tid >> 4) * 16);
  }
  put_flag(ws, tag);
  STAMP(1);
  constexpr int SROW = 272, STG = 53 * SROW, TBL = 4 * STG, UC = 0, UCS = 4160, RV = 8 * UCS, RVS = 4608, EX = RV + 2 * RVS;
  const float dt = rbf(expf(ldt));
  const u32 mx = wave_umax(umax3(umax3(pR & 0x7fffu, (pR >> 16) & 0x7fffu, pC & 0x7fffu), (pC >> 16) & 0x7fffu, 0u));
  int ex = mx >> 7; ex = ex < 13 ? 13 : ex > 254 ? 254 : ex;
  const float sc = __uint_as_float((u32)(266 - ex) << 23), isc = __uint_as_float((u32)(ex - 12) << 23);
  f32x16 kacc = {};
  LAS char* stg = lds + w * STG;
  const int r32 = lane & 31, half = lane >> 5;
  LAS char* px = stg + r32 * SROW + half * 16;
  LAS char* py = stg + (32 + (r32 < 20 ? r32 : 20)) * SROW + half * 16;
  u32x4 fx[8], fy[8];
  u32x4 ud[3]; u32 uck;
  const __amdgpu_buffer_rsrc_t ru = __builtin_amdgcn_make_buffer_rsrc(ws + WS_UX + a * 8192, 0, 8192, 0x00020000);
  const int npc = w < 3 ? 9 : 8;
  auto load_u = [&]() {
    ud[0] = LD_SC1(ru, w * 2048 + lane * 16); ud[1] = LD_SC1(ru, w * 2048 + 1024 + lane * 16);
    ud[2] = LD_SC1(ru, lane < 16 ? w * 2048 + 2048 + lane * 16 : 8192);
    uck = LD1_SC1(rs, lane < npc ? WS_UCK + (cb * 32 + 8 * w + lane) * 128 + jp * 4 : WS_END);
  };
#pragma unroll
  for (int bt = 0; bt < 2; ++bt) {
    const float la = bt ? bfhi(pA) : bflo(pA), ai = bt ? bfhi(pI) : bflo(pI), cr = bt ? bfhi(pR) : bflo(pR), ci = bt ? bfhi(pC) : bflo(pC);
    const float a2 = -rbf(expf(la)) * dt * 1.4426950408889634f, f = ai * dt * 0.15915494309189535f;
    LAS u32* sw = (LAS u32*)(stg + lane * 4);
#define STEP(p0, b0, b0r, r0, p1, b1, b1r, r1) do { \
      const f32x2 t0_ = cm1(p0, b0), t1_ = cm1(p1, b1); const u32 c0_ = pkh(p0), c1_ = pkh(p1); SBAR(); \
      p0 = cm2(p0, b0r, t0_); p1 = cm2(p1, b1r, t1_); sw[(r0) * (SROW / 4)] = c0_; sw[(r1) * (SROW / 4)] = c1_; SBAR(); } while (0)
    const f32x2 z8 = cexp_p(a2, f, 8.f), y1 = cexp_p(a2, -f, 1.f), yq = cexp_p(a2, -f, 256.f);
    const f32x2 z8r = rot(z8), y1r = rot(y1), yqr = rot(yq);
    f32x2 x0 = (f32x2){cr, ci} * sc;
    f32x2 x1 = cm2(x0, z8r, cm1(x0, z8));
    const f32x2 z16 = cm2(z8, z8r, cm1(z8, z8)), z16r = rot(z16);
    SBAR();
#pragma unroll
    for (int r = 0; r < 30; r += 2) {
      STEP(x0, z16, z16r, r, x1, z16, z16r, r + 1);
      if (bt == 1 && r < 16) { kacc = mfma32h(fy[r >> 1], fx[r >> 1], kacc); SBAR(); }
    }
    sw[30 * (SROW / 4)] = pkh(x0); sw[31 * (SROW / 4)] = pkh(x1);
    if (bt == 1) { STAMP(2); load_u(); SBAR(); }
    {
      f32x2 p = y1, q = yq, y2, y4, y6, v;
      sw[32 * (SROW / 4)] = 0x00003C00u;
      STEP(p, y1, y1r, 33, q, yq, yqr, 40); y2 = p;
      STEP(p, y1, y1r, 34, q, yq, yqr, 44);
      STEP(p, y1, y1r, 35, q, yq, yqr, 46); y4 = p;
      STEP(p, y1, y1r, 36, q, yq, yqr, 48);
      STEP(p, y1, y1r, 37, q, yq, yqr, 49); y6 = p;
      STEP(p, y1, y1r, 38, q, yq, yqr, 50);
      v = cm2(y4, yqr, cm1(y4, yq));
      sw[39 * (SROW / 4)] = pkh(p);
      STEP(v, yq, yqr, 42, q, yq, yqr, 51);
      y2 = cm2(y2, yqr, cm1(y2, yq)); y6 = cm2(y6, yqr, cm1(y6, yq));
      sw[52 * (SROW / 4)] = pkh(q); sw[45 * (SROW / 4)] = pkh(v);
      v = cm2(v, yqr, cm1(v, yq));
      sw[41 * (SROW / 4)] = pkh(y2); sw[43 * (SROW / 4)] = pkh(y6); sw[47 * (SROW / 4)] = pkh(v);
    }
#undef STEP
#pragma unroll
    for (int s = 0; s < 8; ++s) { fy[s] = *(LAS u32x4*)(py + 32 * s); fx[s] = *(LAS u32x4*)(px + 32 * s); }
    SBAR();
  }
  STAMP(3);
#pragma unroll
  for (int s = 0; s < 8; ++s) kacc = mfma32h(fy[s], fx[s], kacc);
  u32 sk[8];
#pragma unroll
  for (int i = 0; i < 2; ++i)
#pragma unroll
    for (int k = 0; k < 4; ++k) { sk[4 * i + k] = pkbf(bflo(us[i][k]) * bflo(dk[k]), bfhi(us[i][k]) * bfhi(dk[k])); PIN(sk[4 * i + k]); }
  {
    const float fac = 2.f * dt * isc;
#pragma unroll
    for (int i = 0; i < 12; ++i) *(LAS float*)(lds + TBL + w * 3072 + (((i & 3) + 8 * (i >> 2) + 4 * half) * 32 + r32) * 4) = kacc[i] * fac;
  }
  STAMP(4);
  BAR();
#pragma unroll
  for (int c = 0; c < 2; ++c) {
    float kk[8];
#pragma unroll
    for (int j = 0; j < 8; ++j) kk[j] = *(LAS float*)(lds + TBL + c * 6144 + tix[j]) + *(LAS float*)(lds + TBL + c * 6144 + 3072 + tix[j]);
    *(LAS u32x4*)(lds + RV + c * RVS + tid * 16) = (u32x4){pkbf(kk[0], kk[1]), pkbf(kk[2], kk[3]), pkbf(kk[4], kk[5]), pkbf(kk[6], kk[7])};
  }
  if (tid < 64) *(LAS u32x4*)(lds + RV + half * RVS + 4096 + r32 * 16) = (u32x4){0u, 0u, 0u, 0u};
  SBAR();
#pragma nounroll
  for (int it = 0; it < SPIN_CAP; ++it) {
    if (wave_sum(sum4(sum4(sum4(uck, ud[0]), ud[1]), ud[2])) == npc * tag) break;
    asm volatile("" ::: "memory");
    load_u();
  }
  asm volatile("" :: "v"(dummy));
#pragma unroll
  for (int i = 0; i < 2; ++i) {
    const u32 nx = __builtin_amdgcn_update_dpp(__builtin_amdgcn_readlane(ud[i + 1].x, 0), ud[i].x, 0x130, 0xf, 0xf, false);
    const u32 dw[5] = {ud[i].x, ud[i].y, ud[i].z, ud[i].w, nx};
    LAS char* p = lds + UC + (128 * w + 64 * i + lane + 4) * 8;
#pragma unroll
    for (int c = 0; c < 2; ++c) {
      u32 pr[4];
#pragma unroll
      for (int k = 0; k < 4; ++k) pr[k] = __builtin_amdgcn_perm(dw[k + 1], dw[k], c ? 0x07060302u : 0x05040100u);
      *(LAS u32x2*)(p + (4 * c) * UCS) = (u32x2){pr[0], pr[2]};
      *(LAS u32x2*)(p + (4 * c + 1) * UCS) = (u32x2){pr[1], pr[3]};
      *(LAS u32*)(p + (4 * c + 2) * UCS - 4) = pr[0]; *(LAS u32*)(p + (4 * c + 2) * UCS) = pr[2];
      *(LAS u32*)(p + (4 * c + 3) * UCS - 4) = pr[1]; *(LAS u32*)(p + (4 * c + 3) * UCS) = pr[3];
    }
  }
  if (w == 0) {
    const u32 u0 = __builtin_amdgcn_readlane(ud[0].x, 0);
    const int cs = lane >> 3, ix = lane & 7, last = (cs & 2) ? 6 : 7;
    const u32 v = ((cs & 1) && ix == last) ? (cs & 4 ? u0 & 0xffff0000u : u0 << 16) : 0u;
    if (ix <= last) *(LAS u32*)(lds + UC + cs * UCS + ix * 4) = v;
  }
  BAR();
  STAMP(5);
  {
    const int c16 = lane & 15, hq = lane >> 4;
    LAS char* pa = lds + RV + ch * RVS + 2 * (2032 - 256 * 7 + 128 * hf - 16 * c16 + 8 * hq);
    LAS char* pb = lds + UC + (4 * ch + ((c16 + 1) & 3)) * UCS + 2 * (128 * hf + 8 * hq + 4 * ((c16 + 1) >> 2));
    f32x4 acc[8] = {};
    u32x4 fa[2][8], fb[2][8];
    auto ldf = [&](int rho, int m) {
      fa[rho & 1][m] = *(LAS u32x4*)(pa + 64 * rho + 512 * (7 - m));
      const u32x2 lo = *(volatile LAS u32x2*)(pb + 64 * rho + 512 * m), hi = *(volatile LAS u32x2*)(pb + 64 * rho + 512 * m + 8);
      fb[rho & 1][m] = (u32x4){lo.x, lo.y, hi.x, hi.y};
    };
#pragma unroll
    for (int m = 0; m < 8; ++m) ldf(0, m);
#pragma unroll
    for (int rho = 0; rho < 4; ++rho) {
      SBAR();
#pragma unroll
      for (int g = 0; g < 8; ++g) {
#pragma unroll
        for (int aa = 0; aa <= g; ++aa) {
          if (aa + g < 8) acc[aa + g] = mfma16(fa[rho & 1][g], fb[rho & 1][aa], acc[aa + g]);
          if (aa < g && aa + g < 8) acc[aa + g] = mfma16(fa[rho & 1][aa], fb[rho & 1][g], acc[aa + g]);
        }
        if (rho < 3) { ldf(rho + 1, g); SBAR(); }
      }
    }
    STAMP(6);
#pragma unroll
    for (int t = 0; t < 8; ++t) *(LAS f32x4*)(lds + EX + w * 8192 + t * 1024 + lane * 16) = acc[t];
    BAR();
#pragma unroll
    for (int i = 0; i < 2; ++i) {
      LAS char* p = lds + EX + (2 * w + i) * 1024 + lane * 16;
      const f32x4 y0 = *(LAS f32x4*)p + *(LAS f32x4*)(p + 8192), y1 = *(LAS f32x4*)(p + 16384) + *(LAS f32x4*)(p + 24576);
      const u32x4 v = {pkbf(y0.x, y1.x), pkbf(y0.y, y1.y), pkbf(y0.z, y1.z), pkbf(y0.w, y1.w)};
      ST_SC1(v, rs, WS_YX + (cb * 32 + 4 * (2 * w + i) + hq) * 8192 + jp * 256 + c16 * 16);
      const u32 ck = tag - row_sum(sum4(0u, v));
      if (c16 == 15) ST1_SC1(ck, rs, WS_YCK + a * 128 + (4 * (2 * w + i) + hq) * 4);
    }
  }
  STAMP(7);
  {
    u32x4 y[2];
    __builtin_amdgcn_s_sleep(8);
#pragma nounroll
    for (int it = 0; it < SPIN_CAP; ++it) {
      asm volatile("" ::: "memory");
      y[0] = LD_SC1(rs, WS_YX + a * 8192 + tid * 16); y[1] = LD_SC1(rs, WS_YX + a * 8192 + 4096 + tid * 16);
      const u32 ck = LD1_SC1(rs, lane < 8 ? WS_YCK + (cb * 32 + 16 * (lane >> 2) + 4 * w + (lane & 3)) * 128 + jp * 4 : WS_END);
      if (wave_sum(sum4(sum4(ck, y[0]), y[1])) == 8u * tag) break;
      __builtin_amdgcn_s_sleep(2);
    }
    STAMP(8);
#pragma unroll
    for (int i = 0; i < 2; ++i)
#pragma unroll
      for (int g = 0; g < 4; ++g) *(LAS u32*)(lds + (16 * g + (tid & 15)) * 144 + (16 * i + (tid >> 4)) * 4) = y[i][g];
    BAR();
#pragma unroll
    for (int i = 0; i < 2; ++i) {
      const u32x4 v = *(LAS u32x4*)(lds + (32 * i + (tid >> 3)) * 144 + (tid & 7) * 16);
      u32x4 o;
#pragma unroll
      for (int k = 0; k < 4; ++k) o[k] = pkbf(bflo(v[k]) + bflo(sk[4 * i + k]), bfhi(v[k]) + bfhi(sk[4 * i + k]));
      ST_OUT(o, (u32)((64 * jp + 32 * i + (tid >> 3)) * 1792 + cb * 128 + (tid & 7) * 16));
    }
  }
  STAMP(9);
  STAMP_FLUSH();
}

__global__ void __launch_bounds__(256, 2)
fused_kernel(const char* __restrict__ u, const u32* __restrict__ logA, const u32* __restrict__ Aim, const u32* __restrict__ Cre,
             const u32* __restrict__ Cim, const u16* __restrict__ logdt, const char* __restrict__ Dsk, const char* __restrict__ x,
             const char* __restrict__ wqkv, const char* __restrict__ wo, char* __restrict__ out, char* ws) {
  __shared__ __attribute__((aligned(16))) char smem[81920];
  LAS char* lds = (LAS char*)smem;
  u64 t_first = 0;
  asm volatile("s_memrealtime %0" : "=s"(t_first));
  asm volatile("" :: "s"(u), "s"(logA), "s"(Aim), "s"(Cre), "s"(Cim), "s"(logdt), "s"(Dsk), "s"(x), "s"(wqkv), "s"(wo), "s"(out), "s"(ws));
  if (blockIdx.x >= 256) role_b(lds, x, wqkv, wo, out, ws, t_first);
  else if (blockIdx.x < 224) role_a(lds, u, logA, Aim, Cre, Cim, logdt, Dsk, wo, out, ws, t_first);
  else {
    const int t = threadIdx.x;
    if (blockIdx.x == 224) {
      const u32 tag = load_tag(ws);
      __builtin_amdgcn_s_sleep(127);
      wait_flags(ws, 256 + t, flag_of(tag));
      wait_flags(ws, t < 224 ? t : 0, flag_of(tag));
      __syncthreads();
      if (t == 0) __hip_atomic_store((GAS u32*)ws, tag, RLX_AGENT);
      wait_flags(ws + 64, 256 + t, flag_of(tag));
      wait_flags(ws + 64, t < 224 ? t : 0, flag_of(tag));
      __syncthreads();
      u64 mn = ~0ull, mxv = 0;
      for (int i = 0; i < 8; ++i) {
        const int wv = t + 256 * i, blk = wv >> 2;
        if (blk >= 224 && blk < 256) continue;
        const GAS u64* p = (const GAS u64*)(ws + WS_DBG) + wv * 16;
        const u64 s0 = __hip_atomic_load(p, RLX_AGENT), sk = __hip_atomic_load(p + (READOUT & 15), RLX_AGENT);
        mn = s0 < mn ? s0 : mn;
        if ((READOUT >> 4) == 2 || ((READOUT >> 4) == 1) == (blk >= 256)) mxv = sk > mxv ? sk : mxv;
      }
      LAS u64* l = (LAS u64*)lds;
      l[t] = mn; l[256 + t] = mxv;
      __syncthreads();
      for (int i = 0; i < 256; ++i) { mn = l[i] < mn ? l[i] : mn; mxv = l[256 + i] > mxv ? l[256 + i] : mxv; }
      const u64 until = mn + SPINT + ((READOUT >> 4) == 3 ? 0 : mxv - mn);
      for (int it = 0; it < 1000000 && __builtin_amdgcn_s_memrealtime() < until; ++it) __builtin_amdgcn_s_sleep(1);
    }
  }
}

__global__ void tiny_kernel() {}
__global__ void tiny2_kernel() { __builtin_amdgcn_s_sleep(1); }
extern "C" void kernel_launch(void* const* d_in, const int* in_sizes, int n_in, void* d_out, int out_size, void* d_ws,
                              size_t ws_size, hipStream_t stream) {
  fused_kernel<<<512, 256, 0, stream>>>((const char*)d_in[0], (const u32*)d_in[1], (const u32*)d_in[2], (const u32*)d_in[3],
                                        (const u32*)d_in[4], (const u16*)d_in[5], (const char*)d_in[6], (const char*)d_in[7],
                                        (const char*)d_in[8], (const char*)d_in[9], (char*)d_out, (char*)d_ws);
}
```

```cpp
#include <hip/hip_runtime.h>
#include <stdint.h>

typedef unsigned short u16;
typedef unsigned u32;
typedef unsigned long long u64;
typedef short s16x4 __attribute__((ext_vector_type(4)));
typedef __bf16 bf16x8 __attribute__((ext_vector_type(8)));
typedef __bf16 bf16x2 __attribute__((ext_vector_type(2)));
typedef _Float16 f16x8 __attribute__((ext_vector_type(8)));
typedef _Float16 f16x2 __attribute__((ext_vector_type(2)));
typedef float f32x16 __attribute__((ext_vector_type(16)));
typedef float f32x4 __attribute__((ext_vector_type(4)));
typedef float f32x2 __attribute__((ext_vector_type(2)));
typedef u32 u32x4 __attribute__((ext_vector_type(4)));
typedef u32 u32x2 __attribute__((ext_vector_type(2)));
#define LAS __attribute__((address_space(3)))
#define GAS __attribute__((address_space(1)))
#define DI __device__ __forceinline__
#if defined(__HIP_DEVICE_COMPILE__)
#define NOPK __attribute__((target("no-packed-fp32-ops")))
#else
#define NOPK
#endif
#define RLX_AGENT __ATOMIC_RELAXED, __HIP_MEMORY_SCOPE_AGENT

#define BMAP 1
#define PREFETCH 1
#define CODE_BEFORE 0
#define CODE_LINES 200
#define QEARLY 3
#define GATH_AT 18
#define WO_EARLY 12
#define PUB_FIRST 0
#define UPOLL_AT 4
#define ROT 1
#define APRIO 3
#define ATAIL 3
#define BPRIO 2

constexpr u32 WS_FLAG = 4096;
constexpr u32 WS_MB1 = 73728;
constexpr u32 WS_MB2 = WS_MB1 + 32768;
constexpr u32 WS_MBU = WS_MB2 + 32768;
constexpr u32 WS_MBY = WS_MBU + 57344;
constexpr u32 WS_KVP = 1u << 18;
constexpr u32 WS_OB = WS_KVP + (1u << 20);
constexpr u32 WS_UX = WS_OB + (2u << 20);
constexpr u32 WS_YX = WS_UX + (2u << 20);
constexpr u32 WS_WQ = WS_YX + (2u << 20);
constexpr u32 WS_WO = WS_WQ + (1u << 19);
constexpr u32 WS_END = WS_WO + (1u << 19);
constexpr u32 WS_DBG = WS_END;
constexpr int SPIN_CAP = 1 << 15;

#define STAMP(i) do {} while (0)
#define STAMP_DECL
#define COUNT(i) do {} while (0)
#define STAMP_FLUSH() do {} while (0)

DI float bflo(u32 v) { return __uint_as_float(v << 16); }
DI float bfhi(u32 v) { return __uint_as_float(v & 0xffff0000u); }
DI u32 pkbf(float a, float b) { f32x2 p = {a, b}; return __builtin_bit_cast(u32, __builtin_convertvector(p, bf16x2)); }
DI u32 pkh(f32x2 p) { return __builtin_bit_cast(u32, __builtin_convertvector(p, f16x2)); }
DI float rbf(float a) { return bflo(pkbf(a, a)); }
DI u32 umax3(u32 a, u32 b, u32 c) { u32 m = a > b ? a : b; return m > c ? m : c; }
DI u32 sum4(u32 m, u32x4 v) { return (m + v.x + v.y) + v.z + v.w; }
DI u32 row_sum(u32 v) {
  v += __builtin_amdgcn_update_dpp(0u, v, 0x111, 0xf, 0xf, true); v += __builtin_amdgcn_update_dpp(0u, v, 0x112, 0xf, 0xf, true);
  v += __builtin_amdgcn_update_dpp(0u, v, 0x114, 0xf, 0xf, true); v += __builtin_amdgcn_update_dpp(0u, v, 0x118, 0xf, 0xf, true);
  return v;
}
DI u32 wave_sum(u32 v) {
  v = row_sum(v);
  return (__builtin_amdgcn_readlane(v, 15) + __builtin_amdgcn_readlane(v, 31)) + (__builtin_amdgcn_readlane(v, 47) + __builtin_amdgcn_readlane(v, 63));
}
DI u32 request_epoch(const char* ws) { u32 e; asm volatile("s_load_dword %0, %1, 0x0" : "=s"(e) : "s"(ws) : "memory"); return e; }
DI u32 take_tag(u32 e) { u32 r; asm volatile("s_waitcnt lgkmcnt(0)\n\ts_and_b32 %0, %1, -2" : "=s"(r) : "s"(e) : "memory", "scc"); return r * 1664525u + 2027808447u; }
DI void put_flag(char* ws, u32 tag) { if (threadIdx.x == 0) __hip_atomic_store((GAS u32*)(ws + WS_FLAG) + blockIdx.x * 32, tag, RLX_AGENT); }
#define PSCOPE __HIP_MEMORY_SCOPE_AGENT
DI u32 poll32(char* ws, u32 off, bool on) { u32 z = 0, r = 0; asm volatile("" : "+v"(z)); if (on) r = __hip_atomic_fetch_add((GAS u32*)(ws + off), z, __ATOMIC_RELAXED, PSCOPE); return r; }
DI u64 poll64(char* ws, u32 off, bool on) { u64 z = 0, r = 0; asm volatile("" : "+v"(z)); if (on) r = __hip_atomic_fetch_add((GAS u64*)(ws + off), z, __ATOMIC_RELAXED, PSCOPE); return r; }
#define SBAR() __builtin_amdgcn_sched_barrier(0)
#define PRIO(n) do { SBAR(); __builtin_amdgcn_s_setprio(n); SBAR(); } while (0)
#define PIN(x) asm volatile("" : "+v"(x))
DI f32x16 mfma32(u32x4 a, u32x4 b, f32x16 c) { return __builtin_amdgcn_mfma_f32_32x32x16_bf16(__builtin_bit_cast(bf16x8, a), __builtin_bit_cast(bf16x8, b), c, 0, 0, 0); }
DI f32x16 mfma32h(u32x4 a, u32x4 b, f32x16 c) { return __builtin_amdgcn_mfma_f32_32x32x16_f16(__builtin_bit_cast(f16x8, a), __builtin_bit_cast(f16x8, b), c, 0, 0, 0); }
DI f32x4 mfma16h(u32x4 a, u32x4 b, f32x4 c) { return __builtin_amdgcn_mfma_f32_16x16x32_f16(__builtin_bit_cast(f16x8, a), __builtin_bit_cast(f16x8, b), c, 0, 0, 0); }
DI f32x4 mfma16(u32x4 a, u32x4 b, f32x4 c) { return __builtin_amdgcn_mfma_f32_16x16x32_bf16(__builtin_bit_cast(bf16x8, a), __builtin_bit_cast(bf16x8, b), c, 0, 0, 0); }
DI u32x2 tr_read(LAS char* p) { return __builtin_bit_cast(u32x2, __builtin_amdgcn_ds_read_tr16_b64_v4i16((LAS s16x4*)p)); }
#define RSRC(p, n) __builtin_amdgcn_make_buffer_rsrc((void*)(p), 0, (n), 0x00020000)
#define DMA16(rs, voff, soff, dst, aux) __builtin_amdgcn_raw_ptr_buffer_load_lds((rs), (LAS void*)(dst), 16, (voff), (soff), 0, (aux))
#define WAITVM(n) do { asm volatile("" ::: "memory"); __builtin_amdgcn_s_waitcnt(0x0F70 | ((n) & 15) | (((n) >> 4) << 14)); asm volatile("" ::: "memory"); } while (0)
#define VMCNT(n) asm volatile("s_waitcnt vmcnt(" #n ")" ::: "memory")
#define LGKM0() asm volatile("s_waitcnt lgkmcnt(0)" ::: "memory")
#define BAR() do { asm volatile("s_waitcnt lgkmcnt(0)" ::: "memory"); __builtin_amdgcn_s_barrier(); asm volatile("" ::: "memory"); } while (0)
#define ST_SC1(v, rs, off) __builtin_amdgcn_raw_buffer_store_b128((v), (rs), (off), 0, 16)
#define LD_SC1(rs, off) __builtin_amdgcn_raw_buffer_load_b128((rs), (off), 0, 16)
#define LDS_SC1(rs, off, soff) __builtin_amdgcn_raw_buffer_load_b128((rs), (off), (soff), 16)
#define ST1_SC1(v, rs, off) __builtin_amdgcn_raw_buffer_store_b32((v), (rs), (off), 0, 16)
#define LD1_SC1(rs, off) __builtin_amdgcn_raw_buffer_load_b32((rs), (off), 0, 16)

typedef unsigned short u16x2 __attribute__((ext_vector_type(2)));
DI u32 phi1(float a, float b) {
  const u32 t = pkbf(a, b);
  const f32x2 x = {bflo(t), bfhi(t)};
  const f32x2 y = x * 1.4426950408889634f;
  const float e0 = __builtin_amdgcn_fmed3f(__builtin_amdgcn_exp2f(y.x), 0.f, 1.f);
  const float e1 = __builtin_amdgcn_fmed3f(__builtin_amdgcn_exp2f(y.y), 0.f, 1.f);
  const f32x2 x1 = x + 1.0f;
  return pkbf(__builtin_fmaxf(x1.x, e0), __builtin_fmaxf(x1.y, e1));
}
DI void phi_eps(u32x4 (&p)[2]) {
  u16x2 m = {0xffff, 0xffff};
#pragma unroll
  for (int j = 0; j < 8; ++j) { const u32 e = p[j >> 2][j & 3]; m = __builtin_elementwise_min(m, __builtin_bit_cast(u16x2, e)); }
  if (__any(m.x < 0x3A80 || m.y < 0x3A80)) {
#pragma unroll
    for (int j = 0; j < 8; ++j) { const u32 r = p[j >> 2][j & 3]; p[j >> 2][j & 3] = pkbf(bflo(r) + 1e-6f, bfhi(r) + 1e-6f); }
  }
}

DI void wait_flags(const char* ws, int idx, u32 tag) {
  const GAS u32* f = (const GAS u32*)(ws + WS_FLAG) + idx * 32;
#pragma nounroll
  for (int it = 0; it < SPIN_CAP; ++it) {
    const u32 v = __hip_atomic_load(f, RLX_AGENT);
    if (__all(v == tag)) break;
    __builtin_amdgcn_s_sleep(16);
  }
}

DI void role_b(LAS char* lds, const char* __restrict__ x, const char* __restrict__ wqkv, char* __restrict__ out, char* ws, u32 epoch) {
  const int b = blockIdx.x - 256, tid = threadIdx.x, lane = tid & 63;
  const int w = __builtin_amdgcn_readfirstlane(tid >> 6);
  STAMP_DECL;
  STAMP(0);
  const int rb = 4 * ((b & 7) >> 1) + (b >> 6), h = (b & 1) * 8 + ((b >> 3) & 7);
  const int start = (int)((0x1065432106543210ull >> (4 * ((h & 7) + 2 * (rb & 3)))) & 7);
  const __amdgpu_buffer_rsrc_t rs = RSRC(ws, WS_END), rx = RSRC(x, 2048 * 896), rw = RSRC(wqkv, 448 * 2688);
  constexpr int WR = 49152, KVI = WR + 4096, OST = 16384, OTR = WR;
#define XS(i) ((i) * 16384)
#define QS(cc) ((cc) < 2 ? WR + 24576 + (cc) * 4096 : (cc) < 6 ? XS(1) + ((cc) - 2) * 1024 : WR)
#define QD(cc) ((cc) >= 2 && (cc) < 6 ? 4096 : 1024)
  const int wsb = (56 * h) & ~15;
  const int coff = (h & 1) * 4;
  const int wone = (h & 1) ? 0 : 28;
  const u32 xv0 = (128 * rb + 32 * w + (lane >> 3)) * 896 + (((lane & 7) ^ (lane >> 4)) << 4);
  const u32 xv1 = (xv0 ^ 64) + 8 * 896;
  const u32 wv = (16 * w + (lane >> 2)) * 2688 + wsb + (lane & 3) * 16;
  const u32 l16 = lane * 16;
  int ch_[7];
  auto chunk = [&](int cc) { return ch_[cc]; };
  auto next_chunk = [&](int cc) { int c = cc == 0 ? start : ch_[cc - 1] == 6 ? 0 : ch_[cc - 1] + 1; asm volatile("" : "+s"(c)); ch_[cc] = c; };
  auto issue_x = [&](int cc, int i) { DMA16(rx, (i & 1) ? xv1 : xv0, chunk(cc) * 128 + (i >> 1) * (16 * 896), lds + XS(cc % 3) + w * 4096 + i * 1024, 0); };
  auto issue_w = [&](int cc, int i) { DMA16(rw, wv, chunk(cc) * (64 * 2688) + 896 * (i + 1), lds + WR + (cc & 3) * 8192 + i * 4096 + w * 1024, 0); };
  auto issue_q = [&](int cc) { DMA16(rs, l16, WS_WQ + (h * 28 + 4 * chunk(cc) + w) * 1024, lds + QS(cc) + w * QD(cc), 16); };
#pragma unroll
  for (int cc = 0; cc < 2; ++cc) {
    next_chunk(cc);
    issue_w(cc, 0); issue_w(cc, 1); issue_x(cc, 0); issue_x(cc, 1); issue_x(cc, 2); issue_x(cc, 3);
    SBAR();
  }
  next_chunk(2); next_chunk(3); next_chunk(4); next_chunk(5); next_chunk(6);
  __builtin_amdgcn_s_setprio(BPRIO);
  const int r32 = lane & 31, half = lane >> 5;
  const int xrd = w * 4096 + r32 * 128 + ((half ^ ((r32 >> 1) & 7)) << 4);
  LAS char* whi = lds + WR + (8 * half + ((lane >> 2) & 3)) * 64 + ((lane >> 4) & 1) * 32 + (lane & 3) * 8;
  PIN(whi);
  u32x4 xf[28], wf[2][8];
  f32x16 ak = {}, av = {};
  auto rd = [&](int cc, int i) {
    if ((i & 1) == 0) xf[cc * 4 + (i >> 1)] = *(LAS u32x4*)(lds + XS(cc % 3) + (xrd ^ ((i >> 1) << 5)));
    LAS char* p = whi + (cc & 3) * 8192 + (i >> 1) * 1024 + (i & 1) * 4096;
    const u32x2 lo = tr_read(p), hi = tr_read(p + 256);
    wf[cc & 1][i] = (u32x4){lo.x, lo.y, hi.x, hi.y};
  };
#pragma unroll
  for (int cc = 0; cc < 8; ++cc) {
    if (cc < 7) {
      if (cc == 0) VMCNT(6); else if (cc < 5) VMCNT(8); else if (cc == 5) VMCNT(6); else WAITVM(QEARLY);
      BAR();
      if (cc == 0) { STAMP(1); }
      if (cc >= 5) {
#pragma unroll
        for (int i = 0; i < 8; ++i) rd(cc, i);
        SBAR();
      }
#pragma unroll
      for (int i = 0; i < 8; ++i) {
        if (cc > 0) {
          if (i & 1) av = mfma32(xf[(cc - 1) * 4 + (i >> 1)], wf[(cc - 1) & 1][i], av);
          else ak = mfma32(xf[(cc - 1) * 4 + (i >> 1)], wf[(cc - 1) & 1][i], ak);
        }
        if (cc < 5) rd(cc, i);
        if (cc == 0) { if (i < 2) issue_w(2, i); else if (i < 6) issue_x(2, i - 2); else issue_w(3, i - 6); }
        else if (cc <= 4 && i < 4) issue_x(cc + 2, i);
        else if (cc <= 3 && i < 6) issue_w(cc + 3, i - 4);
        else if (cc == 5 && i < QEARLY) issue_q(i);
        else if (cc == 6 && i < 7 - QEARLY) issue_q(i + QEARLY);
        SBAR();
      }
#pragma unroll
      for (int s = 3; s >= 0; --s) { PIN(wf[cc & 1][2 * s + 1]); PIN(wf[cc & 1][2 * s]); PIN(xf[cc * 4 + s]); }
      if (cc > 0) { PIN(ak); PIN(av); }
    } else {
#pragma unroll
      for (int s = 0; s < 4; ++s) ak = mfma32(xf[24 + s], wf[0][2 * s], ak);
#pragma unroll
      for (int s = 0; s < 4; ++s) av = mfma32(xf[24 + s], wf[0][2 * s + 1], av);
      SBAR();
    }
  }
  STAMP(2);
  const u32 tag = take_tag(epoch);
  const u32 mb1 = WS_MB1 + (h * 16 + rb) * 128 + ((lane + rb) & 15) * 4;
  u32 mail = poll32(ws, mb1, lane < 16);
  const int rt = h & 3, ct = h >> 2, nt = 4 * ct + w < 14 ? 4 * ct + w : 13;
  const int hrot = rt + 4 * (rb & 3);
#define WO_SRC(k) (WS_WO + nt * 32768 + (((k) + 2 * hrot) & 31) * 1024)
  u32x4 wof[32], d[16];
  auto load_wo = [&](int k0, int k1) {
#pragma unroll
    for (int k = k0; k < k1; ++k) wof[k] = LDS_SC1(rs, l16, WO_SRC(k));
    SBAR();
  };
  {
    u32x4 pk[2], pv[2];
#pragma unroll
    for (int j = 0; j < 8; ++j) {
      pk[j >> 2][j & 3] = phi1(ak[2 * j], ak[2 * j + 1]);
      if (j & 1) { PIN(pk[j >> 2][j & 3]); load_wo((j >> 1) * (WO_EARLY / 4), ((j >> 1) + 1) * (WO_EARLY / 4)); }
    }
    phi_eps(pk);
#pragma unroll
    for (int s = 0; s < 2; ++s)
#pragma unroll
      for (int j = 0; j < 4; ++j) {
        const u32 v = pkbf(av[8 * s + 2 * j], av[8 * s + 2 * j + 1]);
        pv[s][j] = r32 == wone ? 0x3F803F80u : v;
      }
    PIN(pk[0]); PIN(pk[1]); PIN(pv[0]); PIN(pv[1]);
    ST1_SC1(tag, rs, lane < 4 ? WS_MB1 + (h * 16 + 4 * w + lane) * 128 + rb * 4 : tid == 4 ? WS_FLAG + blockIdx.x * 128 : WS_END);
    SBAR();
    f32x16 kv = {};
    kv = mfma32(pk[0], pv[0], kv); kv = mfma32(pk[1], pv[1], kv);
#pragma unroll
    for (int g = 0; g < 4; ++g) *(LAS f32x4*)(lds + XS(2) + w * 4096 + g * 1024 + l16) = (f32x4){kv[4 * g], kv[4 * g + 1], kv[4 * g + 2], kv[4 * g + 3]};
    BAR();
    f32x4 s4 = *(LAS f32x4*)(lds + XS(2) + tid * 16);
#pragma unroll
    for (int ww = 1; ww < 4; ++ww) s4 += *(LAS f32x4*)(lds + XS(2) + ww * 4096 + tid * 16);
    u32x4 v = __builtin_bit_cast(u32x4, s4);
    const bool ck = lane == wone + 1;
    if (ck) v = (u32x4){0u, 0u, 0u, 0u};
    const u32 c = tag - wave_sum(sum4(0u, v));
    if (ck) v.x = c;
    ST_SC1(v, rs, WS_KVP + (h * 16 + rb) * 4096 + tid * 16);
    d[0] = v;
  }
  WAITVM(WO_EARLY + 2);
  u32 go = (u32)__ballot(mail == tag) & 0xfffeu, need = 0xfffeu & ~go;
  mail = poll32(ws, mb1, lane < 16 && need != 0);
  SBAR();
  STAMP(3);
  LAS char* qlo = lds + l16; LAS char* qhi = lds + WR + l16;
  PIN(qhi);
#define QP(cc) (QS(cc) >= WR ? qhi + (QS(cc) - WR) : qlo + QS(cc))
#pragma nounroll
  for (int it = 0; it < SPIN_CAP; ++it) {
    u32 m = 0;
#pragma unroll
    for (int cc = 0; cc < 7; ++cc) m = sum4(m, *(LAS u32x4*)(QP(cc) + w * QD(cc)));
    if (wave_sum(m) == 7u * tag) break;
#pragma unroll
    for (int cc = 0; cc < 7; ++cc) issue_q(cc);
    COUNT(15);
    WAITVM(0);
  }
  BAR();
  STAMP(4);
  u32x4 pq[2];
  const u32 t16 = tid * 16;
  auto load_kv = [&](int p, bool on = true) { d[p] = LDS_SC1(rs, t16, on ? WS_KVP + h * 65536 + ((p + rb) & 15) * 4096 : WS_END); };
  {
    f32x16 aq = {};
    u32x4 qf[2][4];
    auto read_q = [&](int cc) {
#pragma unroll
      for (int s = 0; s < 4; ++s) qf[cc & 1][s] = *(LAS u32x4*)(QP(cc) + s * QD(cc));
      SBAR();
    };
    read_q(0);
#pragma unroll
    for (int cc = 0; cc < 7; ++cc) {
      if (cc < 6) read_q(cc + 1);
#pragma unroll
      for (int s = 0; s < 4; ++s) {
        aq = mfma32(qf[cc & 1][s], xf[cc * 4 + s], aq);
        const int i = 4 * cc + s;
        if (i < 15) load_kv(i + 1, go >> (i + 1) & 1);
        if (i == GATH_AT) { go = need & (u32)__ballot(mail == tag); need &= ~go; mail = poll32(ws, mb1, lane < 16 && need != 0); }
        if (i >= GATH_AT) {
#pragma unroll
          for (int p = 1 + 2 * (i - GATH_AT); p < 3 + 2 * (i - GATH_AT) && p < 16; ++p) if (go >> p & 1) load_kv(p);
        }
        PIN(aq);
        SBAR();
      }
    }
#pragma unroll
    for (int j = 0; j < 8; ++j) pq[j >> 2][j & 3] = phi1(aq[2 * j], aq[2 * j + 1]);
    phi_eps(pq);
    if (h & 1) { if (half == 0) { pq[0][0] = 0u; pq[0][1] = 0u; } } else if (half) { pq[1][2] = 0u; pq[1][3] = 0u; }
    PIN(pq[0]); PIN(pq[1]);
  }
  STAMP(5);
  const u32 mb2 = WS_MB2 + (rb * 16 + h) * 128 + ((4 * w + lane + hrot) & 15) * 4;
  {
#pragma nounroll
    for (int it = 0; it < SPIN_CAP; ++it) {
#pragma nounroll
      for (int i2 = 0; i2 < SPIN_CAP && need != 0; ++i2) {
        go = need & (u32)__ballot(mail == tag); need &= ~go;
#pragma unroll
        for (int p = 1; p < 16; ++p) if (go >> p & 1) load_kv(p);
        mail = poll32(ws, mb1, lane < 16 && need != 0);
        COUNT(14);
      }
      u32 m = 0;
#pragma unroll
      for (int p = 0; p < 16; ++p) m = sum4(m, d[p]);
      if (wave_sum(m) == 16u * tag) break;
      need = 0xfffeu; mail = tag;
      asm volatile("" ::: "memory");
      COUNT(11);
    }
    mail = poll32(ws, mb2, lane < 4);
    SBAR();
    f32x4 s4 = __builtin_bit_cast(f32x4, d[0]);
#pragma unroll
    for (int p = 1; p < 16; ++p) s4 += __builtin_bit_cast(f32x4, d[p]);
    const int dw = 8 * w + 4 * half - coff;
    u32x2 o2 = {pkbf(s4.x, s4.y), pkbf(s4.z, s4.w)};
    if (dw < 0 || dw >= 28) o2 = (u32x2){0u, 0u};
    *(LAS u32x2*)(lds + KVI + lane * 32 + w * 8) = o2;
  }
  STAMP(6);
  BAR();
  ST1_SC1(tag, rs, lane < 4 ? WS_MB2 + (rb * 16 + 4 * w + lane) * 128 + h * 4 : WS_END);
  {
    const int i = (r32 & 3) + 4 * (r32 >> 3), hh = (r32 >> 2) & 1;
    const int ew = i < 14 ? 14 * hh + i + coff : wone;
    LAS char* p = lds + KVI + (ew + 32 * half) * 32;
    const u32x4 a0 = *(LAS u32x4*)p, a1 = *(LAS u32x4*)(p + 16);
    f32x16 nm = {};
    nm = mfma32(a0, pq[0], nm); nm = mfma32(a1, pq[1], nm);
    const float den = rbf(rbf(nm[14]) + 1e-6f);
    const float rc = __builtin_amdgcn_rcpf(den);
    u32 o[8];
#pragma unroll
    for (int j = 0; j < 7; ++j) { const u32 t = pkbf(nm[2 * j], nm[2 * j + 1]); o[j] = pkbf(bflo(t) * rc, bfhi(t) * rc); }
    o[7] = tag - ((o[0] + o[1] + o[2]) + (o[3] + o[4] + o[5]) + o[6]);
    const u32 ob_off = WS_OB + ((rb * 4 + w) * 16 + h) * 2048 + l16;
    ST_SC1(((u32x4){o[0], o[1], o[2], o[3]}), rs, ob_off);
    ST_SC1(((u32x4){o[4], o[5], o[6], o[7]}), rs, ob_off + 1024);
    SBAR();
  }
  STAMP(7);
  u32x4 g[8];
  auto load_o = [&](int i, bool on = true) {
    const u32 so = on ? WS_OB + ((rb * 4 + rt) * 16 + ((4 * w + i + hrot) & 15)) * 2048 : WS_END;
    g[2 * i] = LDS_SC1(rs, l16, so); g[2 * i + 1] = LDS_SC1(rs, l16, so + 1024);
  };
  need = 15u;
  go = (u32)__ballot(mail == tag); need &= ~go;
  mail = poll32(ws, mb2, lane < 4 && need != 0);
#pragma unroll
  for (int i = 0; i < 4; ++i) load_o(i, go >> i & 1);
  SBAR();
  load_wo(WO_EARLY, 32);
  PIN(mail);
#pragma nounroll
  for (int it = 0; it < SPIN_CAP; ++it) {
#pragma nounroll
    for (int i2 = 0; i2 < SPIN_CAP && need != 0; ++i2) {
      go = need & (u32)__ballot(mail == tag); need &= ~go;
#pragma unroll
      for (int i = 0; i < 4; ++i) if (go >> i & 1) load_o(i);
      mail = poll32(ws, mb2, lane < 4 && need != 0);
      WAITVM(0);
      COUNT(12);
    }
#pragma unroll
    for (int i = 0; i < 4; ++i) need |= (u32)!__all(sum4(sum4(0u, g[2 * i]), g[2 * i + 1]) == tag) << i;
    if (need == 0) break;
    mail = tag;
    asm volatile("" ::: "memory");
    COUNT(13);
  }
#pragma unroll
  for (int p = 0; p < 8; ++p) {
    if (p & 1) g[p].w = 0u;
    *(LAS u32x4*)(lds + OST + (8 * w + p) * 1024 + l16) = g[p];
  }
  STAMP(8);
  BAR();
  STAMP(9);
  f32x16 acc;
#pragma nounroll
  for (int it = 0; it < SPIN_CAP; ++it) {
    u32 wsum = 0;
    u32x4 of[32];
#pragma unroll
    for (int k = 0; k < 8; ++k) of[k] = *(LAS u32x4*)(lds + OST + k * 1024 + l16);
    acc = (f32x16){};
    SBAR();
#pragma unroll
    for (int k = 0; k < 32; ++k) {
      wsum = sum4(wsum, wof[k]);
      u32x4 a = wof[k];
      if (k & 1) a.w = 0u;
      acc = mfma32(a, of[k], acc);
      if (k + 8 < 32) of[k + 8] = *(LAS u32x4*)(lds + OST + (k + 8) * 1024 + l16);
      PIN(wsum);
      SBAR();
    }
    if (__all(wsum == 16u * tag)) break;
    asm volatile("" ::: "memory");
    load_wo(0, 32);
    WAITVM(0);
  }
  {
    LAS char* p = lds + OTR + r32 * 272 + w * 64 + half * 32;
    *(LAS u32x4*)p = (u32x4){pkbf(acc[0], acc[1]), pkbf(acc[2], acc[3]), pkbf(acc[4], acc[5]), pkbf(acc[6], acc[7])};
    *(LAS u32x4*)(p + 16) = (u32x4){pkbf(acc[8], acc[9]), pkbf(acc[10], acc[11]), pkbf(acc[12], acc[13]), pkbf(acc[14], acc[15])};
  }
  BAR();
  if (ct < 3 || (tid & 15) < 8) {
#pragma unroll
    for (int i = 0; i < 2; ++i)
      *(u32x4*)(out + (u32)((128 * rb + 32 * rt + 16 * i + (tid >> 4)) * 1792 + 896 + ct * 256 + (tid & 15) * 16)) =
          *(LAS u32x4*)(lds + OTR + (16 * i + (tid >> 4)) * 272 + (tid & 15) * 16);
  }
  STAMP(10);
  STAMP_FLUSH();
}

DI f32x2 rot(f32x2 b) { return (f32x2){-b.y, b.x}; }
DI f32x2 cm1(f32x2 a, f32x2 b) { return (f32x2){a.x, a.x} * b; }
DI f32x2 cm2(f32x2 a, f32x2 br, f32x2 t) { return __builtin_elementwise_fma((f32x2){a.y, a.y}, br, t); }
DI f32x2 cexp_p(float a2, float f, float p) {
  const float m = __builtin_amdgcn_exp2f(a2 * p), ph = __builtin_amdgcn_fractf(f * p);
  return (f32x2){m, m} * (f32x2){__builtin_amdgcn_cosf(ph), __builtin_amdgcn_sinf(ph)};
}
DI u32 wave_umax(u32 v) {
  u32 t;
  t = __builtin_amdgcn_update_dpp(0u, v, 0x111, 0xf, 0xf, false); v = v > t ? v : t;
  t = __builtin_amdgcn_update_dpp(0u, v, 0x112, 0xf, 0xf, false); v = v > t ? v : t;
  t = __builtin_amdgcn_update_dpp(0u, v, 0x114, 0xf, 0xf, false); v = v > t ? v : t;
  t = __builtin_amdgcn_update_dpp(0u, v, 0x118, 0xf, 0xf, false); v = v > t ? v : t;
  const u32 a = __builtin_amdgcn_readlane(v, 15), b = __builtin_amdgcn_readlane(v, 31), c = __builtin_amdgcn_readlane(v, 47), d = __builtin_amdgcn_readlane(v, 63);
  const u32 ab = a > b ? a : b, cd = c > d ? c : d;
  return ab > cd ? ab : cd;
}

DI void role_a(LAS char* lds, const char* __restrict__ u, const u32* __restrict__ logA, const u32* __restrict__ Aim,
               const u32* __restrict__ Cre, const u32* __restrict__ Cim, const u16* __restrict__ logdt,
               const char* __restrict__ Dsk, const char* __restrict__ wqkv, const char* __restrict__ wo, char* __restrict__ out, char* ws, u32 epoch) {
  const int a = blockIdx.x, cb = a >> 5, jp = a & 31, tid = threadIdx.x, lane = tid & 63;
  const int w = __builtin_amdgcn_readfirstlane(tid >> 6);
  STAMP_DECL;
  STAMP(0);
  __builtin_amdgcn_s_setprio(APRIO);
  constexpr int SROW = 528, PAR = 21 * SROW, STG = PAR + 2048, TBL = 57664, UC = 0, UCS = 4160, RV = 8 * UCS, RVS = 4608, EX = RV + 2 * RVS;
  constexpr int UIM = TBL + 12288;
  static_assert(4 * STG <= TBL, "");
  const int PKS = TBL + w * 3072;
  const __amdgpu_buffer_rsrc_t rs = RSRC(ws, WS_END);
  const int ch = w >> 1, hf = w & 1, d = 2 * a + ch;
  const int pi = d * 128 + hf * 64 + lane;
  const int r32 = lane & 31, half = lane >> 5;
  u32 pA = logA[pi], pI = Aim[pi], pR = Cre[pi], pC = Cim[pi], ldt = ((const u32*)logdt)[a];
  SBAR();
  const int pkh_ = w == 0 ? (a & 15) : 2 * (a & 7) + (w - 1);
  {
    const __amdgpu_buffer_rsrc_t rp = RSRC(w == 0 ? wo : wqkv, w == 0 ? 448 * 896 : w < 3 ? 448 * 2688 : 0);
    const u32 vo = (lane >> 2) * (w == 0 ? 896 : 2688) + (lane & 3) * 16;
    const u32 so = w == 0 ? 28 * 896 * pkh_ + (a >> 4) * 64 : 16 * 2688 * (a >> 3) + ((56 * pkh_) & ~15);
    DMA16(rp, vo, so, lds + PKS, 0);
    DMA16(rp, vo, w == 0 ? so + 16 * 896 : 448 * 2688, lds + PKS + 1024, 0);
  }
#pragma unroll
  for (int i = 0; i < 2; ++i)
    DMA16(RSRC(u, 2048 * 896), (tid >> 3) * 896 + (((tid & 7) ^ ((tid >> 5) & 7)) << 4), (64 * jp + 32 * i) * 896 + cb * 128, lds + UIM + (32 * i + 8 * w) * 128, 0);
  const u32x4 dk = *(const u32x4*)(Dsk + cb * 128 + (tid & 7) * 16);
  SBAR(); __builtin_amdgcn_s_setprio(0); SBAR();
  int tix[8];
#pragma unroll
  for (int j = 0; j < 8; ++j) {
    const int p = (int)rbf((float)(2047 - 8 * tid - j));
    const int low = p & 7, blk = p >> 8;
    const int k4 = (blk < 4 ? blk : 4) * 4;
    const int sh = (0x32210 >> k4) & 15;
    const int base = blk + ((0xCBA70 >> k4) & 15);
    tix[j] = ((base + (low >> sh)) * 32 + ((p >> 3) & 31)) * 4;
    PIN(tix[j]);
  }
  const u32 tag = take_tag(epoch);
  auto publish_weights = [&]() {
  PRIO(APRIO);
  if (w == 0) {
    const int q = (lane >> 2) & 3, gam = 4 * ((lane >> 4) & 1) + (lane & 3), ng = 4 * (gam & 1) + (gam >> 1);
    LAS char* p = lds + PKS + (14 * half + q) * 64 + 8 * ng;
    const u32x2 r0 = tr_read(p), r1 = tr_read(p + 256), r2 = tr_read(p + 512), r3 = tr_read(p + 768);
    const u32 off = WS_WO + ((a >> 4) * 16 + pkh_) * 2048 + lane * 16;
    ST_SC1(((u32x4){r0.x, r0.y, r1.x, r1.y}), rs, off);
    ST_SC1(((u32x4){r2.x, r2.y, r3.x, tag - ((r0.x + r0.y + r1.x) + (r1.y + r2.x + r2.y) + r3.x)}), rs, off + 1024);
  } else if (w < 3) {
    LAS char* p = lds + PKS + (8 * half + ((lane >> 2) & 3)) * 64 + ((lane >> 4) & 1) * 32 + (lane & 3) * 8;
    const u32x2 r0 = tr_read(p), r1 = tr_read(p + 256);
    u32x4 v = {r0.x, r0.y, r1.x, r1.y};
    const bool ck = lane == ((pkh_ & 1) ? 1 : 29);
    if (ck) v = (u32x4){0u, 0u, 0u, 0u};
    const u32 c = tag - wave_sum(sum4(0u, v));
    if (ck) v.x = c;
    ST_SC1(v, rs, WS_WQ + (pkh_ * 28 + (a >> 3)) * 1024 + lane * 16);
  }
  PRIO(0);
  };
  auto publish_u = [&]() {
  PRIO(APRIO);
  BAR();
#pragma unroll
  for (int i = 0; i < 2; ++i) {
    const int jj = 16 * i + (tid >> 4), n = tid & 15;
    LAS char* p = lds + UIM + 4 * n * 128 + (((jj >> 2) ^ (n & 7)) << 4) + (jj & 3) * 4;
    const u32x4 v = {*(LAS u32*)p, *(LAS u32*)(p + 128), *(LAS u32*)(p + 256), *(LAS u32*)(p + 384)};
    ST_SC1(v, rs, WS_UX + (cb * 32 + jj) * 8192 + jp * 256 + n * 16);
    const u32 ck = tag - row_sum(sum4(0u, v));
    __builtin_amdgcn_raw_buffer_store_b64((u32x2){tag, ck}, rs, n == 15 ? WS_MBU + (cb * 32 + jj) * 256 + jp * 8 : tid + i == 0 ? WS_FLAG + a * 128 : WS_END, 0, 16);
  }
  PRIO(0);
  };
  auto publish = [&]() { WAITVM(0); publish_weights(); publish_u(); };
  if (PUB_FIRST) publish(); else WAITVM(5);
  PIN(pA); PIN(pI); PIN(pR); PIN(pC); PIN(ldt);
  STAMP(1);
  const float dt = rbf(expf(ch ? bfhi(ldt) : bflo(ldt)));
  const u32 mx = wave_umax(umax3(umax3(pR & 0x7fffu, (pR >> 16) & 0x7fffu, pC & 0x7fffu), (pC >> 16) & 0x7fffu, 0u));
  int ex = mx >> 7; ex = ex < 13 ? 13 : ex > 254 ? 254 : ex;
  const float sc = __uint_as_float((u32)(266 - ex) << 23), isc = __uint_as_float((u32)(ex - 12) << 23);
  LAS char* stg = lds + w * STG;
  {
    f32x2 y1[2], y1r[2], yq[2], yqr[2], p[2], q[2], c2[2], c4[2], c6[2];
#pragma unroll
    for (int m = 0; m < 2; ++m) {
      const float la = m ? bfhi(pA) : bflo(pA), ai = m ? bfhi(pI) : bflo(pI), cr = m ? bfhi(pR) : bflo(pR), ci = m ? bfhi(pC) : bflo(pC);
      const float a2 = -rbf(expf(la)) * dt * 1.4426950408889634f, f = ai * dt * 0.15915494309189535f;
      y1[m] = cexp_p(a2, -f, 1.f); yq[m] = cexp_p(a2, -f, 256.f); y1r[m] = rot(y1[m]); yqr[m] = rot(yq[m]);
      const f32x2 z128 = cexp_p(a2, f, 128.f);
      *(LAS f32x4*)(stg + PAR + lane * 32 + m * 16) = (f32x4){__builtin_fmaxf(8.f * a2, -1e30f), __builtin_amdgcn_fractf(8.f * f), z128.x, z128.y};
      p[m] = q[m] = (f32x2){cr, -ci} * sc;
    }
    LAS char* sw = stg + lane * 8;
#define PUT(r, v) *(LAS u32x2*)(sw + (r) * SROW) = (u32x2){pkh(v[0]), pkh(v[1])}
#define MUL(v, b) do { const f32x2 t0_ = cm1(v[0], b[0]), t1_ = cm1(v[1], b[1]); v[0] = cm2(v[0], b##r[0], t0_); v[1] = cm2(v[1], b##r[1], t1_); } while (0)
#define CPY(dst, src) do { dst[0] = src[0]; dst[1] = src[1]; } while (0)
    SBAR();
    PUT(0, p);
    MUL(p, y1); MUL(q, yq); PUT(1, p); PUT(8, q); SBAR();
    MUL(p, y1); MUL(q, yq); PUT(2, p); PUT(12, q); CPY(c2, p); SBAR();
    MUL(p, y1); MUL(q, yq); PUT(3, p); PUT(14, q); SBAR();
    MUL(p, y1); MUL(q, yq); PUT(4, p); PUT(16, q); CPY(c4, p); SBAR();
    MUL(p, y1); MUL(q, yq); PUT(5, p); PUT(17, q); SBAR();
    MUL(p, y1); MUL(q, yq); PUT(6, p); PUT(18, q); CPY(c6, p); SBAR();
    MUL(p, y1); MUL(q, yq); PUT(7, p); PUT(19, q); SBAR();
    MUL(c4, yq); MUL(q, yq); PUT(10, c4); PUT(20, q); SBAR();
    MUL(c4, yq); MUL(c2, yq); PUT(13, c4); PUT(9, c2); SBAR();
    MUL(c4, yq); MUL(c6, yq); PUT(15, c4); PUT(11, c6); SBAR();
#undef PUT
#undef MUL
#undef CPY
  }
  STAMP(2);
  if (!PUB_FIRST) publish();
  u32x4 ud[3];
  const __amdgpu_buffer_rsrc_t ru = RSRC(ws + WS_UX + a * 8192, 8192);
  const int npc = w < 3 ? 9 : 8;
  u64 mail = 0;
  const u32 mbu = WS_MBU + a * 256 + (8 * w + lane) * 8;
  auto load_u = [&]() {
    PRIO(APRIO);
    ud[0] = LD_SC1(ru, w * 2048 + lane * 16); ud[1] = LD_SC1(ru, w * 2048 + 1024 + lane * 16);
    ud[2] = LD_SC1(ru, lane < 16 ? w * 2048 + 2048 + lane * 16 : 8192);
    PRIO(0);
  };
  bool have_u = false;
  {
    const int c16 = lane & 15, quad = lane >> 4;
    const float rowf = (float)c16;
    LAS char* pp = stg + PAR + quad * 64;
    LAS char* py0 = stg + c16 * SROW + quad * 16;
    LAS char* py1 = stg + (16 + (c16 < 4 ? c16 : 4)) * SROW + quad * 16;
    f32x4 ka[4] = {};
    f32x4 pr[4]; u32x4 fy0, fy1;
#pragma unroll
    for (int i = 0; i < 4; ++i) pr[i] = *(LAS f32x4*)(pp + i * 16);
    auto kstep = [&](int s) {
      fy0 = *(LAS u32x4*)(py0 + s * 64); fy1 = *(LAS u32x4*)(py1 + s * 64);
      f32x4 cur[4];
#pragma unroll
      for (int i = 0; i < 4; ++i) { cur[i] = pr[i]; pr[i] = *(LAS f32x4*)(pp + (s + 1) * 256 + i * 16); }
      u32x4 x0, x1;
      f32x2 t[4], z[4], v[4];
#pragma unroll
      for (int i = 0; i < 4; ++i) t[i] = (f32x2){cur[i].x, cur[i].y} * rowf;
#pragma unroll
      for (int i = 0; i < 4; ++i) { const float m = __builtin_amdgcn_exp2f(t[i].x); z[i] = (f32x2){__builtin_amdgcn_cosf(t[i].y), __builtin_amdgcn_sinf(t[i].y)} * m; }
#pragma unroll
      for (int i = 0; i < 4; ++i) { v[i] = cm2((f32x2){cur[i].z, cur[i].w}, rot(z[i]), z[i] * cur[i].z); x0[i] = pkh(z[i]); }
#pragma unroll
      for (int i = 0; i < 4; ++i) x1[i] = pkh(v[i]);
      ka[0] = mfma16h(fy0, x0, ka[0]); ka[2] = mfma16h(fy1, x0, ka[2]);
      ka[1] = mfma16h(fy0, x1, ka[1]); ka[3] = mfma16h(fy1, x1, ka[3]);
    };
#pragma nounroll
    for (int s = 0; s < UPOLL_AT; s += 2) { kstep(s); kstep(s + 1); }
    PRIO(APRIO); mail = poll64(ws, mbu, lane < npc); PRIO(0);
#pragma nounroll
    for (int s = UPOLL_AT; s < 8; s += 2) { kstep(s); kstep(s + 1); }
    STAMP(3);
    if (__popcll(__ballot((u32)mail == tag)) == npc) { load_u(); have_u = true; }
    else { PRIO(APRIO); mail = poll64(ws, mbu, lane < npc); PRIO(0); }
    const float fac = 2.f * dt * isc;
    LAS float* tb = (LAS float*)(lds + TBL + w * 3072) + 4 * quad * 32 + c16;
#pragma unroll
    for (int g = 0; g < 4; ++g) { tb[g * 32] = ka[0][g] * fac; tb[g * 32 + 16] = ka[1][g] * fac; }
    if (quad < 2) {
#pragma unroll
      for (int g = 0; g < 4; ++g) { tb[(16 + g) * 32] = ka[2][g] * fac; tb[(16 + g) * 32 + 16] = ka[3][g] * fac; }
    }
  }
  u32 sk[8];
#pragma unroll
  for (int i = 0; i < 2; ++i) {
    const u32x4 us = *(LAS u32x4*)(lds + UIM + (32 * i + (tid >> 3)) * 128 + (((tid & 7) ^ ((tid >> 5) & 7)) << 4));
#pragma unroll
    for (int k = 0; k < 4; ++k) { sk[4 * i + k] = pkbf(bflo(us[k]) * bflo(dk[k]), bfhi(us[k]) * bfhi(dk[k])); PIN(sk[4 * i + k]); }
  }
  STAMP(4);
  BAR();
  {
    float kk[4][8];
#pragma unroll
    for (int c = 0; c < 4; ++c)
#pragma unroll
      for (int j = 0; j < 8; ++j) kk[c][j] = *(LAS float*)(lds + TBL + c * 3072 + tix[j]);
    SBAR();
#pragma unroll
    for (int c = 0; c < 2; ++c) {
      u32x4 v;
#pragma unroll
      for (int j = 0; j < 4; ++j) v[j] = pkbf(kk[2 * c][2 * j] + kk[2 * c + 1][2 * j], kk[2 * c][2 * j + 1] + kk[2 * c + 1][2 * j + 1]);
      *(LAS u32x4*)(lds + RV + c * RVS + tid * 16) = v;
    }
  }
  if (tid < 64) *(LAS u32x4*)(lds + RV + half * RVS + 4096 + r32 * 16) = (u32x4){0u, 0u, 0u, 0u};
  SBAR();
#pragma nounroll
  for (int it = 0; it < SPIN_CAP; ++it) {
    if (!have_u) {
#pragma nounroll
      for (int i2 = 0; i2 < SPIN_CAP && __popcll(__ballot((u32)mail == tag)) != npc; ++i2) { mail = poll64(ws, mbu, lane < npc); COUNT(14); }
      load_u();
    }
    if (wave_sum(sum4(sum4(sum4((u32)(mail >> 32), ud[0]), ud[1]), ud[2])) == npc * tag) break;
    have_u = false;
    asm volatile("" ::: "memory");
    COUNT(11);
  }
#pragma unroll
  for (int i = 0; i < 2; ++i) {
    const u32 nx = __builtin_amdgcn_update_dpp(__builtin_amdgcn_readlane(ud[i + 1].x, 0), ud[i].x, 0x130, 0xf, 0xf, false);
    const u32 dw[5] = {ud[i].x, ud[i].y, ud[i].z, ud[i].w, nx};
    LAS char* p = lds + UC + (128 * w + 64 * i + lane + 4) * 8;
#pragma unroll
    for (int c = 0; c < 2; ++c) {
      u32 pr[4];
#pragma unroll
      for (int k = 0; k < 4; ++k) pr[k] = __builtin_amdgcn_perm(dw[k + 1], dw[k], c ? 0x07060302u : 0x05040100u);
      *(LAS u32x2*)(p + (4 * c) * UCS) = (u32x2){pr[0], pr[2]};
      *(LAS u32x2*)(p + (4 * c + 1) * UCS) = (u32x2){pr[1], pr[3]};
      *(LAS u32*)(p + (4 * c + 2) * UCS - 4) = pr[0]; *(LAS u32*)(p + (4 * c + 2) * UCS) = pr[2];
      *(LAS u32*)(p + (4 * c + 3) * UCS - 4) = pr[1]; *(LAS u32*)(p + (4 * c + 3) * UCS) = pr[3];
    }
  }
  if (w == 0) {
    const u32 u0 = __builtin_amdgcn_readlane(ud[0].x, 0);
    const int cs = lane >> 3, ix = lane & 7, last = (cs & 2) ? 6 : 7;
    const u32 v = ((cs & 1) && ix == last) ? (cs & 4 ? u0 & 0xffff0000u : u0 << 16) : 0u;
    if (ix <= last) *(LAS u32*)(lds + UC + cs * UCS + ix * 4) = v;
  }
  BAR();
  STAMP(5);
  {
    const int c16 = lane & 15, hq = lane >> 4;
    LAS char* pa = lds + RV + ch * RVS + 2 * (2032 - 256 * 7 + 128 * hf - 16 * c16 + 8 * hq);
    LAS char* pb = lds + UC + (4 * ch + ((c16 + 1) & 3)) * UCS + 2 * (128 * hf + 8 * hq + 4 * ((c16 + 1) >> 2));
    f32x4 acc[8] = {};
    u32x4 fa[2][8], fb[2][8];
    auto ldf = [&](int rho, int m) {
      fa[rho & 1][m] = *(LAS u32x4*)(pa + 64 * rho + 512 * (7 - m));
      const u32x2 lo = *(volatile LAS u32x2*)(pb + 64 * rho + 512 * m), hi = *(volatile LAS u32x2*)(pb + 64 * rho + 512 * m + 8);
      fb[rho & 1][m] = (u32x4){lo.x, lo.y, hi.x, hi.y};
    };
#pragma unroll
    for (int m = 0; m < 8; ++m) ldf(0, m);
#pragma unroll
    for (int rho = 0; rho < 4; ++rho) {
      SBAR();
#pragma unroll
      for (int g = 0; g < 8; ++g) {
#pragma unroll
        for (int aa = 0; aa <= g; ++aa) {
          if (aa + g < 8) acc[aa + g] = mfma16(fa[rho & 1][g], fb[rho & 1][aa], acc[aa + g]);
          if (aa < g && aa + g < 8) acc[aa + g] = mfma16(fa[rho & 1][aa], fb[rho & 1][g], acc[aa + g]);
        }
        if (rho < 3) { ldf(rho + 1, g); SBAR(); }
      }
    }
    STAMP(6);
    PRIO(ATAIL);
#pragma unroll
    for (int t = 0; t < 8; ++t) *(LAS f32x4*)(lds + EX + w * 8192 + t * 1024 + lane * 16) = acc[t];
    BAR();
#pragma unroll
    for (int i = 0; i < 2; ++i) {
      LAS char* p = lds + EX + (2 * w + i) * 1024 + lane * 16;
      const f32x4 y0 = *(LAS f32x4*)p + *(LAS f32x4*)(p + 8192), y1 = *(LAS f32x4*)(p + 16384) + *(LAS f32x4*)(p + 24576);
      const u32x4 v = {pkbf(y0.x, y1.x), pkbf(y0.y, y1.y), pkbf(y0.z, y1.z), pkbf(y0.w, y1.w)};
      ST_SC1(v, rs, WS_YX + (cb * 32 + 4 * (2 * w + i) + hq) * 8192 + jp * 256 + c16 * 16);
      const u32 ck = tag - row_sum(sum4(0u, v));
      if (c16 == 15) __builtin_amdgcn_raw_buffer_store_b64((u32x2){tag, ck}, rs, WS_MBY + (cb * 32 + 4 * (2 * w + i) + hq) * 256 + jp * 8, 0, 16);
    }
  }
  STAMP(7);
  {
    u32x4 y[2];
    const u32 mby = WS_MBY + a * 256 + (8 * w + lane) * 8;
#pragma nounroll
    for (int it = 0; it < SPIN_CAP; ++it) {
      mail = poll64(ws, mby, lane < 8);
      if (__ballot((u32)mail == tag) == 0xffull) break;
      COUNT(13);
    }
#pragma nounroll
    for (int it = 0; it < SPIN_CAP; ++it) {
      asm volatile("" ::: "memory");
      y[0] = LD_SC1(rs, WS_YX + a * 8192 + w * 2048 + lane * 16); y[1] = LD_SC1(rs, WS_YX + a * 8192 + w * 2048 + 1024 + lane * 16);
      if (wave_sum(sum4(sum4((u32)(mail >> 32), y[0]), y[1])) == 8u * tag) break;
      COUNT(12);
    }
    STAMP(8);
#pragma unroll
    for (int i = 0; i < 2; ++i)
#pragma unroll
      for (int g = 0; g < 4; ++g) *(LAS u32*)(lds + (16 * g + (lane & 15)) * 144 + (8 * w + 4 * i + (lane >> 4)) * 4) = y[i][g];
    BAR();
#pragma unroll
    for (int i = 0; i < 2; ++i) {
      const u32x4 v = *(LAS u32x4*)(lds + (32 * i + (tid >> 3)) * 144 + (tid & 7) * 16);
      u32x4 o;
#pragma unroll
      for (int k = 0; k < 4; ++k) o[k] = pkbf(bflo(v[k]) + bflo(sk[4 * i + k]), bfhi(v[k]) + bfhi(sk[4 * i + k]));
      *(u32x4*)(out + (u32)((64 * jp + 32 * i + (tid >> 3)) * 1792 + cb * 128 + (tid & 7) * 16)) = o;
    }
  }
  STAMP(9);
  STAMP_FLUSH();
}

__global__ void NOPK __launch_bounds__(256, 2)
fused_kernel(const char* __restrict__ u, const u32* __restrict__ logA, const u32* __restrict__ Aim, const u32* __restrict__ Cre,
             const u32* __restrict__ Cim, const u16* __restrict__ logdt, const char* __restrict__ Dsk, const char* __restrict__ x,
             const char* __restrict__ wqkv, const char* __restrict__ wo, char* __restrict__ out, char* ws) {
  __shared__ __attribute__((aligned(16))) char smem[81920];
  LAS char* lds = (LAS char*)smem;
  if (blockIdx.x < 8 * PREFETCH) {
    u64 pc;
    asm volatile("s_getpc_b64 %0" : "=s"(pc));
    if (threadIdx.x < CODE_LINES)
      __builtin_amdgcn_global_load_lds((const GAS void*)((pc & ~127ull) + threadIdx.x * 128), (LAS void*)(lds + 80896 + __builtin_amdgcn_readfirstlane(threadIdx.x >> 6) * 256), 4, 0, 0);
  }
  asm volatile("" :: "s"(u), "s"(logA), "s"(Aim), "s"(Cre), "s"(Cim), "s"(logdt), "s"(Dsk), "s"(x), "s"(wqkv), "s"(wo), "s"(out), "s"(ws));
  const u32 epoch = request_epoch(ws);
  if (blockIdx.x >= 256) role_b(lds, x, wqkv, out, ws, epoch);
  else if (blockIdx.x < 224) role_a(lds, u, logA, Aim, Cre, Cim, logdt, Dsk, wqkv, wo, out, ws, epoch);
  else {
    const int t = threadIdx.x;
    if (blockIdx.x == 224) {
      const u32 tag = take_tag(epoch);
      __builtin_amdgcn_s_sleep(127);
      wait_flags(ws, 256 + t, tag);
      wait_flags(ws, t < 224 ? t : 0, tag);
      BAR();
      if (t == 0) __hip_atomic_store((GAS u32*)ws, tag, RLX_AGENT);
    }
  }
}

extern "C" void kernel_launch(void* const* d_in, const int* in_sizes, int n_in, void* d_out, int out_size, void* d_ws,
                              size_t ws_size, hipStream_t stream) {
  fused_kernel<<<512, 256, 0, stream>>>((const char*)d_in[0], (const u32*)d_in[1], (const u32*)d_in[2], (const u32*)d_in[3],
                                        (const u32*)d_in[4], (const u16*)d_in[5], (const char*)d_in[6], (const char*)d_in[7],
                                        (const char*)d_in[8], (const char*)d_in[9], (char*)d_out, (char*)d_ws);
}
```

```cpp
#include <hip/hip_runtime.h>
#include <stdint.h>

typedef unsigned short u16;
typedef unsigned u32;
typedef unsigned long long u64;
typedef short s16x4 __attribute__((ext_vector_type(4)));
typedef __bf16 bf16x8 __attribute__((ext_vector_type(8)));
typedef __bf16 bf16x2 __attribute__((ext_vector_type(2)));
typedef _Float16 f16x8 __attribute__((ext_vector_type(8)));
typedef _Float16 f16x2 __attribute__((ext_vector_type(2)));
typedef float f32x16 __attribute__((ext_vector_type(16)));
typedef float f32x4 __attribute__((ext_vector_type(4)));
typedef float f32x2 __attribute__((ext_vector_type(2)));
typedef u32 u32x4 __attribute__((ext_vector_type(4)));
typedef u32 u32x2 __attribute__((ext_vector_type(2)));
#define LAS __attribute__((address_space(3)))
#define GAS __attribute__((address_space(1)))
#define DI __device__ __forceinline__
#if defined(__HIP_DEVICE_COMPILE__)
#define NOPK __attribute__((target("no-packed-fp32-ops")))
#else
#define NOPK
#endif
#define RLX_AGENT __ATOMIC_RELAXED, __HIP_MEMORY_SCOPE_AGENT

#define BMAP 1
#define PREFETCH 1
#define CODE_BEFORE 0
#define CODE_LINES 220
#define QEARLY 3
#define GATH_AT 18
#define WO_EARLY 0
#define WO_MID 12
#define WO_G 32
#define WO_E 8
#define KVSTEP 1
#define PUB_FIRST 0
#define UPOLL_AT 8
#define TMAP 1
#define BPRIO1 2
#define ABASE 0
#define ACONV 0
#define L2PF 0
#define NTA 1
#define NTX 0
#define W0FIRST 1
#define ORD 4
#define PF2AT 1
#define UMID 0
#define WFIRST 0
#define HEADC 1
#define ASLEEP 0
#define TDELAY 0
#define UEARLY 0
#define ROT 1
#define APRIO 3
#define ATAIL 1
#define BPRIO 2

constexpr u32 WS_FLAG = 4096;
constexpr u32 WS_MB1 = 73728;
constexpr u32 WS_MB2 = WS_MB1 + 32768;
constexpr u32 WS_MBU = WS_MB2 + 32768;
constexpr u32 WS_MBY = WS_MBU + 57344;
constexpr u32 WS_KVP = 1u << 18;
constexpr u32 WS_OB = WS_KVP + (1u << 20);
constexpr u32 WS_UX = WS_OB + (2u << 20);
constexpr u32 WS_YX = WS_UX + (2u << 20);
constexpr u32 WS_WQ = WS_YX + (2u << 20);
constexpr u32 WS_WO = WS_WQ + (1u << 19);
constexpr u32 WS_MBQ = WS_WO + 458752;
constexpr u32 WS_END = WS_WO + (1u << 19);
constexpr u32 WS_DBG = WS_END;
constexpr int SPIN_CAP = 1 << 15;

#define STAMP(i) do {} while (0)
#define STAMP_DECL
#define COUNT(i) do {} while (0)
#define STAMP_FLUSH() do {} while (0)

DI float bflo(u32 v) { return __uint_as_float(v << 16); }
DI float bfhi(u32 v) { return __uint_as_float(v & 0xffff0000u); }
DI u32 pkbf(float a, float b) { f32x2 p = {a, b}; return __builtin_bit_cast(u32, __builtin_convertvector(p, bf16x2)); }
DI u32 pkh(f32x2 p) { return __builtin_bit_cast(u32, __builtin_convertvector(p, f16x2)); }
DI float rbf(float a) { return bflo(pkbf(a, a)); }
DI u32 umax3(u32 a, u32 b, u32 c) { u32 m = a > b ? a : b; return m > c ? m : c; }
DI u32 sum4(u32 m, u32x4 v) { return (m + v.x + v.y) + v.z + v.w; }
DI u32 row_sum(u32 v) {
  v += __builtin_amdgcn_update_dpp(0u, v, 0x111, 0xf, 0xf, true); v += __builtin_amdgcn_update_dpp(0u, v, 0x112, 0xf, 0xf, true);
  v += __builtin_amdgcn_update_dpp(0u, v, 0x114, 0xf, 0xf, true); v += __builtin_amdgcn_update_dpp(0u, v, 0x118, 0xf, 0xf, true);
  return v;
}
DI u32 wave_sum(u32 v) {
  v = row_sum(v);
  return (__builtin_amdgcn_readlane(v, 15) + __builtin_amdgcn_readlane(v, 31)) + (__builtin_amdgcn_readlane(v, 47) + __builtin_amdgcn_readlane(v, 63));
}
DI u32 wave_umax(u32 v) {
  u32 t;
  t = __builtin_amdgcn_update_dpp(0u, v, 0x111, 0xf, 0xf, false); v = v > t ? v : t;
  t = __builtin_amdgcn_update_dpp(0u, v, 0x112, 0xf, 0xf, false); v = v > t ? v : t;
  t = __builtin_amdgcn_update_dpp(0u, v, 0x114, 0xf, 0xf, false); v = v > t ? v : t;
  t = __builtin_amdgcn_update_dpp(0u, v, 0x118, 0xf, 0xf, false); v = v > t ? v : t;
  const u32 a = __builtin_amdgcn_readlane(v, 15), b = __builtin_amdgcn_readlane(v, 31), c = __builtin_amdgcn_readlane(v, 47), d = __builtin_amdgcn_readlane(v, 63);
  const u32 ab = a > b ? a : b, cd = c > d ? c : d;
  return ab > cd ? ab : cd;
}

DI u32 request_epoch(const char* ws) { u32 e; asm volatile("s_load_dword %0, %1, 0x0" : "=s"(e) : "s"(ws) : "memory"); return e; }
DI u32 take_tag(u32 e) { u32 r; asm volatile("s_waitcnt lgkmcnt(0)\n\ts_and_b32 %0, %1, -2" : "=s"(r) : "s"(e) : "memory", "scc"); return r * 1664525u + 2027808447u; }
DI void put_flag(char* ws, u32 tag) { if (threadIdx.x == 0) __hip_atomic_store((GAS u32*)(ws + WS_FLAG) + blockIdx.x * 32, tag, RLX_AGENT); }
#define PSCOPE __HIP_MEMORY_SCOPE_AGENT
DI u32 poll32(char* ws, u32 off, bool on) { u32 z = 0, r = 0; asm volatile("" : "+v"(z)); if (on) r = __hip_atomic_fetch_add((GAS u32*)(ws + off), z, __ATOMIC_RELAXED, PSCOPE); return r; }
DI u64 poll64(char* ws, u32 off, bool on) { u64 z = 0, r = 0; asm volatile("" : "+v"(z)); if (on) r = __hip_atomic_fetch_add((GAS u64*)(ws + off), z, __ATOMIC_RELAXED, PSCOPE); return r; }
#define SBAR() __builtin_amdgcn_sched_barrier(0)
#define PRIO(n) do { SBAR(); __builtin_amdgcn_s_setprio(n); SBAR(); } while (0)
#define PIN(x) asm volatile("" : "+v"(x))
DI f32x16 mfma32(u32x4 a, u32x4 b, f32x16 c) { return __builtin_amdgcn_mfma_f32_32x32x16_bf16(__builtin_bit_cast(bf16x8, a), __builtin_bit_cast(bf16x8, b), c, 0, 0, 0); }
DI f32x16 mfma32h(u32x4 a, u32x4 b, f32x16 c) { return __builtin_amdgcn_mfma_f32_32x32x16_f16(__builtin_bit_cast(f16x8, a), __builtin_bit_cast(f16x8, b), c, 0, 0, 0); }
DI f32x4 mfma16h(u32x4 a, u32x4 b, f32x4 c) { return __builtin_amdgcn_mfma_f32_16x16x32_f16(__builtin_bit_cast(f16x8, a), __builtin_bit_cast(f16x8, b), c, 0, 0, 0); }
DI f32x4 mfma16(u32x4 a, u32x4 b, f32x4 c) { return __builtin_amdgcn_mfma_f32_16x16x32_bf16(__builtin_bit_cast(bf16x8, a), __builtin_bit_cast(bf16x8, b), c, 0, 0, 0); }
DI u32x2 tr_read(LAS char* p) { return __builtin_bit_cast(u32x2, __builtin_amdgcn_ds_read_tr16_b64_v4i16((LAS s16x4*)p)); }
#define RSRC(p, n) __builtin_amdgcn_make_buffer_rsrc((void*)(p), 0, (n), 0x00020000)
#define DMA16(rs, voff, soff, dst, aux) __builtin_amdgcn_raw_ptr_buffer_load_lds((rs), (LAS void*)(dst), 16, (voff), (soff), 0, (aux))
#define WAITVM(n) do { asm volatile("" ::: "memory"); __builtin_amdgcn_s_waitcnt(0x0F70 | ((n) & 15) | (((n) >> 4) << 14)); asm volatile("" ::: "memory"); } while (0)
#define VMCNT(n) asm volatile("s_waitcnt vmcnt(" #n ")" ::: "memory")
#define LGKM0() asm volatile("s_waitcnt lgkmcnt(0)" ::: "memory")
#define BAR() do { asm volatile("s_waitcnt lgkmcnt(0)" ::: "memory"); __builtin_amdgcn_s_barrier(); asm volatile("" ::: "memory"); } while (0)
#define ST_SC1(v, rs, off) __builtin_amdgcn_raw_buffer_store_b128((v), (rs), (off), 0, 16)
#define LD_SC1(rs, off) __builtin_amdgcn_raw_buffer_load_b128((rs), (off), 0, 16)
#define LDS_SC1(rs, off, soff) __builtin_amdgcn_raw_buffer_load_b128((rs), (off), (soff), 16)
#define ST1_SC1(v, rs, off) __builtin_amdgcn_raw_buffer_store_b32((v), (rs), (off), 0, 16)
#define LD1_SC1(rs, off) __builtin_amdgcn_raw_buffer_load_b32((rs), (off), 0, 16)

typedef unsigned short u16x2 __attribute__((ext_vector_type(2)));
DI u32 phi1(float a, float b) {
  const u32 t = pkbf(a, b);
  const f32x2 x = {bflo(t), bfhi(t)};
  const f32x2 y = x * 1.4426950408889634f;
  const float e0 = __builtin_amdgcn_fmed3f(__builtin_amdgcn_exp2f(y.x), 0.f, 1.f);
  const float e1 = __builtin_amdgcn_fmed3f(__builtin_amdgcn_exp2f(y.y), 0.f, 1.f);
  const f32x2 x1 = x + 1.0f;
  return pkbf(__builtin_fmaxf(x1.x, e0), __builtin_fmaxf(x1.y, e1));
}
DI void phi_eps(u32x4 (&p)[2]) {
  u16x2 m = {0xffff, 0xffff};
#pragma unroll
  for (int j = 0; j < 8; ++j) { const u32 e = p[j >> 2][j & 3]; m = __builtin_elementwise_min(m, __builtin_bit_cast(u16x2, e)); }
  if (__any(m.x < 0x3A80 || m.y < 0x3A80)) {
#pragma unroll
    for (int j = 0; j < 8; ++j) { const u32 r = p[j >> 2][j & 3]; p[j >> 2][j & 3] = pkbf(bflo(r) + 1e-6f, bfhi(r) + 1e-6f); }
  }
}

DI void wait_flags(const char* ws, int idx, u32 tag) {
  const GAS u32* f = (const GAS u32*)(ws + WS_FLAG) + idx * 32;
#pragma nounroll
  for (int it = 0; it < SPIN_CAP; ++it) {
    const u32 v = __hip_atomic_load(f, RLX_AGENT);
    if (__all(v == tag)) break;
    __builtin_amdgcn_s_sleep(16);
  }
}

template <bool HEAD>
DI void role_b(LAS char* lds, const char* __restrict__ x, const char* __restrict__ wqkv, char* __restrict__ out, char* ws, u32 epoch) {
  const int b = blockIdx.x - 256, tid = threadIdx.x, lane = tid & 63;
  const int w = __builtin_amdgcn_readfirstlane(tid >> 6);
  STAMP_DECL;
  STAMP(0);
  const int rb = 4 * ((b & 7) >> 1) + (b >> 6), h = (b & 1) * 8 + ((b >> 3) & 7);
  const int start = (int)((0x1065432106543210ull >> (4 * ((h & 7) + 2 * (rb & 3)))) & 7);
  const __amdgpu_buffer_rsrc_t rs = RSRC(ws, WS_END), rx = RSRC(x, 2048 * 896), rw = RSRC(wqkv, 448 * 2688);
  constexpr int WR = 49152, KVI = WR + 4096, OST = 16384, OTR = WR;
#define XS(i) ((i) * 16384)
#define QS(cc) ((cc) < 2 ? WR + 24576 + (cc) * 4096 : (cc) < 6 ? XS(1) + ((cc) - 2) * 1024 : WR)
#define QD(cc) ((cc) >= 2 && (cc) < 6 ? 4096 : 1024)
  const int wsb = (56 * h) & ~15;
  const int coff = (h & 1) * 4;
  const int wone = (h & 1) ? 0 : 28;
  const u32 xv0 = (128 * rb + 32 * w + (lane >> 3)) * 896 + (((lane & 7) ^ (lane >> 4)) << 4);
  const u32 xv1 = (xv0 ^ 64) + 8 * 896;
  const u32 wv = (16 * w + (lane >> 2)) * 2688 + wsb + (lane & 3) * 16;
  const u32 l16 = lane * 16;
  int ch_[7];
  auto chunk = [&](int cc) { return ch_[cc]; };
  const int gi = h + rb;
  const u32 ordc = ORD == 1 ? 0x6543210u : ORD == 2 ? ((gi & 1) ? 0x5461032u : 0x6543210u) : ORD == 3 ? ((gi & 1) ? 0x6452301u : 0x6543210u)
                 : ((gi & 2) ? ((gi & 1) ? 0x4652103u : 0x5461032u) : ((gi & 1) ? 0x4650321u : 0x6543210u));
  auto next_chunk = [&](int cc) { int c = (ordc >> (4 * cc)) & 7; asm volatile("" : "+s"(c)); ch_[cc] = c; };
  auto issue_x = [&](int cc, int i) { DMA16(rx, (i & 1) ? xv1 : xv0, chunk(cc) * 128 + (i >> 1) * (16 * 896), lds + XS(cc % 3) + w * 4096 + i * 1024, NTX); };
  auto issue_w = [&](int cc, int i) { DMA16(rw, wv, chunk(cc) * (64 * 2688) + 896 * (i + 1), lds + WR + (cc & 3) * 8192 + i * 4096 + w * 1024, 0); };
  bool qok = true;
  auto issue_q = [&](int cc) { DMA16(rs, l16, qok ? WS_WQ + (h * 28 + 4 * chunk(cc) + w) * 1024 : WS_END, lds + QS(cc) + w * QD(cc), 16); };
#pragma unroll
  for (int cc = 0; cc < 2; ++cc) {
    next_chunk(cc);
    if (HEAD == (cc < HEADC)) { issue_w(cc, 0); issue_w(cc, 1); issue_x(cc, 0); issue_x(cc, 1); issue_x(cc, 2); issue_x(cc, 3); }
    SBAR();
  }
  if (HEAD) return;
  next_chunk(2); next_chunk(3); next_chunk(4); next_chunk(5); next_chunk(6);
  __builtin_amdgcn_s_setprio(BPRIO1);
  const int r32 = lane & 31, half = lane >> 5;
  const int xrd = w * 4096 + r32 * 128 + ((half ^ ((r32 >> 1) & 7)) << 4);
  LAS char* whi = lds + WR + (8 * half + ((lane >> 2) & 3)) * 64 + ((lane >> 4) & 1) * 32 + (lane & 3) * 8;
  PIN(whi);
  const int rt = h >> 2, ct = (h + ((h & 8) ? 2 : 1)) & 3;
  u32 wo_src = WS_WO + (4 * ct + w < 14 ? 4 * ct + w : 13) * 32768, kvp_src = WS_KVP + h * 65536 + w * 16384;
  u32 ob_dst = WS_OB + ((rb * 4 + w) * 16 + h) * 2048, ob_src = WS_OB + ((rb * 4 + rt) * 16 + 4 * w) * 2048;
  asm volatile("" : "+s"(wo_src), "+s"(kvp_src), "+s"(ob_dst), "+s"(ob_src));
  u32 tell1 = lane < 4 ? WS_MB1 + (h * 16 + 4 * w + lane) * 128 + rb * 4 : tid == 4 ? WS_FLAG + blockIdx.x * 128 : WS_END;
  u32 tell2 = lane < 4 ? WS_MB2 + (rb * 16 + 4 * w + lane) * 128 + h * 4 : WS_END;
  u32 mb1 = WS_MB1 + (h * 16 + rb) * 128 + ((lane + rb) & 15) * 4, mb2 = WS_MB2 + (rb * 16 + h) * 128 + ((4 * w + lane) & 15) * 4;
  u32 kvp_dst = kvp_src + rb * 1024 + l16, kvs[16];
#pragma unroll
  for (int p = 1; p < 16; ++p) { kvs[p] = kvp_src + ((p + rb) & 15) * 1024; asm volatile("" : "+s"(kvs[p])); }
  const int dw = 8 * w + 4 * half - coff;
  u32 rowmask = dw < 0 || dw >= 28 ? 0u : ~0u, padlo = (h & 1) && !half ? 0u : ~0u, padhi = !(h & 1) && half ? 0u : ~0u;
  const int oi = (r32 & 3) + 4 * (r32 >> 3);
  LAS char* kvrd = lds + KVI + ((oi < 14 ? 14 * ((r32 >> 2) & 1) + oi + coff : wone) + 32 * half) * 32;
  LAS char* qlo = lds + l16; LAS char* qhi = lds + WR + l16;
  PIN(tell1); PIN(tell2); PIN(mb1); PIN(mb2); PIN(kvp_dst); PIN(rowmask); PIN(padlo); PIN(padhi); PIN(kvrd); PIN(qhi);
  const bool ck = lane == wone + 1;
  u32 mbq = WS_MBQ + (h * 16 + rb) * 128 + (4 * (lane & 7) + w) * 4, mail = 0, tag = 0;
  PIN(mbq);
  u32x4 xf[28], wf[2][8];
  f32x16 ak = {}, av = {};
  auto rd = [&](int cc, int i) {
    if ((i & 1) == 0) xf[cc * 4 + (i >> 1)] = *(LAS u32x4*)(lds + XS(cc % 3) + (xrd ^ ((i >> 1) << 5)));
    LAS char* p = whi + (cc & 3) * 8192 + (i >> 1) * 1024 + (i & 1) * 4096;
    const u32x2 lo = tr_read(p), hi = tr_read(p + 256);
    wf[cc & 1][i] = (u32x4){lo.x, lo.y, hi.x, hi.y};
  };
#pragma unroll
  for (int cc = 0; cc < 8; ++cc) {
    if (cc < 7) {
      if (cc == 0) VMCNT(6); else if (cc < 5) VMCNT(8); else if (cc == 5) VMCNT(7); else VMCNT(1);
      BAR();
      if (cc == 5) __builtin_amdgcn_s_setprio(BPRIO);
      if (cc == 6) tag = take_tag(epoch);
      if (cc == 0) { STAMP(1); }
      if (cc == 2) { STAMP(23); } if (cc == 4) { STAMP(24); } if (cc == 5) { STAMP(25); } if (cc == 6) { STAMP(26); }
      if (cc >= 5) {
#pragma unroll
        for (int i = 0; i < 8; ++i) rd(cc, i);
        SBAR();
      }
#pragma unroll
      for (int i = 0; i < 8; ++i) {
        if (cc > 0) {
          if (i & 1) av = mfma32(xf[(cc - 1) * 4 + (i >> 1)], wf[(cc - 1) & 1][i], av);
          else ak = mfma32(xf[(cc - 1) * 4 + (i >> 1)], wf[(cc - 1) & 1][i], ak);
        }
        if (cc < 5) rd(cc, i);
        if (cc == 0) { if (i < 2) issue_w(2, i); else if (i < 6) issue_x(2, i - 2); else issue_w(3, i - 6); }
        else if (cc <= 4 && i < 4) issue_x(cc + 2, i);
        else if (cc <= 3 && i < 6) issue_w(cc + 3, i - 4);
        else if (cc == 4 && i == 4) mail = poll32(ws, mbq, lane < 7);
        else if (cc == 6 && i >= 4) {
          if (i == 4) { WAITVM(0); qok = __ballot(mail == tag) == 0x7full; }
          issue_q(2 * (i - 4)); if (i < 7) issue_q(2 * (i - 4) + 1);
        }
        SBAR();
      }
#pragma unroll
      for (int s = 3; s >= 0; --s) { PIN(wf[cc & 1][2 * s + 1]); PIN(wf[cc & 1][2 * s]); PIN(xf[cc * 4 + s]); }
      if (cc > 0) { PIN(ak); PIN(av); }
    } else {
#pragma unroll
      for (int s = 0; s < 4; ++s) ak = mfma32(xf[24 + s], wf[0][2 * s], ak);
#pragma unroll
      for (int s = 0; s < 4; ++s) av = mfma32(xf[24 + s], wf[0][2 * s + 1], av);
      SBAR();
    }
  }
  STAMP(2);
  u32x4 wof[32], d[16], pq[2];
  auto load_wo = [&](int k) { wof[k] = __builtin_amdgcn_raw_buffer_load_b128(rs, l16 + (k & 3) * 1024, wo_src + (k >> 2) * 4096, 16); };
  {
    u32x4 pk[2], pv[2];
#pragma unroll
    for (int j = 0; j < 8; ++j) {
      pk[j >> 2][j & 3] = phi1(ak[2 * j], ak[2 * j + 1]);
      if (j < WO_E) { PIN(pk[j >> 2][j & 3]); load_wo(j); SBAR(); }
    }
    phi_eps(pk);
    STAMP(11);
#pragma unroll
    for (int j = 0; j < 8; ++j) { const u32 v = pkbf(av[2 * j], av[2 * j + 1]); pv[j >> 2][j & 3] = r32 == wone ? 0x3F803F80u : v; }
    f32x16 kv = {};
    kv = mfma32(pk[0], pv[0], kv); kv = mfma32(pk[1], pv[1], kv);
    if (!qok) {
#pragma nounroll
      for (int it = 0; it < SPIN_CAP && __ballot(poll32(ws, mbq, lane < 7) == tag) != 0x7full; ++it) {}
      qok = true;
#pragma unroll
      for (int cc = 0; cc < 7; ++cc) issue_q(cc);
      WAITVM(0);
      COUNT(13);
    }
    WAITVM(WO_E);
    ST1_SC1(tag, rs, tell1);
#pragma unroll
    for (int g = 0; g < 4; ++g) *(LAS f32x4*)(lds + XS(2) + w * 4096 + g * 1024 + l16) = (f32x4){kv[4 * g], kv[4 * g + 1], kv[4 * g + 2], kv[4 * g + 3]};
    STAMP(12);
    BAR();
    f32x4 s4 = *(LAS f32x4*)(lds + XS(2) + tid * 16);
#pragma unroll
    for (int ww = 1; ww < 4; ++ww) s4 += *(LAS f32x4*)(lds + XS(2) + ww * 4096 + tid * 16);
    u32x4 v = __builtin_bit_cast(u32x4, s4);
    const u32 part = v.y + v.z + v.w;
    const u32 c = tag - wave_sum(ck ? part : part + v.x);
    if (ck) v.x = c;
    ST_SC1(v, rs, kvp_dst);
    d[0] = v;
  }
  u32 go, need = 0xfffeu;
  mail = poll32(ws, mb1, lane < 16);
  STAMP(3);
#define QP(cc) (QS(cc) >= WR ? qhi + (QS(cc) - WR) : qlo + QS(cc))
  auto load_kv = [&](int p, bool on = true) { d[p] = __builtin_amdgcn_raw_buffer_load_b128(rs, l16, on ? kvs[p] : WS_END, 16); };
  {
    f32x16 aq = {};
    u32x4 qf[2][4];
    u32 qsum = 0;
    auto read_q = [&](int cc) {
#pragma unroll
      for (int s = 0; s < 4; ++s) qf[cc & 1][s] = *(LAS u32x4*)(QP(cc) + s * QD(cc));
      SBAR();
    };
    read_q(0);
#pragma unroll
    for (int cc = 0; cc < 7; ++cc) {
      if (cc < 6) read_q(cc + 1);
#pragma unroll
      for (int s = 0; s < 4; ++s) {
        aq = mfma32(qf[cc & 1][s], xf[cc * 4 + s], aq);
        qsum = sum4(qsum, qf[cc & 1][s]);
        if (s & 1) load_wo(WO_E + 2 * cc + (s >> 1));
        PIN(aq); PIN(qsum);
        SBAR();
      }
    }
    qok = wave_sum(qsum) == 28u * tag;
    STAMP(14);
    go = need & (u32)__ballot(mail == tag); need &= ~go;
    mail = poll32(ws, mb1, lane < 16 && need != 0);
    STAMP(15);
#pragma unroll
    for (int j = 0; j < 8; ++j) {
      if (j) load_kv(2 * j, go >> (2 * j) & 1);
      load_kv(2 * j + 1, go >> (2 * j + 1) & 1);
      pq[j >> 2][j & 3] = phi1(aq[2 * j], aq[2 * j + 1]);
      PIN(pq[j >> 2][j & 3]);
      SBAR();
    }
    phi_eps(pq);
  }
  STAMP(5);
  PIN(mail);
  STAMP(16);
#pragma nounroll
  for (int i2 = 0; i2 < SPIN_CAP && need != 0; ++i2) {
    go = need & (u32)__ballot(mail == tag); need &= ~go;
#pragma unroll
    for (int p = 1; p < 16; ++p) if (go >> p & 1) load_kv(p);
    if (need != 0) { mail = poll32(ws, mb1, lane < 16); WAITVM(0); }
    COUNT(14);
  }
#pragma unroll
  for (int k = WO_E + 14; k < WO_G; ++k) load_wo(k);
  WAITVM(WO_G - WO_E - 14);
  STAMP(18);
#pragma nounroll
  for (int it = 0; it < SPIN_CAP; ++it) {
    u32 m = 0;
#pragma unroll
    for (int p = 0; p < 16; ++p) m = sum4(m, d[p]);
    if (wave_sum(m) == 16u * tag) break;
    asm volatile("" ::: "memory");
#pragma unroll
    for (int p = 1; p < 16; ++p) load_kv(p);
    WAITVM(0);
    COUNT(11);
  }
  mail = poll32(ws, mb2, lane < 4);
  SBAR();
  {
    f32x4 s4 = __builtin_bit_cast(f32x4, d[0]);
#pragma unroll
    for (int p = 1; p < 16; ++p) s4 += __builtin_bit_cast(f32x4, d[p]);
    *(LAS u32x2*)(lds + KVI + lane * 32 + w * 8) = (u32x2){pkbf(s4.x, s4.y) & rowmask, pkbf(s4.z, s4.w) & rowmask};
  }
  STAMP(6);
  BAR();
  {
    const u32x4 a0 = *(LAS u32x4*)kvrd, a1 = *(LAS u32x4*)(kvrd + 16);
    if (!qok) {
      f32x16 aq = {};
#pragma nounroll
      for (int k2 = 0; k2 < 28; ++k2) {
        const int ks = 4 * ((ordc >> (k2 & 28)) & 7) + (k2 & 3);
        u32x4 f;
#pragma nounroll
        for (int it = 0; it < SPIN_CAP; ++it) {
          asm volatile("" ::: "memory");
          f = LDS_SC1(rs, l16, WS_WQ + (h * 28 + ks) * 1024);
          if (wave_sum(sum4(0u, f)) == tag) break;
        }
        aq = mfma32(f, __builtin_amdgcn_raw_buffer_load_b128(rx, (128 * rb + 32 * w + r32) * 896 + half * 16, ks * 32, 0), aq);
      }
#pragma unroll
      for (int j = 0; j < 8; ++j) pq[j >> 2][j & 3] = phi1(aq[2 * j], aq[2 * j + 1]);
      phi_eps(pq);
      COUNT(15);
    }
    ST1_SC1(tag, rs, tell2);
    pq[0][0] &= padlo; pq[0][1] &= padlo; pq[1][2] &= padhi; pq[1][3] &= padhi;
    f32x16 nm = {};
    nm = mfma32(a0, pq[0], nm); nm = mfma32(a1, pq[1], nm);
    const float den = rbf(rbf(nm[14]) + 1e-6f);
    const float rc = __builtin_amdgcn_rcpf(den);
    u32 o[8];
#pragma unroll
    for (int j = 0; j < 7; ++j) { const u32 t = pkbf(nm[2 * j], nm[2 * j + 1]); o[j] = pkbf(bflo(t) * rc, bfhi(t) * rc); }
    o[7] = tag - ((o[0] + o[1] + o[2]) + (o[3] + o[4] + o[5]) + o[6]);
    __builtin_amdgcn_raw_buffer_store_b128((u32x4){o[0], o[1], o[2], o[3]}, rs, l16, ob_dst, 16);
    __builtin_amdgcn_raw_buffer_store_b128((u32x4){o[4], o[5], o[6], o[7]}, rs, l16 + 1024, ob_dst, 16);
    SBAR();
  }
  STAMP(7);
  u32x4 g[8];
  auto load_o = [&](int i, bool on = true) {
    const u32 so = on ? ob_src + (i >> 1) * 4096 : WS_END;
    g[2 * i] = __builtin_amdgcn_raw_buffer_load_b128(rs, l16 + (i & 1) * 2048, so, 16);
    g[2 * i + 1] = __builtin_amdgcn_raw_buffer_load_b128(rs, l16 + (i & 1) * 2048 + 1024, so, 16);
  };
  WAITVM(3);
  go = (u32)__ballot(mail == tag); need = 15u & ~go;
  mail = poll32(ws, mb2, lane < 4 && need != 0);
#pragma unroll
  for (int i = 0; i < 4; ++i) load_o(i, go >> i & 1);
  SBAR();
  STAMP(19);
#pragma nounroll
  for (int it = 0; it < SPIN_CAP; ++it) {
    u32 m = 0;
#pragma unroll
    for (int k = 0; k < 32; ++k) m = sum4(m, wof[k]);
    if (__all(m == 16u * tag)) break;
    asm volatile("" ::: "memory");
#pragma unroll
    for (int k = 0; k < 32; ++k) load_wo(k);
    WAITVM(0);
  }
#pragma unroll
  for (int k = 1; k < 32; k += 2) { wof[k].w = 0u; PIN(wof[k]); }
  STAMP(20);
#pragma nounroll
  for (int it = 0; it < SPIN_CAP; ++it) {
#pragma nounroll
    for (int i2 = 0; i2 < SPIN_CAP && need != 0; ++i2) {
      go = need & (u32)__ballot(mail == tag); need &= ~go;
      if (need != 0) mail = poll32(ws, mb2, lane < 4);
#pragma unroll
      for (int i = 0; i < 4; ++i) if (go >> i & 1) load_o(i);
      COUNT(12);
    }
#pragma unroll
    for (int i = 0; i < 4; ++i) need |= (u32)!__all(sum4(sum4(0u, g[2 * i]), g[2 * i + 1]) == tag) << i;
    if (need == 0) break;
    mail = tag;
    asm volatile("" ::: "memory");
    COUNT(13);
  }
#pragma unroll
  for (int p = 0; p < 8; ++p) {
    if (p & 1) g[p].w = 0u;
    *(LAS u32x4*)(lds + OST + (8 * w + p) * 1024 + l16) = g[p];
  }
  STAMP(8);
  BAR();
  STAMP(9);
  f32x16 acc = {};
  {
    u32x4 of[32];
#pragma unroll
    for (int k = 0; k < 6; ++k) of[k] = *(LAS u32x4*)(lds + OST + k * 1024 + l16);
    SBAR();
#pragma unroll
    for (int k = 0; k < 32; ++k) {
      acc = mfma32(wof[k], of[k], acc);
      if (k + 6 < 32) of[k + 6] = *(LAS u32x4*)(lds + OST + (k + 6) * 1024 + l16);
      SBAR();
    }
  }
  STAMP(22);
  {
    LAS char* p = lds + OTR + r32 * 272 + w * 64 + half * 32;
    *(LAS u32x4*)p = (u32x4){pkbf(acc[0], acc[1]), pkbf(acc[2], acc[3]), pkbf(acc[4], acc[5]), pkbf(acc[6], acc[7])};
    *(LAS u32x4*)(p + 16) = (u32x4){pkbf(acc[8], acc[9]), pkbf(acc[10], acc[11]), pkbf(acc[12], acc[13]), pkbf(acc[14], acc[15])};
  }
  BAR();
  if (ct < 3 || (tid & 15) < 8) {
#pragma unroll
    for (int i = 0; i < 2; ++i)
      *(GAS u32x4*)(out + (u32)((128 * rb + 32 * rt + 16 * i + (tid >> 4)) * 1792 + 896 + ct * 256 + (tid & 15) * 16)) =
          *(LAS u32x4*)(lds + OTR + (16 * i + (tid >> 4)) * 272 + (tid & 15) * 16);
  }
  STAMP(10);
  STAMP_FLUSH();
}

DI u32 hmul1(u32 a, u32 b) { const f16x2 x = __builtin_bit_cast(f16x2, a), y = __builtin_bit_cast(f16x2, b); return __builtin_bit_cast(u32, (f16x2){x.x, x.x} * y); }
DI u32 hmul2(u32 a, u32 b, u32 t) {
  const f16x2 x = __builtin_bit_cast(f16x2, a), y = __builtin_bit_cast(f16x2, b);
  return __builtin_bit_cast(u32, __builtin_elementwise_fma((f16x2){x.y, x.y}, (f16x2){-y.y, y.x}, __builtin_bit_cast(f16x2, t)));
}
DI f32x2 rot(f32x2 b) { return (f32x2){-b.y, b.x}; }
DI f32x2 cm1(f32x2 a, f32x2 b) { return (f32x2){a.x, a.x} * b; }
DI f32x2 cm2(f32x2 a, f32x2 br, f32x2 t) { return __builtin_elementwise_fma((f32x2){a.y, a.y}, br, t); }
DI f32x2 cexp_p(float a2, float f, float p) {
  const float m = __builtin_amdgcn_exp2f(a2 * p), ph = __builtin_amdgcn_fractf(f * p);
  return (f32x2){m, m} * (f32x2){__builtin_amdgcn_cosf(ph), __builtin_amdgcn_sinf(ph)};
}
DI void role_a(LAS char* lds, const char* __restrict__ u, const char* __restrict__ Dsk, const char* __restrict__ wqkv, const char* __restrict__ wo, char* __restrict__ out, char* ws, u32 epoch,
               u32 pA, u32 pI, u32 pR, u32 pC, u32 ldt) {
  const int a = blockIdx.x, cb = a >> 5, jp = a & 31, tid = threadIdx.x, lane = tid & 63;
  const int w = __builtin_amdgcn_readfirstlane(tid >> 6);
  STAMP_DECL;
  STAMP(0);
  __builtin_amdgcn_s_setprio(APRIO);
  constexpr int SROW = 528, STG = 32 * SROW, UC = 0, UCS = 4160, RV = 36864, RVS = 4608, EX = RV + 2 * RVS, UIM = 4 * STG;
  const int PKS = UIM + 8192 + (w ? 1024 + w * 1024 : 0);
  const __amdgpu_buffer_rsrc_t rs = RSRC(ws, WS_END);
  const int ch = w >> 1, hf = w & 1, d = 2 * a + ch;
  const int r32 = lane & 31, half = lane >> 5;
  const int pkh_ = w == 0 ? (a & 15) : 2 * (a & 7) + (w - 1);
  {
    const __amdgpu_buffer_rsrc_t rp = RSRC(w == 0 ? wo : wqkv, w == 0 ? 448 * 896 : w < 3 ? 448 * 2688 : 0);
    const u32 vo = (lane >> 2) * (w == 0 ? 896 : 2688) + (lane & 3) * 16;
    const u32 so = w == 0 ? 28 * 896 * pkh_ + (a >> 4) * 64 : 16 * 2688 * (a >> 3) + ((56 * pkh_) & ~15);
    DMA16(rp, vo, so, lds + PKS, 0);
    DMA16(rp, vo, w == 0 ? so + 16 * 896 : 448 * 2688, lds + (w == 0 ? PKS + 1024 : UIM + 8192 + 5120), 0);
  }
  u32x4 dk;
  auto load_tile = [&]() {
#pragma unroll
    for (int i = 0; i < 2; ++i)
      DMA16(RSRC(u, 2048 * 896), (tid >> 3) * 896 + (((tid & 7) ^ ((tid >> 5) & 7)) << 4), (64 * jp + 32 * i) * 896 + cb * 128, lds + UIM + (32 * i + 8 * w) * 128, NTA ? 2 : 0);
    dk = *(const GAS u32x4*)(Dsk + cb * 128 + (tid & 7) * 16);
  };
  if (UEARLY == 1) load_tile();
  PRIO(ABASE);
  int tix[8];
#pragma unroll
  for (int j = 0; j < 8; ++j) {
    const int p = (int)rbf((float)(2047 - 8 * tid - j));
    const int low = p & 7, blk = p >> 8;
    const int k4 = (blk < 4 ? blk : 4) * 4;
    const int sh = (0x32210 >> k4) & 15;
    const int base = blk + ((0xCBA70 >> k4) & 15);
    tix[j] = ((base + (low >> sh)) * 32 + ((p >> 3) & 31)) * 4;
    PIN(tix[j]);
  }
  const u32 tag = take_tag(epoch);
  auto publish_weights = [&]() {
  PRIO(APRIO);
  if (w == 0) {
    const int q = (lane >> 2) & 3, gam = 4 * ((lane >> 4) & 1) + (lane & 3), ng = 4 * (gam & 1) + (gam >> 1);
    LAS char* p = lds + PKS + (14 * half + q) * 64 + 8 * ng;
    const u32x2 r0 = tr_read(p), r1 = tr_read(p + 256), r2 = tr_read(p + 512), r3 = tr_read(p + 768);
    const u32 off = WS_WO + ((a >> 4) * 16 + pkh_) * 2048 + lane * 16;
    ST_SC1(((u32x4){r0.x, r0.y, r1.x, r1.y}), rs, off);
    ST_SC1(((u32x4){r2.x, r2.y, r3.x, tag - ((r0.x + r0.y + r1.x) + (r1.y + r2.x + r2.y) + r3.x)}), rs, off + 1024);
  } else if (w < 3) {
    LAS char* p = lds + PKS + (8 * half + ((lane >> 2) & 3)) * 64 + ((lane >> 4) & 1) * 32 + (lane & 3) * 8;
    const u32x2 r0 = tr_read(p), r1 = tr_read(p + 256);
    u32x4 v = {r0.x, r0.y, r1.x, r1.y};
    const bool ck = lane == ((pkh_ & 1) ? 1 : 29);
    if (ck) v = (u32x4){0u, 0u, 0u, 0u};
    const u32 c = tag - wave_sum(sum4(0u, v));
    if (ck) v.x = c;
    ST_SC1(v, rs, WS_WQ + (pkh_ * 28 + (a >> 3)) * 1024 + lane * 16);
    if (lane < 16) ST1_SC1(tag, rs, WS_MBQ + (pkh_ * 16 + lane) * 128 + (a >> 3) * 4);
  }
  PRIO(ABASE);
  };
  auto publish_u = [&]() {
  PRIO(APRIO);
  BAR();
#pragma unroll
  for (int i = 0; i < 2; ++i) {
    const int jj = 16 * i + (tid >> 4), n = tid & 15;
    LAS char* p = lds + UIM + 4 * n * 128 + (((jj >> 2) ^ (n & 7)) << 4) + (jj & 3) * 4;
    const u32x4 v = {*(LAS u32*)p, *(LAS u32*)(p + 128), *(LAS u32*)(p + 256), *(LAS u32*)(p + 384)};
    ST_SC1(v, rs, WS_UX + (cb * 32 + jj) * 8192 + jp * 256 + n * 16);
    const u32 ck = tag - row_sum(sum4(0u, v));
    __builtin_amdgcn_raw_buffer_store_b64((u32x2){tag, ck}, rs, n == 15 ? WS_MBU + (cb * 32 + jj) * 256 + jp * 8 : tid + i == 0 ? WS_FLAG + a * 128 : WS_END, 0, 16);
  }
  PRIO(ABASE);
  };
  if (WFIRST) { WAITVM(0); publish_weights(); if (UEARLY == 1) publish_u(); } else if (UEARLY == 1) WAITVM(5); else WAITVM(2);
  PIN(pA); PIN(pI); PIN(pR); PIN(pC); PIN(ldt);
  if (UEARLY == 2) { PRIO(APRIO); load_tile(); PRIO(ABASE); }
  STAMP(1);
  const float dt = rbf(expf(ch ? bfhi(ldt) : bflo(ldt)));
  const u32 mx = wave_umax(umax3(umax3(pR & 0x7fffu, (pR >> 16) & 0x7fffu, pC & 0x7fffu), (pC >> 16) & 0x7fffu, 0u));
  int ex = mx >> 7; ex = ex < 13 ? 13 : ex > 254 ? 254 : ex;
  const float sc = __uint_as_float((u32)(266 - ex) << 23), isc = __uint_as_float((u32)(ex - 12) << 23);
  LAS char* stg = lds + w * STG;
  u32x4 ud[3];
  const __amdgpu_buffer_rsrc_t ru = RSRC(ws + WS_UX + a * 8192, 8192);
  const int npc = w < 3 ? 9 : 8;
  u64 mail = 0;
  const u32 mbu = WS_MBU + a * 256 + (8 * w + lane) * 8;
  auto load_u = [&]() {
    PRIO(APRIO);
    ud[0] = LD_SC1(ru, w * 2048 + lane * 16); ud[1] = LD_SC1(ru, w * 2048 + 1024 + lane * 16);
    ud[2] = LD_SC1(ru, lane < 16 ? w * 2048 + 2048 + lane * 16 : 8192);
    PRIO(ABASE);
  };
  bool have_u = false;
  auto try_u = [&]() {
    if (!have_u) { if (__popcll(__ballot((u32)mail == tag)) == npc) { load_u(); have_u = true; } else { PRIO(APRIO); mail = poll64(ws, mbu, lane < npc); PRIO(ABASE); } }
  };
  {
    f32x2 y1[2], yq[2], z8[2], z32[2], p[2], q[2], c2[2], c4[2], c6[2];
#pragma unroll
    for (int m = 0; m < 2; ++m) {
      const float la = m ? bfhi(pA) : bflo(pA), ai = m ? bfhi(pI) : bflo(pI), cr = m ? bfhi(pR) : bflo(pR), ci = m ? bfhi(pC) : bflo(pC);
      const float a2 = -rbf(expf(la)) * dt * 1.4426950408889634f, f = ai * dt * 0.15915494309189535f;
      y1[m] = cexp_p(a2, -f, 1.f); yq[m] = cexp_p(a2, -f, 256.f); z8[m] = cexp_p(a2, f, 8.f);
      p[m] = q[m] = (f32x2){cr, -ci} * sc;
    }
    LAS char* sw = stg + lane * 8;
#define PUT(r, v) *(LAS u32x2*)(sw + (r) * SROW) = (u32x2){pkh(v[0]), pkh(v[1])}
#define MUL(v, b) do { v[0] = cm2(v[0], rot(b[0]), cm1(v[0], b[0])); v[1] = cm2(v[1], rot(b[1]), cm1(v[1], b[1])); } while (0)
#define CPY(dst, src) do { dst[0] = src[0]; dst[1] = src[1]; } while (0)
    SBAR();
    PUT(0, p); *(LAS u32x2*)(sw + 21 * SROW) = (u32x2){0x3C00u, 0x3C00u};
    MUL(p, y1); MUL(q, yq); PUT(1, p); PUT(8, q); SBAR();
    MUL(p, y1); MUL(q, yq); PUT(2, p); PUT(12, q); CPY(c2, p); SBAR();
    MUL(p, y1); MUL(q, yq); PUT(3, p); PUT(14, q); SBAR();
    MUL(p, y1); MUL(q, yq); PUT(4, p); PUT(16, q); CPY(c4, p); SBAR();
    MUL(p, y1); MUL(q, yq); PUT(5, p); PUT(17, q); SBAR();
    MUL(p, y1); MUL(q, yq); PUT(6, p); PUT(18, q); CPY(c6, p); SBAR();
    MUL(p, y1); MUL(q, yq); PUT(7, p); PUT(19, q); SBAR();
    if (WFIRST && UEARLY == 1) { PRIO(APRIO); mail = poll64(ws, mbu, lane < npc); PRIO(ABASE); }
    MUL(c4, yq); MUL(q, yq); PUT(10, c4); PUT(20, q); SBAR();
    MUL(c4, yq); MUL(c2, yq); PUT(13, c4); PUT(9, c2); SBAR();
    MUL(c4, yq); MUL(c6, yq); PUT(15, c4); PUT(11, c6); SBAR();
    CPY(p, z8); PUT(22, p); MUL(p, z8); PUT(23, p); SBAR();
    MUL(p, z8); PUT(24, p); MUL(p, z8); PUT(25, p); CPY(z32, p); SBAR();
    MUL(p, z32); PUT(26, p); MUL(p, z32); PUT(27, p); SBAR();
    MUL(p, z32); PUT(28, p); MUL(p, z32); PUT(29, p); SBAR();
    MUL(p, z32); PUT(30, p); MUL(p, z32); PUT(31, p); SBAR();
#undef PUT
#undef MUL
#undef CPY
  }
  STAMP(2);
  if (!WFIRST) { WAITVM(0); publish_weights(); if (UEARLY) publish_u(); } else if (UEARLY == 1) try_u();
  STAMP(13);
  {
    const int c16 = lane & 15, quad = lane >> 4;
    LAS char* py0 = stg + c16 * SROW + quad * 16;
    LAS char* py1 = stg + (16 + (c16 < 4 ? c16 : 4)) * SROW + quad * 16;
    LAS char* pt1 = stg + (21 + (c16 & 3)) * SROW + quad * 16;
    LAS char* pt2 = stg + (c16 < 4 ? 21 : 24 + (c16 >> 2)) * SROW + quad * 16;
    LAS char* pt3 = stg + (28 + (c16 >> 2)) * SROW + quad * 16;
    f32x4 ka[4] = {};
    u32x4 fr[2][5];
    auto rdk = [&](int s) {
      fr[s & 1][0] = *(LAS u32x4*)(pt1 + s * 64); fr[s & 1][1] = *(LAS u32x4*)(pt2 + s * 64); fr[s & 1][2] = *(LAS u32x4*)(pt3 + s * 64);
      fr[s & 1][3] = *(LAS u32x4*)(py0 + s * 64); fr[s & 1][4] = *(LAS u32x4*)(py1 + s * 64);
    };
    rdk(0);
#pragma unroll
    for (int s = 0; s < 8; ++s) {
      if (s < 7) rdk(s + 1);
      SBAR();
      u32x4 x0, x1;
#pragma unroll
      for (int i = 0; i < 4; ++i) { x0[i] = hmul1(fr[s & 1][0][i], fr[s & 1][1][i]); x1[i] = hmul1(fr[s & 1][0][i], fr[s & 1][2][i]); }
#pragma unroll
      for (int i = 0; i < 4; ++i) { x0[i] = hmul2(fr[s & 1][0][i], fr[s & 1][1][i], x0[i]); x1[i] = hmul2(fr[s & 1][0][i], fr[s & 1][2][i], x1[i]); }
      ka[0] = mfma16h(fr[s & 1][3], x0, ka[0]); ka[2] = mfma16h(fr[s & 1][4], x0, ka[2]);
      ka[1] = mfma16h(fr[s & 1][3], x1, ka[1]); ka[3] = mfma16h(fr[s & 1][4], x1, ka[3]);
      SBAR();
    }
    if (UEARLY) try_u(); else { __builtin_amdgcn_s_sleep(TDELAY); PRIO(APRIO); load_tile(); PRIO(ABASE); }
    STAMP(3);
    const float fac = 2.f * dt * isc;
    LAS float* tb = (LAS float*)stg + 4 * quad * 32 + c16;
#pragma unroll
    for (int g = 0; g < 4; ++g) { tb[g * 32] = ka[0][g] * fac; tb[g * 32 + 16] = ka[1][g] * fac; }
    if (quad < 2) {
#pragma unroll
      for (int g = 0; g < 4; ++g) { tb[(16 + g) * 32] = ka[2][g] * fac; tb[(16 + g) * 32 + 16] = ka[3][g] * fac; }
    }
  }
  STAMP(4);
  BAR();
  {
    float kk[4][8];
#pragma unroll
    for (int c = 0; c < 4; ++c)
#pragma unroll
      for (int j = 0; j < 8; ++j) kk[c][j] = *(LAS float*)(lds + c * STG + tix[j]);
    SBAR();
#pragma unroll
    for (int c = 0; c < 2; ++c) {
      u32x4 v;
#pragma unroll
      for (int j = 0; j < 4; ++j) v[j] = pkbf(kk[2 * c][2 * j] + kk[2 * c + 1][2 * j], kk[2 * c][2 * j + 1] + kk[2 * c + 1][2 * j + 1]);
      *(LAS u32x4*)(lds + RV + c * RVS + tid * 16) = v;
    }
  }
  STAMP(11);
  if (UEARLY) {
    try_u();
    BAR();
  } else { WAITVM(0); publish_u(); }
  STAMP(12);
  u32 sk[8];
#pragma unroll
  for (int i = 0; i < 2; ++i) {
    const u32x4 us = *(LAS u32x4*)(lds + UIM + (32 * i + (tid >> 3)) * 128 + (((tid & 7) ^ ((tid >> 5) & 7)) << 4));
#pragma unroll
    for (int k = 0; k < 4; ++k) { sk[4 * i + k] = pkbf(bflo(us[k]) * bflo(dk[k]), bfhi(us[k]) * bfhi(dk[k])); PIN(sk[4 * i + k]); }
  }
  if (!UEARLY) { PRIO(APRIO); mail = poll64(ws, mbu, lane < npc); PRIO(ABASE); }
  STAMP(14);
  if (tid < 64) *(LAS u32x4*)(lds + RV + half * RVS + 4096 + r32 * 16) = (u32x4){0u, 0u, 0u, 0u};
  SBAR();
#pragma nounroll
  for (int it = 0; it < SPIN_CAP; ++it) {
    if (!have_u) {
#pragma nounroll
      for (int i2 = 0; i2 < SPIN_CAP && __popcll(__ballot((u32)mail == tag)) != npc; ++i2) { __builtin_amdgcn_s_sleep(ASLEEP); mail = poll64(ws, mbu, lane < npc); COUNT(14); }
      load_u();
    }
    if (wave_sum(sum4(sum4(sum4((u32)(mail >> 32), ud[0]), ud[1]), ud[2])) == npc * tag) break;
    have_u = false;
    asm volatile("" ::: "memory");
    COUNT(11);
  }
  STAMP(15);
#pragma unroll
  for (int i = 0; i < 2; ++i) {
    const u32 nx = __builtin_amdgcn_update_dpp(__builtin_amdgcn_readlane(ud[i + 1].x, 0), ud[i].x, 0x130, 0xf, 0xf, false);
    const u32 dw[5] = {ud[i].x, ud[i].y, ud[i].z, ud[i].w, nx};
    LAS char* p = lds + UC + (128 * w + 64 * i + lane + 4) * 8;
#pragma unroll
    for (int c = 0; c < 2; ++c) {
      u32 pr[4];
#pragma unroll
      for (int k = 0; k < 4; ++k) pr[k] = __builtin_amdgcn_perm(dw[k + 1], dw[k], c ? 0x07060302u : 0x05040100u);
      *(LAS u32x2*)(p + (4 * c) * UCS) = (u32x2){pr[0], pr[2]};
      *(LAS u32x2*)(p + (4 * c + 1) * UCS) = (u32x2){pr[1], pr[3]};
      *(LAS u32*)(p + (4 * c + 2) * UCS - 4) = pr[0]; *(LAS u32*)(p + (4 * c + 2) * UCS) = pr[2];
      *(LAS u32*)(p + (4 * c + 3) * UCS - 4) = pr[1]; *(LAS u32*)(p + (4 * c + 3) * UCS) = pr[3];
    }
  }
  if (w == 0) {
    const u32 u0 = __builtin_amdgcn_readlane(ud[0].x, 0);
    const int cs = lane >> 3, ix = lane & 7, last = (cs & 2) ? 6 : 7;
    const u32 v = ((cs & 1) && ix == last) ? (cs & 4 ? u0 & 0xffff0000u : u0 << 16) : 0u;
    if (ix <= last) *(LAS u32*)(lds + UC + cs * UCS + ix * 4) = v;
  }
  STAMP(16);
  BAR();
  STAMP(5);
  PRIO(ACONV);
  {
    const int c16 = lane & 15, hq = lane >> 4;
    LAS char* pa = lds + RV + ch * RVS + 2 * (2032 - 256 * 7 + 128 * hf - 16 * c16 + 8 * hq);
    LAS char* pb = lds + UC + (4 * ch + ((c16 + 1) & 3)) * UCS + 2 * (128 * hf + 8 * hq + 4 * ((c16 + 1) >> 2));
    f32x4 acc[8] = {};
    u32x4 fa[2][8], fb[2][8];
    auto ldf = [&](int rho, int m) {
      fa[rho & 1][m] = *(LAS u32x4*)(pa + 64 * rho + 512 * (7 - m));
      const u32x2 lo = *(volatile LAS u32x2*)(pb + 64 * rho + 512 * m), hi = *(volatile LAS u32x2*)(pb + 64 * rho + 512 * m + 8);
      fb[rho & 1][m] = (u32x4){lo.x, lo.y, hi.x, hi.y};
    };
#pragma unroll
    for (int m = 0; m < 8; ++m) ldf(0, m);
#pragma unroll
    for (int rho = 0; rho < 4; ++rho) {
      SBAR();
#pragma unroll
      for (int g = 0; g < 8; ++g) {
#pragma unroll
        for (int aa = 0; aa <= g; ++aa) {
          if (aa + g < 8) acc[aa + g] = mfma16(fa[rho & 1][g], fb[rho & 1][aa], acc[aa + g]);
          if (aa < g && aa + g < 8) acc[aa + g] = mfma16(fa[rho & 1][aa], fb[rho & 1][g], acc[aa + g]);
        }
        if (rho < 3) { ldf(rho + 1, g); SBAR(); }
      }
    }
    STAMP(6);
    PRIO(ATAIL);
#pragma unroll
    for (int t = 0; t < 8; ++t) *(LAS f32x4*)(lds + EX + w * 8192 + t * 1024 + lane * 16) = acc[t];
    STAMP(17);
    BAR();
    STAMP(18);
#pragma unroll
    for (int i = 0; i < 2; ++i) {
      LAS char* p = lds + EX + (2 * w + i) * 1024 + lane * 16;
      const f32x4 y0 = *(LAS f32x4*)p + *(LAS f32x4*)(p + 8192), y1 = *(LAS f32x4*)(p + 16384) + *(LAS f32x4*)(p + 24576);
      const u32x4 v = {pkbf(y0.x, y1.x), pkbf(y0.y, y1.y), pkbf(y0.z, y1.z), pkbf(y0.w, y1.w)};
      ST_SC1(v, rs, WS_YX + (cb * 32 + 4 * (2 * w + i) + hq) * 8192 + jp * 256 + c16 * 16);
      const u32 ck = tag - row_sum(sum4(0u, v));
      if (c16 == 15) __builtin_amdgcn_raw_buffer_store_b64((u32x2){tag, ck}, rs, WS_MBY + (cb * 32 + 4 * (2 * w + i) + hq) * 256 + jp * 8, 0, 16);
    }
  }
  STAMP(7);
  {
    u32x4 y[2];
    const u32 mby = WS_MBY + a * 256 + (8 * w + lane) * 8;
#pragma nounroll
    for (int it = 0; it < SPIN_CAP; ++it) {
      __builtin_amdgcn_s_sleep(ASLEEP);
      mail = poll64(ws, mby, lane < 8);
      if (__ballot((u32)mail == tag) == 0xffull) break;
      COUNT(13);
    }
    STAMP(19);
#pragma nounroll
    for (int it = 0; it < SPIN_CAP; ++it) {
      asm volatile("" ::: "memory");
      y[0] = LD_SC1(rs, WS_YX + a * 8192 + w * 2048 + lane * 16); y[1] = LD_SC1(rs, WS_YX + a * 8192 + w * 2048 + 1024 + lane * 16);
      if (wave_sum(sum4(sum4((u32)(mail >> 32), y[0]), y[1])) == 8u * tag) break;
      COUNT(12);
    }
    STAMP(8);
#pragma unroll
    for (int i = 0; i < 2; ++i)
#pragma unroll
      for (int g = 0; g < 4; ++g) *(LAS u32*)(lds + (16 * g + (lane & 15)) * 144 + (8 * w + 4 * i + (lane >> 4)) * 4) = y[i][g];
    BAR();
#pragma unroll
    for (int i = 0; i < 2; ++i) {
      const u32x4 v = *(LAS u32x4*)(lds + (32 * i + (tid >> 3)) * 144 + (tid & 7) * 16);
      u32x4 o;
#pragma unroll
      for (int k = 0; k < 4; ++k) o[k] = pkbf(bflo(v[k]) + bflo(sk[4 * i + k]), bfhi(v[k]) + bfhi(sk[4 * i + k]));
      *(GAS u32x4*)(out + (u32)((64 * jp + 32 * i + (tid >> 3)) * 1792 + cb * 128 + (tid & 7) * 16)) = o;
    }
  }
  STAMP(9);
  STAMP_FLUSH();
}

__global__ void NOPK __launch_bounds__(256, 2)
fused_kernel(const void*, const void*, const void*, const void*, const void*, const void*, const void*, const void*, const void*,
             const void*, void*, void*) {
  __shared__ __attribute__((aligned(16))) char smem[81920];
  LAS char* lds = (LAS char*)smem;
  if ((blockIdx.x >> 5) == 7) {
    u64 pc;
    asm volatile("s_getpc_b64 %0" : "=s"(pc));
    if (threadIdx.x < CODE_LINES / 4)
      __builtin_amdgcn_global_load_lds((const GAS void*)((pc & ~127ull) + (threadIdx.x * 4 + ((blockIdx.x >> 3) & 3)) * 128), (LAS void*)lds, 4, 0, 0);
  }
  const __attribute__((address_space(4))) u64* ka = (const __attribute__((address_space(4))) u64*)__builtin_amdgcn_kernarg_segment_ptr();
  asm volatile("" : "+s"(ka));
  const char* u = (const char*)ka[0]; const GAS u32* logA = (const GAS u32*)ka[1]; const GAS u32* Aim = (const GAS u32*)ka[2];
  const GAS u32* Cre = (const GAS u32*)ka[3]; const GAS u32* Cim = (const GAS u32*)ka[4]; const GAS u32* logdt = (const GAS u32*)ka[5];
  const char* Dsk = (const char*)ka[6]; const char* x = (const char*)ka[7]; const char* wqkv = (const char*)ka[8];
  const char* wo = (const char*)ka[9]; char* out = (char*)ka[10]; char* ws = (char*)ka[11];
  asm volatile("" :: "s"(u), "s"(logA), "s"(Aim), "s"(Cre), "s"(Cim), "s"(logdt), "s"(Dsk), "s"(x), "s"(wqkv), "s"(wo), "s"(out), "s"(ws));
  const u32 epoch = request_epoch(ws);
  u32 pA, pI, pR, pC, ldt;
  int bid = blockIdx.x;
  if (bid >= 256) role_b<true>(lds, x, wqkv, out, ws, epoch);
  else if (bid < 224) {
    const int pi = bid * 256 + threadIdx.x;
    pA = __builtin_nontemporal_load(logA + pi); pI = __builtin_nontemporal_load(Aim + pi);
    pR = __builtin_nontemporal_load(Cre + pi); pC = __builtin_nontemporal_load(Cim + pi); ldt = logdt[bid];
  }
  asm volatile("" : "+s"(bid) :: "memory");
  if (bid >= 256) role_b<false>(lds, x, wqkv, out, ws, epoch);
  else if (bid < 224) role_a(lds, u, Dsk, wqkv, wo, out, ws, epoch, pA, pI, pR, pC, ldt);
  else {
    const int t = threadIdx.x;
    if (blockIdx.x == 224) {
      const u32 tag = take_tag(epoch);
      __builtin_amdgcn_s_sleep(127);
      wait_flags(ws, 256 + t, tag);
      wait_flags(ws, t < 224 ? t : 0, tag);
      BAR();
      if (t == 0) __hip_atomic_store((GAS u32*)ws, tag, RLX_AGENT);
    }
  }
}

extern "C" void kernel_launch(void* const* d_in, const int* in_sizes, int n_in, void* d_out, int out_size, void* d_ws,
                              size_t ws_size, hipStream_t stream) {
  fused_kernel<<<512, 256, 0, stream>>>(d_in[0], d_in[1], d_in[2], d_in[3], d_in[4], d_in[5], d_in[6], d_in[7], d_in[8], d_in[9], d_out, d_ws);
}
```

```cpp
#define READOUT 0
#include <hip/hip_runtime.h>
#include <stdint.h>

typedef unsigned short u16;
typedef unsigned u32;
typedef unsigned long long u64;
typedef short s16x4 __attribute__((ext_vector_type(4)));
typedef __bf16 bf16x8 __attribute__((ext_vector_type(8)));
typedef __bf16 bf16x2 __attribute__((ext_vector_type(2)));
typedef _Float16 f16x8 __attribute__((ext_vector_type(8)));
typedef _Float16 f16x2 __attribute__((ext_vector_type(2)));
typedef float f32x16 __attribute__((ext_vector_type(16)));
typedef float f32x4 __attribute__((ext_vector_type(4)));
typedef float f32x2 __attribute__((ext_vector_type(2)));
typedef u32 u32x4 __attribute__((ext_vector_type(4)));
typedef u32 u32x2 __attribute__((ext_vector_type(2)));
#define LAS __attribute__((address_space(3)))
#define GAS __attribute__((address_space(1)))
#define DI __device__ __forceinline__
#if defined(__HIP_DEVICE_COMPILE__)
#define NOPK __attribute__((target("no-packed-fp32-ops")))
#else
#define NOPK
#endif
#define RLX_AGENT __ATOMIC_RELAXED, __HIP_MEMORY_SCOPE_AGENT

#define BMAP 1
#define PREFETCH 1
#define CODE_BEFORE 0
#define CODE_LINES 220
#define QEARLY 3
#define GATH_AT 18
#define WO_EARLY 0
#define WO_MID 12
#define WO_G 32
#define WO_E 8
#define KVSTEP 1
#define PUB_FIRST 0
#define UPOLL_AT 8
#define TMAP 1
#define BPRIO1 2
#define ABASE 0
#define ACONV 0
#define L2PF 0
#define NTA 1
#define NTX 0
#define W0FIRST 1
#define ORD 4
#define PF2AT 1
#define UMID 0
#define WFIRST 0
#define HEADC 1
#define ASLEEP 0
#define TDELAY 0
#define UEARLY 0
#define ROT 1
#define APRIO 3
#define ATAIL 1
#define BPRIO 2

constexpr u32 WS_FLAG = 4096;
constexpr u32 WS_MB1 = 73728;
constexpr u32 WS_MB2 = WS_MB1 + 32768;
constexpr u32 WS_MBU = WS_MB2 + 32768;
constexpr u32 WS_MBY = WS_MBU + 57344;
constexpr u32 WS_KVP = 1u << 18;
constexpr u32 WS_OB = WS_KVP + (1u << 20);
constexpr u32 WS_UX = WS_OB + (2u << 20);
constexpr u32 WS_YX = WS_UX + (2u << 20);
constexpr u32 WS_WQ = WS_YX + (2u << 20);
constexpr u32 WS_WO = WS_WQ + (1u << 19);
constexpr u32 WS_MBQ = WS_WO + 458752;
constexpr u32 WS_END = WS_WO + (1u << 19);
constexpr u32 WS_DBG = WS_END;
constexpr int SPIN_CAP = 1 << 15;

#define RK (READOUT & 31)
#define STAMP(i) do { if ((i) == 0) st0_ = __builtin_amdgcn_s_memrealtime(); else if ((i) == RK) stk_ = __builtin_amdgcn_s_memrealtime(); } while (0)
#define STAMP_DECL u64 st0_ = 0, stk_ = 0
#define COUNT(i) do {} while (0)
#define STAMP_FLUSH() do { if (lane == 0) { GAS u64* p_ = (GAS u64*)(ws + WS_DBG) + (blockIdx.x * 4 + w) * 2; __hip_atomic_store(p_, st0_, RLX_AGENT); __hip_atomic_store(p_ + 1, stk_, RLX_AGENT); } \
   asm volatile("s_waitcnt vmcnt(0)" ::: "memory"); BAR(); if (threadIdx.x == 0) __hip_atomic_store((GAS u32*)(ws + WS_FLAG) + blockIdx.x * 32 + 16, tag, RLX_AGENT); } while (0)

DI float bflo(u32 v) { return __uint_as_float(v << 16); }
DI float bfhi(u32 v) { return __uint_as_float(v & 0xffff0000u); }
DI u32 pkbf(float a, float b) { f32x2 p = {a, b}; return __builtin_bit_cast(u32, __builtin_convertvector(p, bf16x2)); }
DI u32 pkh(f32x2 p) { return __builtin_bit_cast(u32, __builtin_convertvector(p, f16x2)); }
DI float rbf(float a) { return bflo(pkbf(a, a)); }
DI u32 umax3(u32 a, u32 b, u32 c) { u32 m = a > b ? a : b; return m > c ? m : c; }
DI u32 sum4(u32 m, u32x4 v) { return (m + v.x + v.y) + v.z + v.w; }
DI u32 row_sum(u32 v) {
  v += __builtin_amdgcn_update_dpp(0u, v, 0x111, 0xf, 0xf, true); v += __builtin_amdgcn_update_dpp(0u, v, 0x112, 0xf, 0xf, true);
  v += __builtin_amdgcn_update_dpp(0u, v, 0x114, 0xf, 0xf, true); v += __builtin_amdgcn_update_dpp(0u, v, 0x118, 0xf, 0xf, true);
  return v;
}
DI u32 wave_sum(u32 v) {
  v = row_sum(v);
  return (__builtin_amdgcn_readlane(v, 15) + __builtin_amdgcn_readlane(v, 31)) + (__builtin_amdgcn_readlane(v, 47) + __builtin_amdgcn_readlane(v, 63));
}
DI u32 wave_umax(u32 v) {
  u32 t;
  t = __builtin_amdgcn_update_dpp(0u, v, 0x111, 0xf, 0xf, false); v = v > t ? v : t;
  t = __builtin_amdgcn_update_dpp(0u, v, 0x112, 0xf, 0xf, false); v = v > t ? v : t;
  t = __builtin_amdgcn_update_dpp(0u, v, 0x114, 0xf, 0xf, false); v = v > t ? v : t;
  t = __builtin_amdgcn_update_dpp(0u, v, 0x118, 0xf, 0xf, false); v = v > t ? v : t;
  const u32 a = __builtin_amdgcn_readlane(v, 15), b = __builtin_amdgcn_readlane(v, 31), c = __builtin_amdgcn_readlane(v, 47), d = __builtin_amdgcn_readlane(v, 63);
  const u32 ab = a > b ? a : b, cd = c > d ? c : d;
  return ab > cd ? ab : cd;
}

DI u32 request_epoch(const char* ws) { u32 e; asm volatile("s_load_dword %0, %1, 0x0" : "=s"(e) : "s"(ws) : "memory"); return e; }
DI u32 take_tag(u32 e) { u32 r; asm volatile("s_waitcnt lgkmcnt(0)\n\ts_and_b32 %0, %1, -2" : "=s"(r) : "s"(e) : "memory", "scc"); return r * 1664525u + 2027808447u; }
DI void put_flag(char* ws, u32 tag) { if (threadIdx.x == 0) __hip_atomic_store((GAS u32*)(ws + WS_FLAG) + blockIdx.x * 32, tag, RLX_AGENT); }
#define PSCOPE __HIP_MEMORY_SCOPE_AGENT
DI u32 poll32(char* ws, u32 off, bool on) { u32 z = 0, r = 0; asm volatile("" : "+v"(z)); if (on) r = __hip_atomic_fetch_add((GAS u32*)(ws + off), z, __ATOMIC_RELAXED, PSCOPE); return r; }
DI u64 poll64(char* ws, u32 off, bool on) { u64 z = 0, r = 0; asm volatile("" : "+v"(z)); if (on) r = __hip_atomic_fetch_add((GAS u64*)(ws + off), z, __ATOMIC_RELAXED, PSCOPE); return r; }
#define SBAR() __builtin_amdgcn_sched_barrier(0)
#define PRIO(n) do { SBAR(); __builtin_amdgcn_s_setprio(n); SBAR(); } while (0)
#define PIN(x) asm volatile("" : "+v"(x))
DI f32x16 mfma32(u32x4 a, u32x4 b, f32x16 c) { return __builtin_amdgcn_mfma_f32_32x32x16_bf16(__builtin_bit_cast(bf16x8, a), __builtin_bit_cast(bf16x8, b), c, 0, 0, 0); }
DI f32x16 mfma32h(u32x4 a, u32x4 b, f32x16 c) { return __builtin_amdgcn_mfma_f32_32x32x16_f16(__builtin_bit_cast(f16x8, a), __builtin_bit_cast(f16x8, b), c, 0, 0, 0); }
DI f32x4 mfma16h(u32x4 a, u32x4 b, f32x4 c) { return __builtin_amdgcn_mfma_f32_16x16x32_f16(__builtin_bit_cast(f16x8, a), __builtin_bit_cast(f16x8, b), c, 0, 0, 0); }
DI f32x4 mfma16(u32x4 a, u32x4 b, f32x4 c) { return __builtin_amdgcn_mfma_f32_16x16x32_bf16(__builtin_bit_cast(bf16x8, a), __builtin_bit_cast(bf16x8, b), c, 0, 0, 0); }
DI u32x2 tr_read(LAS char* p) { return __builtin_bit_cast(u32x2, __builtin_amdgcn_ds_read_tr16_b64_v4i16((LAS s16x4*)p)); }
#define RSRC(p, n) __builtin_amdgcn_make_buffer_rsrc((void*)(p), 0, (n), 0x00020000)
#define DMA16(rs, voff, soff, dst, aux) __builtin_amdgcn_raw_ptr_buffer_load_lds((rs), (LAS void*)(dst), 16, (voff), (soff), 0, (aux))
#define WAITVM(n) do { asm volatile("" ::: "memory"); __builtin_amdgcn_s_waitcnt(0x0F70 | ((n) & 15) | (((n) >> 4) << 14)); asm volatile("" ::: "memory"); } while (0)
#define VMCNT(n) asm volatile("s_waitcnt vmcnt(" #n ")" ::: "memory")
#define LGKM0() asm volatile("s_waitcnt lgkmcnt(0)" ::: "memory")
#define BAR() do { asm volatile("s_waitcnt lgkmcnt(0)" ::: "memory"); __builtin_amdgcn_s_barrier(); asm volatile("" ::: "memory"); } while (0)
#define ST_SC1(v, rs, off) __builtin_amdgcn_raw_buffer_store_b128((v), (rs), (off), 0, 16)
#define LD_SC1(rs, off) __builtin_amdgcn_raw_buffer_load_b128((rs), (off), 0, 16)
#define LDS_SC1(rs, off, soff) __builtin_amdgcn_raw_buffer_load_b128((rs), (off), (soff), 16)
#define ST1_SC1(v, rs, off) __builtin_amdgcn_raw_buffer_store_b32((v), (rs), (off), 0, 16)
#define LD1_SC1(rs, off) __builtin_amdgcn_raw_buffer_load_b32((rs), (off), 0, 16)

typedef unsigned short u16x2 __attribute__((ext_vector_type(2)));
DI u32 phi1(float a, float b) {
  const u32 t = pkbf(a, b);
  const f32x2 x = {bflo(t), bfhi(t)};
  const f32x2 y = x * 1.4426950408889634f;
  const float e0 = __builtin_amdgcn_fmed3f(__builtin_amdgcn_exp2f(y.x), 0.f, 1.f);
  const float e1 = __builtin_amdgcn_fmed3f(__builtin_amdgcn_exp2f(y.y), 0.f, 1.f);
  const f32x2 x1 = x + 1.0f;
  return pkbf(__builtin_fmaxf(x1.x, e0), __builtin_fmaxf(x1.y, e1));
}
DI void phi_eps(u32x4 (&p)[2]) {
  u16x2 m = {0xffff, 0xffff};
#pragma unroll
  for (int j = 0; j < 8; ++j) { const u32 e = p[j >> 2][j & 3]; m = __builtin_elementwise_min(m, __builtin_bit_cast(u16x2, e)); }
  if (__any(m.x < 0x3A80 || m.y < 0x3A80)) {
#pragma unroll
    for (int j = 0; j < 8; ++j) { const u32 r = p[j >> 2][j & 3]; p[j >> 2][j & 3] = pkbf(bflo(r) + 1e-6f, bfhi(r) + 1e-6f); }
  }
}

DI void wait_flags(const char* ws, int idx, u32 tag) {
  const GAS u32* f = (const GAS u32*)(ws + WS_FLAG) + idx * 32;
#pragma nounroll
  for (int it = 0; it < SPIN_CAP; ++it) {
    const u32 v = __hip_atomic_load(f, RLX_AGENT);
    if (__all(v == tag)) break;
    __builtin_amdgcn_s_sleep(16);
  }
}

template <bool HEAD>
DI void role_b(LAS char* lds, const char* __restrict__ x, const char* __restrict__ wqkv, char* __restrict__ out, char* ws, u32 epoch) {
  const int b = blockIdx.x - 256, tid = threadIdx.x, lane = tid & 63;
  const int w = __builtin_amdgcn_readfirstlane(tid >> 6);
  STAMP_DECL;
  STAMP(0);
  const int rb = 4 * ((b & 7) >> 1) + (b >> 6), h = (b & 1) * 8 + ((b >> 3) & 7);
  const int start = (int)((0x1065432106543210ull >> (4 * ((h & 7) + 2 * (rb & 3)))) & 7);
  const __amdgpu_buffer_rsrc_t rs = RSRC(ws, WS_END), rx = RSRC(x, 2048 * 896), rw = RSRC(wqkv, 448 * 2688);
  constexpr int WR = 49152, KVI = WR + 4096, OST = 16384, OTR = WR;
#define XS(i) ((i) * 16384)
#define QS(cc) ((cc) < 2 ? WR + 24576 + (cc) * 4096 : (cc) < 6 ? XS(1) + ((cc) - 2) * 1024 : WR)
#define QD(cc) ((cc) >= 2 && (cc) < 6 ? 4096 : 1024)
  const int wsb = (56 * h) & ~15;
  const int coff = (h & 1) * 4;
  const int wone = (h & 1) ? 0 : 28;
  const u32 xv0 = (128 * rb + 32 * w + (lane >> 3)) * 896 + (((lane & 7) ^ (lane >> 4)) << 4);
  const u32 xv1 = (xv0 ^ 64) + 8 * 896;
  const u32 wv = (16 * w + (lane >> 2)) * 2688 + wsb + (lane & 3) * 16;
  const u32 l16 = lane * 16;
  int ch_[7];
  auto chunk = [&](int cc) { return ch_[cc]; };
  const int gi = h + rb;
  const u32 ordc = ORD == 1 ? 0x6543210u : ORD == 2 ? ((gi & 1) ? 0x5461032u : 0x6543210u) : ORD == 3 ? ((gi & 1) ? 0x6452301u : 0x6543210u)
                 : ((gi & 2) ? ((gi & 1) ? 0x4652103u : 0x5461032u) : ((gi & 1) ? 0x4650321u : 0x6543210u));
  auto next_chunk = [&](int cc) { int c = (ordc >> (4 * cc)) & 7; asm volatile("" : "+s"(c)); ch_[cc] = c; };
  auto issue_x = [&](int cc, int i) { DMA16(rx, (i & 1) ? xv1 : xv0, chunk(cc) * 128 + (i >> 1) * (16 * 896), lds + XS(cc % 3) + w * 4096 + i * 1024, NTX); };
  auto issue_w = [&](int cc, int i) { DMA16(rw, wv, chunk(cc) * (64 * 2688) + 896 * (i + 1), lds + WR + (cc & 3) * 8192 + i * 4096 + w * 1024, 0); };
  bool qok = true;
  auto issue_q = [&](int cc) { DMA16(rs, l16, qok ? WS_WQ + (h * 28 + 4 * chunk(cc) + w) * 1024 : WS_END, lds + QS(cc) + w * QD(cc), 16); };
#pragma unroll
  for (int cc = 0; cc < 2; ++cc) {
    next_chunk(cc);
    if (HEAD == (cc < HEADC)) { issue_w(cc, 0); issue_w(cc, 1); issue_x(cc, 0); issue_x(cc, 1); issue_x(cc, 2); issue_x(cc, 3); }
    SBAR();
  }
  if (HEAD) return;
  next_chunk(2); next_chunk(3); next_chunk(4); next_chunk(5); next_chunk(6);
  __builtin_amdgcn_s_setprio(BPRIO1);
  const int r32 = lane & 31, half = lane >> 5;
  const int xrd = w * 4096 + r32 * 128 + ((half ^ ((r32 >> 1) & 7)) << 4);
  LAS char* whi = lds + WR + (8 * half + ((lane >> 2) & 3)) * 64 + ((lane >> 4) & 1) * 32 + (lane & 3) * 8;
  PIN(whi);
  const int rt = h >> 2, ct = (h + ((h & 8) ? 2 : 1)) & 3;
  u32 wo_src = WS_WO + (4 * ct + w < 14 ? 4 * ct + w : 13) * 32768, kvp_src = WS_KVP + h * 65536 + w * 16384;
  u32 ob_dst = WS_OB + ((rb * 4 + w) * 16 + h) * 2048, ob_src = WS_OB + ((rb * 4 + rt) * 16 + 4 * w) * 2048;
  asm volatile("" : "+s"(wo_src), "+s"(kvp_src), "+s"(ob_dst), "+s"(ob_src));
  u32 tell1 = lane < 4 ? WS_MB1 + (h * 16 + 4 * w + lane) * 128 + rb * 4 : tid == 4 ? WS_FLAG + blockIdx.x * 128 : WS_END;
  u32 tell2 = lane < 4 ? WS_MB2 + (rb * 16 + 4 * w + lane) * 128 + h * 4 : WS_END;
  u32 mb1 = WS_MB1 + (h * 16 + rb) * 128 + ((lane + rb) & 15) * 4, mb2 = WS_MB2 + (rb * 16 + h) * 128 + ((4 * w + lane) & 15) * 4;
  u32 kvp_dst = kvp_src + rb * 1024 + l16, kvs[16];
#pragma unroll
  for (int p = 1; p < 16; ++p) { kvs[p] = kvp_src + ((p + rb) & 15) * 1024; asm volatile("" : "+s"(kvs[p])); }
  const int dw = 8 * w + 4 * half - coff;
  u32 rowmask = dw < 0 || dw >= 28 ? 0u : ~0u, padlo = (h & 1) && !half ? 0u : ~0u, padhi = !(h & 1) && half ? 0u : ~0u;
  const int oi = (r32 & 3) + 4 * (r32 >> 3);
  LAS char* kvrd = lds + KVI + ((oi < 14 ? 14 * ((r32 >> 2) & 1) + oi + coff : wone) + 32 * half) * 32;
  LAS char* qlo = lds + l16; LAS char* qhi = lds + WR + l16;
  PIN(tell1); PIN(tell2); PIN(mb1); PIN(mb2); PIN(kvp_dst); PIN(rowmask); PIN(padlo); PIN(padhi); PIN(kvrd); PIN(qhi);
  const bool ck = lane == wone + 1;
  u32 mbq = WS_MBQ + (h * 16 + rb) * 128 + (4 * (lane & 7) + w) * 4, mail = 0, tag = 0;
  PIN(mbq);
  u32x4 xf[28], wf[2][8];
  f32x16 ak = {}, av = {};
  auto rd = [&](int cc, int i) {
    if ((i & 1) == 0) xf[cc * 4 + (i >> 1)] = *(LAS u32x4*)(lds + XS(cc % 3) + (xrd ^ ((i >> 1) << 5)));
    LAS char* p = whi + (cc & 3) * 8192 + (i >> 1) * 1024 + (i & 1) * 4096;
    const u32x2 lo = tr_read(p), hi = tr_read(p + 256);
    wf[cc & 1][i] = (u32x4){lo.x, lo.y, hi.x, hi.y};
  };
#pragma unroll
  for (int cc = 0; cc < 8; ++cc) {
    if (cc < 7) {
      if (cc == 0) VMCNT(6); else if (cc < 5) VMCNT(8); else if (cc == 5) VMCNT(7); else VMCNT(1);
      BAR();
      if (cc == 5) __builtin_amdgcn_s_setprio(BPRIO);
      if (cc == 6) tag = take_tag(epoch);
      if (cc == 0) { STAMP(1); }
      if (cc == 2) { STAMP(23); } if (cc == 4) { STAMP(24); } if (cc == 5) { STAMP(25); } if (cc == 6) { STAMP(26); }
      if (cc >= 5) {
#pragma unroll
        for (int i = 0; i < 8; ++i) rd(cc, i);
        SBAR();
      }
#pragma unroll
      for (int i = 0; i < 8; ++i) {
        if (cc > 0) {
          if (i & 1) av = mfma32(xf[(cc - 1) * 4 + (i >> 1)], wf[(cc - 1) & 1][i], av);
          else ak = mfma32(xf[(cc - 1) * 4 + (i >> 1)], wf[(cc - 1) & 1][i], ak);
        }
        if (cc < 5) rd(cc, i);
        if (cc == 0) { if (i < 2) issue_w(2, i); else if (i < 6) issue_x(2, i - 2); else issue_w(3, i - 6); }
        else if (cc <= 4 && i < 4) issue_x(cc + 2, i);
        else if (cc <= 3 && i < 6) issue_w(cc + 3, i - 4);
        else if (cc == 4 && i == 4) mail = poll32(ws, mbq, lane < 7);
        else if (cc == 6 && i >= 4) {
          if (i == 4) { WAITVM(0); qok = __ballot(mail == tag) == 0x7full; }
          issue_q(2 * (i - 4)); if (i < 7) issue_q(2 * (i - 4) + 1);
        }
        SBAR();
      }
#pragma unroll
      for (int s = 3; s >= 0; --s) { PIN(wf[cc & 1][2 * s + 1]); PIN(wf[cc & 1][2 * s]); PIN(xf[cc * 4 + s]); }
      if (cc > 0) { PIN(ak); PIN(av); }
    } else {
#pragma unroll
      for (int s = 0; s < 4; ++s) ak = mfma32(xf[24 + s], wf[0][2 * s], ak);
#pragma unroll
      for (int s = 0; s < 4; ++s) av = mfma32(xf[24 + s], wf[0][2 * s + 1], av);
      SBAR();
    }
  }
  STAMP(2);
  u32x4 wof[32], d[16], pq[2];
  auto load_wo = [&](int k) { wof[k] = __builtin_amdgcn_raw_buffer_load_b128(rs, l16 + (k & 3) * 1024, wo_src + (k >> 2) * 4096, 16); };
  {
    u32x4 pk[2], pv[2];
#pragma unroll
    for (int j = 0; j < 8; ++j) {
      pk[j >> 2][j & 3] = phi1(ak[2 * j], ak[2 * j + 1]);
      if (j < WO_E) { PIN(pk[j >> 2][j & 3]); load_wo(j); SBAR(); }
    }
    phi_eps(pk);
    STAMP(11);
#pragma unroll
    for (int j = 0; j < 8; ++j) { const u32 v = pkbf(av[2 * j], av[2 * j + 1]); pv[j >> 2][j & 3] = r32 == wone ? 0x3F803F80u : v; }
    f32x16 kv = {};
    kv = mfma32(pk[0], pv[0], kv); kv = mfma32(pk[1], pv[1], kv);
    if (!qok) {
#pragma nounroll
      for (int it = 0; it < SPIN_CAP && __ballot(poll32(ws, mbq, lane < 7) == tag) != 0x7full; ++it) {}
      qok = true;
#pragma unroll
      for (int cc = 0; cc < 7; ++cc) issue_q(cc);
      WAITVM(0);
      COUNT(13);
    }
    WAITVM(WO_E);
    ST1_SC1(tag, rs, tell1);
#pragma unroll
    for (int g = 0; g < 4; ++g) *(LAS f32x4*)(lds + XS(2) + w * 4096 + g * 1024 + l16) = (f32x4){kv[4 * g], kv[4 * g + 1], kv[4 * g + 2], kv[4 * g + 3]};
    STAMP(12);
    BAR();
    f32x4 s4 = *(LAS f32x4*)(lds + XS(2) + tid * 16);
#pragma unroll
    for (int ww = 1; ww < 4; ++ww) s4 += *(LAS f32x4*)(lds + XS(2) + ww * 4096 + tid * 16);
    u32x4 v = __builtin_bit_cast(u32x4, s4);
    const u32 part = v.y + v.z + v.w;
    const u32 c = tag - wave_sum(ck ? part : part + v.x);
    if (ck) v.x = c;
    ST_SC1(v, rs, kvp_dst);
    d[0] = v;
  }
  u32 go, need = 0xfffeu;
  mail = poll32(ws, mb1, lane < 16);
  STAMP(3);
#define QP(cc) (QS(cc) >= WR ? qhi + (QS(cc) - WR) : qlo + QS(cc))
  auto load_kv = [&](int p, bool on = true) { d[p] = __builtin_amdgcn_raw_buffer_load_b128(rs, l16, on ? kvs[p] : WS_END, 16); };
  {
    f32x16 aq = {};
    u32x4 qf[2][4];
    u32 qsum = 0;
    auto read_q = [&](int cc) {
#pragma unroll
      for (int s = 0; s < 4; ++s) qf[cc & 1][s] = *(LAS u32x4*)(QP(cc) + s * QD(cc));
      SBAR();
    };
    read_q(0);
#pragma unroll
    for (int cc = 0; cc < 7; ++cc) {
      if (cc < 6) read_q(cc + 1);
#pragma unroll
      for (int s = 0; s < 4; ++s) {
        aq = mfma32(qf[cc & 1][s], xf[cc * 4 + s], aq);
        qsum = sum4(qsum, qf[cc & 1][s]);
        if (s & 1) load_wo(WO_E + 2 * cc + (s >> 1));
        PIN(aq); PIN(qsum);
        SBAR();
      }
    }
    qok = wave_sum(qsum) == 28u * tag;
    STAMP(14);
    go = need & (u32)__ballot(mail == tag); need &= ~go;
    mail = poll32(ws, mb1, lane < 16 && need != 0);
    STAMP(15);
#pragma unroll
    for (int j = 0; j < 8; ++j) {
      if (j) load_kv(2 * j, go >> (2 * j) & 1);
      load_kv(2 * j + 1, go >> (2 * j + 1) & 1);
      pq[j >> 2][j & 3] = phi1(aq[2 * j], aq[2 * j + 1]);
      PIN(pq[j >> 2][j & 3]);
      SBAR();
    }
    phi_eps(pq);
  }
  STAMP(5);
  PIN(mail);
  STAMP(16);
#pragma nounroll
  for (int i2 = 0; i2 < SPIN_CAP && need != 0; ++i2) {
    go = need & (u32)__ballot(mail == tag); need &= ~go;
#pragma unroll
    for (int p = 1; p < 16; ++p) if (go >> p & 1) load_kv(p);
    if (need != 0) { mail = poll32(ws, mb1, lane < 16); WAITVM(0); }
    COUNT(14);
  }
#pragma unroll
  for (int k = WO_E + 14; k < WO_G; ++k) load_wo(k);
  WAITVM(WO_G - WO_E - 14);
  STAMP(18);
#pragma nounroll
  for (int it = 0; it < SPIN_CAP; ++it) {
    u32 m = 0;
#pragma unroll
    for (int p = 0; p < 16; ++p) m = sum4(m, d[p]);
    if (wave_sum(m) == 16u * tag) break;
    asm volatile("" ::: "memory");
#pragma unroll
    for (int p = 1; p < 16; ++p) load_kv(p);
    WAITVM(0);
    COUNT(11);
  }
  mail = poll32(ws, mb2, lane < 4);
  SBAR();
  {
    f32x4 s4 = __builtin_bit_cast(f32x4, d[0]);
#pragma unroll
    for (int p = 1; p < 16; ++p) s4 += __builtin_bit_cast(f32x4, d[p]);
    *(LAS u32x2*)(lds + KVI + lane * 32 + w * 8) = (u32x2){pkbf(s4.x, s4.y) & rowmask, pkbf(s4.z, s4.w) & rowmask};
  }
  STAMP(6);
  BAR();
  {
    const u32x4 a0 = *(LAS u32x4*)kvrd, a1 = *(LAS u32x4*)(kvrd + 16);
    if (!qok) {
      f32x16 aq = {};
#pragma nounroll
      for (int k2 = 0; k2 < 28; ++k2) {
        const int ks = 4 * ((ordc >> (k2 & 28)) & 7) + (k2 & 3);
        u32x4 f;
#pragma nounroll
        for (int it = 0; it < SPIN_CAP; ++it) {
          asm volatile("" ::: "memory");
          f = LDS_SC1(rs, l16, WS_WQ + (h * 28 + ks) * 1024);
          if (wave_sum(sum4(0u, f)) == tag) break;
        }
        aq = mfma32(f, __builtin_amdgcn_raw_buffer_load_b128(rx, (128 * rb + 32 * w + r32) * 896 + half * 16, ks * 32, 0), aq);
      }
#pragma unroll
      for (int j = 0; j < 8; ++j) pq[j >> 2][j & 3] = phi1(aq[2 * j], aq[2 * j + 1]);
      phi_eps(pq);
      COUNT(15);
    }
    ST1_SC1(tag, rs, tell2);
    pq[0][0] &= padlo; pq[0][1] &= padlo; pq[1][2] &= padhi; pq[1][3] &= padhi;
    f32x16 nm = {};
    nm = mfma32(a0, pq[0], nm); nm = mfma32(a1, pq[1], nm);
    const float den = rbf(rbf(nm[14]) + 1e-6f);
    const float rc = __builtin_amdgcn_rcpf(den);
    u32 o[8];
#pragma unroll
    for (int j = 0; j < 7; ++j) { const u32 t = pkbf(nm[2 * j], nm[2 * j + 1]); o[j] = pkbf(bflo(t) * rc, bfhi(t) * rc); }
    o[7] = tag - ((o[0] + o[1] + o[2]) + (o[3] + o[4] + o[5]) + o[6]);
    __builtin_amdgcn_raw_buffer_store_b128((u32x4){o[0], o[1], o[2], o[3]}, rs, l16, ob_dst, 16);
    __builtin_amdgcn_raw_buffer_store_b128((u32x4){o[4], o[5], o[6], o[7]}, rs, l16 + 1024, ob_dst, 16);
    SBAR();
  }
  STAMP(7);
  u32x4 g[8];
  auto load_o = [&](int i, bool on = true) {
    const u32 so = on ? ob_src + (i >> 1) * 4096 : WS_END;
    g[2 * i] = __builtin_amdgcn_raw_buffer_load_b128(rs, l16 + (i & 1) * 2048, so, 16);
    g[2 * i + 1] = __builtin_amdgcn_raw_buffer_load_b128(rs, l16 + (i & 1) * 2048 + 1024, so, 16);
  };
  WAITVM(3);
  go = (u32)__ballot(mail == tag); need = 15u & ~go;
  mail = poll32(ws, mb2, lane < 4 && need != 0);
#pragma unroll
  for (int i = 0; i < 4; ++i) load_o(i, go >> i & 1);
  SBAR();
  STAMP(19);
#pragma nounroll
  for (int it = 0; it < SPIN_CAP; ++it) {
    u32 m = 0;
#pragma unroll
    for (int k = 0; k < 32; ++k) m = sum4(m, wof[k]);
    if (__all(m == 16u * tag)) break;
    asm volatile("" ::: "memory");
#pragma unroll
    for (int k = 0; k < 32; ++k) load_wo(k);
    WAITVM(0);
  }
#pragma unroll
  for (int k = 1; k < 32; k += 2) { wof[k].w = 0u; PIN(wof[k]); }
  STAMP(20);
#pragma nounroll
  for (int it = 0; it < SPIN_CAP; ++it) {
#pragma nounroll
    for (int i2 = 0; i2 < SPIN_CAP && need != 0; ++i2) {
      go = need & (u32)__ballot(mail == tag); need &= ~go;
      if (need != 0) mail = poll32(ws, mb2, lane < 4);
#pragma unroll
      for (int i = 0; i < 4; ++i) if (go >> i & 1) load_o(i);
      COUNT(12);
    }
#pragma unroll
    for (int i = 0; i < 4; ++i) need |= (u32)!__all(sum4(sum4(0u, g[2 * i]), g[2 * i + 1]) == tag) << i;
    if (need == 0) break;
    mail = tag;
    asm volatile("" ::: "memory");
    COUNT(13);
  }
#pragma unroll
  for (int p = 0; p < 8; ++p) {
    if (p & 1) g[p].w = 0u;
    *(LAS u32x4*)(lds + OST + (8 * w + p) * 1024 + l16) = g[p];
  }
  STAMP(8);
  BAR();
  STAMP(9);
  f32x16 acc = {};
  {
    u32x4 of[32];
#pragma unroll
    for (int k = 0; k < 6; ++k) of[k] = *(LAS u32x4*)(lds + OST + k * 1024 + l16);
    SBAR();
#pragma unroll
    for (int k = 0; k < 32; ++k) {
      acc = mfma32(wof[k], of[k], acc);
      if (k + 6 < 32) of[k + 6] = *(LAS u32x4*)(lds + OST + (k + 6) * 1024 + l16);
      SBAR();
    }
  }
  STAMP(22);
  {
    LAS char* p = lds + OTR + r32 * 272 + w * 64 + half * 32;
    *(LAS u32x4*)p = (u32x4){pkbf(acc[0], acc[1]), pkbf(acc[2], acc[3]), pkbf(acc[4], acc[5]), pkbf(acc[6], acc[7])};
    *(LAS u32x4*)(p + 16) = (u32x4){pkbf(acc[8], acc[9]), pkbf(acc[10], acc[11]), pkbf(acc[12], acc[13]), pkbf(acc[14], acc[15])};
  }
  BAR();
  if (ct < 3 || (tid & 15) < 8) {
#pragma unroll
    for (int i = 0; i < 2; ++i)
      *(GAS u32x4*)(out + (u32)((128 * rb + 32 * rt + 16 * i + (tid >> 4)) * 1792 + 896 + ct * 256 + (tid & 15) * 16)) =
          *(LAS u32x4*)(lds + OTR + (16 * i + (tid >> 4)) * 272 + (tid & 15) * 16);
  }
  STAMP(10);
  STAMP_FLUSH();
}

DI u32 hmul1(u32 a, u32 b) { const f16x2 x = __builtin_bit_cast(f16x2, a), y = __builtin_bit_cast(f16x2, b); return __builtin_bit_cast(u32, (f16x2){x.x, x.x} * y); }
DI u32 hmul2(u32 a, u32 b, u32 t) {
  const f16x2 x = __builtin_bit_cast(f16x2, a), y = __builtin_bit_cast(f16x2, b);
  return __builtin_bit_cast(u32, __builtin_elementwise_fma((f16x2){x.y, x.y}, (f16x2){-y.y, y.x}, __builtin_bit_cast(f16x2, t)));
}
DI f32x2 rot(f32x2 b) { return (f32x2){-b.y, b.x}; }
DI f32x2 cm1(f32x2 a, f32x2 b) { return (f32x2){a.x, a.x} * b; }
DI f32x2 cm2(f32x2 a, f32x2 br, f32x2 t) { return __builtin_elementwise_fma((f32x2){a.y, a.y}, br, t); }
DI f32x2 cexp_p(float a2, float f, float p) {
  const float m = __builtin_amdgcn_exp2f(a2 * p), ph = __builtin_amdgcn_fractf(f * p);
  return (f32x2){m, m} * (f32x2){__builtin_amdgcn_cosf(ph), __builtin_amdgcn_sinf(ph)};
}
DI void role_a(LAS char* lds, const char* __restrict__ u, const char* __restrict__ Dsk, const char* __restrict__ wqkv, const char* __restrict__ wo, char* __restrict__ out, char* ws, u32 epoch,
               u32 pA, u32 pI, u32 pR, u32 pC, u32 ldt) {
  const int a = blockIdx.x, cb = a >> 5, jp = a & 31, tid = threadIdx.x, lane = tid & 63;
  const int w = __builtin_amdgcn_readfirstlane(tid >> 6);
  STAMP_DECL;
  STAMP(0);
  __builtin_amdgcn_s_setprio(APRIO);
  constexpr int SROW = 528, STG = 32 * SROW, UC = 0, UCS = 4160, RV = 36864, RVS = 4608, EX = RV + 2 * RVS, UIM = 4 * STG;
  const int PKS = UIM + 8192 + (w ? 1024 + w * 1024 : 0);
  const __amdgpu_buffer_rsrc_t rs = RSRC(ws, WS_END);
  const int ch = w >> 1, hf = w & 1, d = 2 * a + ch;
  const int r32 = lane & 31, half = lane >> 5;
  const int pkh_ = w == 0 ? (a & 15) : 2 * (a & 7) + (w - 1);
  {
    const __amdgpu_buffer_rsrc_t rp = RSRC(w == 0 ? wo : wqkv, w == 0 ? 448 * 896 : w < 3 ? 448 * 2688 : 0);
    const u32 vo = (lane >> 2) * (w == 0 ? 896 : 2688) + (lane & 3) * 16;
    const u32 so = w == 0 ? 28 * 896 * pkh_ + (a >> 4) * 64 : 16 * 2688 * (a >> 3) + ((56 * pkh_) & ~15);
    DMA16(rp, vo, so, lds + PKS, 0);
    DMA16(rp, vo, w == 0 ? so + 16 * 896 : 448 * 2688, lds + (w == 0 ? PKS + 1024 : UIM + 8192 + 5120), 0);
  }
  u32x4 dk;
  auto load_tile = [&]() {
#pragma unroll
    for (int i = 0; i < 2; ++i)
      DMA16(RSRC(u, 2048 * 896), (tid >> 3) * 896 + (((tid & 7) ^ ((tid >> 5) & 7)) << 4), (64 * jp + 32 * i) * 896 + cb * 128, lds + UIM + (32 * i + 8 * w) * 128, NTA ? 2 : 0);
    dk = *(const GAS u32x4*)(Dsk + cb * 128 + (tid & 7) * 16);
  };
  if (UEARLY == 1) load_tile();
  PRIO(ABASE);
  int tix[8];
#pragma unroll
  for (int j = 0; j < 8; ++j) {
    const int p = (int)rbf((float)(2047 - 8 * tid - j));
    const int low = p & 7, blk = p >> 8;
    const int k4 = (blk < 4 ? blk : 4) * 4;
    const int sh = (0x32210 >> k4) & 15;
    const int base = blk + ((0xCBA70 >> k4) & 15);
    tix[j] = ((base + (low >> sh)) * 32 + ((p >> 3) & 31)) * 4;
    PIN(tix[j]);
  }
  const u32 tag = take_tag(epoch);
  auto publish_weights = [&]() {
  PRIO(APRIO);
  if (w == 0) {
    const int q = (lane >> 2) & 3, gam = 4 * ((lane >> 4) & 1) + (lane & 3), ng = 4 * (gam & 1) + (gam >> 1);
    LAS char* p = lds + PKS + (14 * half + q) * 64 + 8 * ng;
    const u32x2 r0 = tr_read(p), r1 = tr_read(p + 256), r2 = tr_read(p + 512), r3 = tr_read(p + 768);
    const u32 off = WS_WO + ((a >> 4) * 16 + pkh_) * 2048 + lane * 16;
    ST_SC1(((u32x4){r0.x, r0.y, r1.x, r1.y}), rs, off);
    ST_SC1(((u32x4){r2.x, r2.y, r3.x, tag - ((r0.x + r0.y + r1.x) + (r1.y + r2.x + r2.y) + r3.x)}), rs, off + 1024);
  } else if (w < 3) {
    LAS char* p = lds + PKS + (8 * half + ((lane >> 2) & 3)) * 64 + ((lane >> 4) & 1) * 32 + (lane & 3) * 8;
    const u32x2 r0 = tr_read(p), r1 = tr_read(p + 256);
    u32x4 v = {r0.x, r0.y, r1.x, r1.y};
    const bool ck = lane == ((pkh_ & 1) ? 1 : 29);
    if (ck) v = (u32x4){0u, 0u, 0u, 0u};
    const u32 c = tag - wave_sum(sum4(0u, v));
    if (ck) v.x = c;
    ST_SC1(v, rs, WS_WQ + (pkh_ * 28 + (a >> 3)) * 1024 + lane * 16);
    if (lane < 16) ST1_SC1(tag, rs, WS_MBQ + (pkh_ * 16 + lane) * 128 + (a >> 3) * 4);
  }
  PRIO(ABASE);
  };
  auto publish_u = [&]() {
  PRIO(APRIO);
  BAR();
#pragma unroll
  for (int i = 0; i < 2; ++i) {
    const int jj = 16 * i + (tid >> 4), n = tid & 15;
    LAS char* p = lds + UIM + 4 * n * 128 + (((jj >> 2) ^ (n & 7)) << 4) + (jj & 3) * 4;
    const u32x4 v = {*(LAS u32*)p, *(LAS u32*)(p + 128), *(LAS u32*)(p + 256), *(LAS u32*)(p + 384)};
    ST_SC1(v, rs, WS_UX + (cb * 32 + jj) * 8192 + jp * 256 + n * 16);
    const u32 ck = tag - row_sum(sum4(0u, v));
    __builtin_amdgcn_raw_buffer_store_b64((u32x2){tag, ck}, rs, n == 15 ? WS_MBU + (cb * 32 + jj) * 256 + jp * 8 : tid + i == 0 ? WS_FLAG + a * 128 : WS_END, 0, 16);
  }
  PRIO(ABASE);
  };
  if (WFIRST) { WAITVM(0); publish_weights(); if (UEARLY == 1) publish_u(); } else if (UEARLY == 1) WAITVM(5); else WAITVM(2);
  PIN(pA); PIN(pI); PIN(pR); PIN(pC); PIN(ldt);
  if (UEARLY == 2) { PRIO(APRIO); load_tile(); PRIO(ABASE); }
  STAMP(1);
  const float dt = rbf(expf(ch ? bfhi(ldt) : bflo(ldt)));
  const u32 mx = wave_umax(umax3(umax3(pR & 0x7fffu, (pR >> 16) & 0x7fffu, pC & 0x7fffu), (pC >> 16) & 0x7fffu, 0u));
  int ex = mx >> 7; ex = ex < 13 ? 13 : ex > 254 ? 254 : ex;
  const float sc = __uint_as_float((u32)(266 - ex) << 23), isc = __uint_as_float((u32)(ex - 12) << 23);
  LAS char* stg = lds + w * STG;
  u32x4 ud[3];
  const __amdgpu_buffer_rsrc_t ru = RSRC(ws + WS_UX + a * 8192, 8192);
  const int npc = w < 3 ? 9 : 8;
  u64 mail = 0;
  const u32 mbu = WS_MBU + a * 256 + (8 * w + lane) * 8;
  auto load_u = [&]() {
    PRIO(APRIO);
    ud[0] = LD_SC1(ru, w * 2048 + lane * 16); ud[1] = LD_SC1(ru, w * 2048 + 1024 + lane * 16);
    ud[2] = LD_SC1(ru, lane < 16 ? w * 2048 + 2048 + lane * 16 : 8192);
    PRIO(ABASE);
  };
  bool have_u = false;
  auto try_u = [&]() {
    if (!have_u) { if (__popcll(__ballot((u32)mail == tag)) == npc) { load_u(); have_u = true; } else { PRIO(APRIO); mail = poll64(ws, mbu, lane < npc); PRIO(ABASE); } }
  };
  {
    f32x2 y1[2], yq[2], z8[2], z32[2], p[2], q[2], c2[2], c4[2], c6[2];
#pragma unroll
    for (int m = 0; m < 2; ++m) {
      const float la = m ? bfhi(pA) : bflo(pA), ai = m ? bfhi(pI) : bflo(pI), cr = m ? bfhi(pR) : bflo(pR), ci = m ? bfhi(pC) : bflo(pC);
      const float a2 = -rbf(expf(la)) * dt * 1.4426950408889634f, f = ai * dt * 0.15915494309189535f;
      y1[m] = cexp_p(a2, -f, 1.f); yq[m] = cexp_p(a2, -f, 256.f); z8[m] = cexp_p(a2, f, 8.f);
      p[m] = q[m] = (f32x2){cr, -ci} * sc;
    }
    LAS char* sw = stg + lane * 8;
#define PUT(r, v) *(LAS u32x2*)(sw + (r) * SROW) = (u32x2){pkh(v[0]), pkh(v[1])}
#define MUL(v, b) do { v[0] = cm2(v[0], rot(b[0]), cm1(v[0], b[0])); v[1] = cm2(v[1], rot(b[1]), cm1(v[1], b[1])); } while (0)
#define CPY(dst, src) do { dst[0] = src[0]; dst[1] = src[1]; } while (0)
    SBAR();
    PUT(0, p); *(LAS u32x2*)(sw + 21 * SROW) = (u32x2){0x3C00u, 0x3C00u};
    MUL(p, y1); MUL(q, yq); PUT(1, p); PUT(8, q); SBAR();
    MUL(p, y1); MUL(q, yq); PUT(2, p); PUT(12, q); CPY(c2, p); SBAR();
    MUL(p, y1); MUL(q, yq); PUT(3, p); PUT(14, q); SBAR();
    MUL(p, y1); MUL(q, yq); PUT(4, p); PUT(16, q); CPY(c4, p); SBAR();
    MUL(p, y1); MUL(q, yq); PUT(5, p); PUT(17, q); SBAR();
    MUL(p, y1); MUL(q, yq); PUT(6, p); PUT(18, q); CPY(c6, p); SBAR();
    MUL(p, y1); MUL(q, yq); PUT(7, p); PUT(19, q); SBAR();
    if (WFIRST && UEARLY == 1) { PRIO(APRIO); mail = poll64(ws, mbu, lane < npc); PRIO(ABASE); }
    MUL(c4, yq); MUL(q, yq); PUT(10, c4); PUT(20, q); SBAR();
    MUL(c4, yq); MUL(c2, yq); PUT(13, c4); PUT(9, c2); SBAR();
    MUL(c4, yq); MUL(c6, yq); PUT(15, c4); PUT(11, c6); SBAR();
    CPY(p, z8); PUT(22, p); MUL(p, z8); PUT(23, p); SBAR();
    MUL(p, z8); PUT(24, p); MUL(p, z8); PUT(25, p); CPY(z32, p); SBAR();
    MUL(p, z32); PUT(26, p); MUL(p, z32); PUT(27, p); SBAR();
    MUL(p, z32); PUT(28, p); MUL(p, z32); PUT(29, p); SBAR();
    MUL(p, z32); PUT(30, p); MUL(p, z32); PUT(31, p); SBAR();
#undef PUT
#undef MUL
#undef CPY
  }
  STAMP(2);
  if (!WFIRST) { WAITVM(0); publish_weights(); if (UEARLY) publish_u(); } else if (UEARLY == 1) try_u();
  STAMP(13);
  {
    const int c16 = lane & 15, quad = lane >> 4;
    LAS char* py0 = stg + c16 * SROW + quad * 16;
    LAS char* py1 = stg + (16 + (c16 < 4 ? c16 : 4)) * SROW + quad * 16;
    LAS char* pt1 = stg + (21 + (c16 & 3)) * SROW + quad * 16;
    LAS char* pt2 = stg + (c16 < 4 ? 21 : 24 + (c16 >> 2)) * SROW + quad * 16;
    LAS char* pt3 = stg + (28 + (c16 >> 2)) * SROW + quad * 16;
    f32x4 ka[4] = {};
    u32x4 fr[2][5];
    auto rdk = [&](int s) {
      fr[s & 1][0] = *(LAS u32x4*)(pt1 + s * 64); fr[s & 1][1] = *(LAS u32x4*)(pt2 + s * 64); fr[s & 1][2] = *(LAS u32x4*)(pt3 + s * 64);
      fr[s & 1][3] = *(LAS u32x4*)(py0 + s * 64); fr[s & 1][4] = *(LAS u32x4*)(py1 + s * 64);
    };
    rdk(0);
#pragma unroll
    for (int s = 0; s < 8; ++s) {
      if (s < 7) rdk(s + 1);
      SBAR();
      u32x4 x0, x1;
#pragma unroll
      for (int i = 0; i < 4; ++i) { x0[i] = hmul1(fr[s & 1][0][i], fr[s & 1][1][i]); x1[i] = hmul1(fr[s & 1][0][i], fr[s & 1][2][i]); }
#pragma unroll
      for (int i = 0; i < 4; ++i) { x0[i] = hmul2(fr[s & 1][0][i], fr[s & 1][1][i], x0[i]); x1[i] = hmul2(fr[s & 1][0][i], fr[s & 1][2][i], x1[i]); }
      ka[0] = mfma16h(fr[s & 1][3], x0, ka[0]); ka[2] = mfma16h(fr[s & 1][4], x0, ka[2]);
      ka[1] = mfma16h(fr[s & 1][3], x1, ka[1]); ka[3] = mfma16h(fr[s & 1][4], x1, ka[3]);
      SBAR();
    }
    if (UEARLY) try_u(); else { __builtin_amdgcn_s_sleep(TDELAY); PRIO(APRIO); load_tile(); PRIO(ABASE); }
    STAMP(3);
    const float fac = 2.f * dt * isc;
    LAS float* tb = (LAS float*)stg + 4 * quad * 32 + c16;
#pragma unroll
    for (int g = 0; g < 4; ++g) { tb[g * 32] = ka[0][g] * fac; tb[g * 32 + 16] = ka[1][g] * fac; }
    if (quad < 2) {
#pragma unroll
      for (int g = 0; g < 4; ++g) { tb[(16 + g) * 32] = ka[2][g] * fac; tb[(16 + g) * 32 + 16] = ka[3][g] * fac; }
    }
  }
  STAMP(4);
  BAR();
  {
    float kk[4][8];
#pragma unroll
    for (int c = 0; c < 4; ++c)
#pragma unroll
      for (int j = 0; j < 8; ++j) kk[c][j] = *(LAS float*)(lds + c * STG + tix[j]);
    SBAR();
#pragma unroll
    for (int c = 0; c < 2; ++c) {
      u32x4 v;
#pragma unroll
      for (int j = 0; j < 4; ++j) v[j] = pkbf(kk[2 * c][2 * j] + kk[2 * c + 1][2 * j], kk[2 * c][2 * j + 1] + kk[2 * c + 1][2 * j + 1]);
      *(LAS u32x4*)(lds + RV + c * RVS + tid * 16) = v;
    }
  }
  STAMP(11);
  if (UEARLY) {
    try_u();
    BAR();
  } else { WAITVM(0); publish_u(); }
  STAMP(12);
  u32 sk[8];
#pragma unroll
  for (int i = 0; i < 2; ++i) {
    const u32x4 us = *(LAS u32x4*)(lds + UIM + (32 * i + (tid >> 3)) * 128 + (((tid & 7) ^ ((tid >> 5) & 7)) << 4));
#pragma unroll
    for (int k = 0; k < 4; ++k) { sk[4 * i + k] = pkbf(bflo(us[k]) * bflo(dk[k]), bfhi(us[k]) * bfhi(dk[k])); PIN(sk[4 * i + k]); }
  }
  if (!UEARLY) { PRIO(APRIO); mail = poll64(ws, mbu, lane < npc); PRIO(ABASE); }
  STAMP(14);
  if (tid < 64) *(LAS u32x4*)(lds + RV + half * RVS + 4096 + r32 * 16) = (u32x4){0u, 0u, 0u, 0u};
  SBAR();
#pragma nounroll
  for (int it = 0; it < SPIN_CAP; ++it) {
    if (!have_u) {
#pragma nounroll
      for (int i2 = 0; i2 < SPIN_CAP && __popcll(__ballot((u32)mail == tag)) != npc; ++i2) { __builtin_amdgcn_s_sleep(ASLEEP); mail = poll64(ws, mbu, lane < npc); COUNT(14); }
      load_u();
    }
    if (wave_sum(sum4(sum4(sum4((u32)(mail >> 32), ud[0]), ud[1]), ud[2])) == npc * tag) break;
    have_u = false;
    asm volatile("" ::: "memory");
    COUNT(11);
  }
  STAMP(15);
#pragma unroll
  for (int i = 0; i < 2; ++i) {
    const u32 nx = __builtin_amdgcn_update_dpp(__builtin_amdgcn_readlane(ud[i + 1].x, 0), ud[i].x, 0x130, 0xf, 0xf, false);
    const u32 dw[5] = {ud[i].x, ud[i].y, ud[i].z, ud[i].w, nx};
    LAS char* p = lds + UC + (128 * w + 64 * i + lane + 4) * 8;
#pragma unroll
    for (int c = 0; c < 2; ++c) {
      u32 pr[4];
#pragma unroll
      for (int k = 0; k < 4; ++k) pr[k] = __builtin_amdgcn_perm(dw[k + 1], dw[k], c ? 0x07060302u : 0x05040100u);
      *(LAS u32x2*)(p + (4 * c) * UCS) = (u32x2){pr[0], pr[2]};
      *(LAS u32x2*)(p + (4 * c + 1) * UCS) = (u32x2){pr[1], pr[3]};
      *(LAS u32*)(p + (4 * c + 2) * UCS - 4) = pr[0]; *(LAS u32*)(p + (4 * c + 2) * UCS) = pr[2];
      *(LAS u32*)(p + (4 * c + 3) * UCS - 4) = pr[1]; *(LAS u32*)(p + (4 * c + 3) * UCS) = pr[3];
    }
  }
  if (w == 0) {
    const u32 u0 = __builtin_amdgcn_readlane(ud[0].x, 0);
    const int cs = lane >> 3, ix = lane & 7, last = (cs & 2) ? 6 : 7;
    const u32 v = ((cs & 1) && ix == last) ? (cs & 4 ? u0 & 0xffff0000u : u0 << 16) : 0u;
    if (ix <= last) *(LAS u32*)(lds + UC + cs * UCS + ix * 4) = v;
  }
  STAMP(16);
  BAR();
  STAMP(5);
  PRIO(ACONV);
  {
    const int c16 = lane & 15, hq = lane >> 4;
    LAS char* pa = lds + RV + ch * RVS + 2 * (2032 - 256 * 7 + 128 * hf - 16 * c16 + 8 * hq);
    LAS char* pb = lds + UC + (4 * ch + ((c16 + 1) & 3)) * UCS + 2 * (128 * hf + 8 * hq + 4 * ((c16 + 1) >> 2));
    f32x4 acc[8] = {};
    u32x4 fa[2][8], fb[2][8];
    auto ldf = [&](int rho, int m) {
      fa[rho & 1][m] = *(LAS u32x4*)(pa + 64 * rho + 512 * (7 - m));
      const u32x2 lo = *(volatile LAS u32x2*)(pb + 64 * rho + 512 * m), hi = *(volatile LAS u32x2*)(pb + 64 * rho + 512 * m + 8);
      fb[rho & 1][m] = (u32x4){lo.x, lo.y, hi.x, hi.y};
    };
#pragma unroll
    for (int m = 0; m < 8; ++m) ldf(0, m);
#pragma unroll
    for (int rho = 0; rho < 4; ++rho) {
      SBAR();
#pragma unroll
      for (int g = 0; g < 8; ++g) {
#pragma unroll
        for (int aa = 0; aa <= g; ++aa) {
          if (aa + g < 8) acc[aa + g] = mfma16(fa[rho & 1][g], fb[rho & 1][aa], acc[aa + g]);
          if (aa < g && aa + g < 8) acc[aa + g] = mfma16(fa[rho & 1][aa], fb[rho & 1][g], acc[aa + g]);
        }
        if (rho < 3) { ldf(rho + 1, g); SBAR(); }
      }
    }
    STAMP(6);
    PRIO(ATAIL);
#pragma unroll
    for (int t = 0; t < 8; ++t) *(LAS f32x4*)(lds + EX + w * 8192 + t * 1024 + lane * 16) = acc[t];
    STAMP(17);
    BAR();
    STAMP(18);
#pragma unroll
    for (int i = 0; i < 2; ++i) {
      LAS char* p = lds + EX + (2 * w + i) * 1024 + lane * 16;
      const f32x4 y0 = *(LAS f32x4*)p + *(LAS f32x4*)(p + 8192), y1 = *(LAS f32x4*)(p + 16384) + *(LAS f32x4*)(p + 24576);
      const u32x4 v = {pkbf(y0.x, y1.x), pkbf(y0.y, y1.y), pkbf(y0.z, y1.z), pkbf(y0.w, y1.w)};
      ST_SC1(v, rs, WS_YX + (cb * 32 + 4 * (2 * w + i) + hq) * 8192 + jp * 256 + c16 * 16);
      const u32 ck = tag - row_sum(sum4(0u, v));
      if (c16 == 15) __builtin_amdgcn_raw_buffer_store_b64((u32x2){tag, ck}, rs, WS_MBY + (cb * 32 + 4 * (2 * w + i) + hq) * 256 + jp * 8, 0, 16);
    }
  }
  STAMP(7);
  {
    u32x4 y[2];
    const u32 mby = WS_MBY + a * 256 + (8 * w + lane) * 8;
#pragma nounroll
    for (int it = 0; it < SPIN_CAP; ++it) {
      __builtin_amdgcn_s_sleep(ASLEEP);
      mail = poll64(ws, mby, lane < 8);
      if (__ballot((u32)mail == tag) == 0xffull) break;
      COUNT(13);
    }
    STAMP(19);
#pragma nounroll
    for (int it = 0; it < SPIN_CAP; ++it) {
      asm volatile("" ::: "memory");
      y[0] = LD_SC1(rs, WS_YX + a * 8192 + w * 2048 + lane * 16); y[1] = LD_SC1(rs, WS_YX + a * 8192 + w * 2048 + 1024 + lane * 16);
      if (wave_sum(sum4(sum4((u32)(mail >> 32), y[0]), y[1])) == 8u * tag) break;
      COUNT(12);
    }
    STAMP(8);
#pragma unroll
    for (int i = 0; i < 2; ++i)
#pragma unroll
      for (int g = 0; g < 4; ++g) *(LAS u32*)(lds + (16 * g + (lane & 15)) * 144 + (8 * w + 4 * i + (lane >> 4)) * 4) = y[i][g];
    BAR();
#pragma unroll
    for (int i = 0; i < 2; ++i) {
      const u32x4 v = *(LAS u32x4*)(lds + (32 * i + (tid >> 3)) * 144 + (tid & 7) * 16);
      u32x4 o;
#pragma unroll
      for (int k = 0; k < 4; ++k) o[k] = pkbf(bflo(v[k]) + bflo(sk[4 * i + k]), bfhi(v[k]) + bfhi(sk[4 * i + k]));
      *(GAS u32x4*)(out + (u32)((64 * jp + 32 * i + (tid >> 3)) * 1792 + cb * 128 + (tid & 7) * 16)) = o;
    }
  }
  STAMP(9);
  STAMP_FLUSH();
}

__global__ void NOPK __launch_bounds__(256, 2)
fused_kernel(const void*, const void*, const void*, const void*, const void*, const void*, const void*, const void*, const void*,
             const void*, void*, void*) {
  __shared__ __attribute__((aligned(16))) char smem[81920];
  LAS char* lds = (LAS char*)smem;
  if ((blockIdx.x >> 5) == 7) {
    u64 pc;
    asm volatile("s_getpc_b64 %0" : "=s"(pc));
    if (threadIdx.x < CODE_LINES / 4)
      __builtin_amdgcn_global_load_lds((const GAS void*)((pc & ~127ull) + (threadIdx.x * 4 + ((blockIdx.x >> 3) & 3)) * 128), (LAS void*)lds, 4, 0, 0);
  }
  const __attribute__((address_space(4))) u64* ka = (const __attribute__((address_space(4))) u64*)__builtin_amdgcn_kernarg_segment_ptr();
  asm volatile("" : "+s"(ka));
  const char* u = (const char*)ka[0]; const GAS u32* logA = (const GAS u32*)ka[1]; const GAS u32* Aim = (const GAS u32*)ka[2];
  const GAS u32* Cre = (const GAS u32*)ka[3]; const GAS u32* Cim = (const GAS u32*)ka[4]; const GAS u32* logdt = (const GAS u32*)ka[5];
  const char* Dsk = (const char*)ka[6]; const char* x = (const char*)ka[7]; const char* wqkv = (const char*)ka[8];
  const char* wo = (const char*)ka[9]; char* out = (char*)ka[10]; char* ws = (char*)ka[11];
  asm volatile("" :: "s"(u), "s"(logA), "s"(Aim), "s"(Cre), "s"(Cim), "s"(logdt), "s"(Dsk), "s"(x), "s"(wqkv), "s"(wo), "s"(out), "s"(ws));
  const u32 epoch = request_epoch(ws);
  u32 pA, pI, pR, pC, ldt;
  int bid = blockIdx.x;
  if (bid >= 256) role_b<true>(lds, x, wqkv, out, ws, epoch);
  else if (bid < 224) {
    const int pi = bid * 256 + threadIdx.x;
    pA = __builtin_nontemporal_load(logA + pi); pI = __builtin_nontemporal_load(Aim + pi);
    pR = __builtin_nontemporal_load(Cre + pi); pC = __builtin_nontemporal_load(Cim + pi); ldt = logdt[bid];
  }
  asm volatile("" : "+s"(bid) :: "memory");
  if (bid >= 256) role_b<false>(lds, x, wqkv, out, ws, epoch);
  else if (bid < 224) role_a(lds, u, Dsk, wqkv, wo, out, ws, epoch, pA, pI, pR, pC, ldt);
  else {
    const int t = threadIdx.x;
    if (blockIdx.x == 224) {
      const u32 tag = take_tag(epoch);
      __builtin_amdgcn_s_sleep(127);
      wait_flags(ws, 256 + t, tag);
      wait_flags(ws, t < 224 ? t : 0, tag);
      BAR();
      if (t == 0) __hip_atomic_store((GAS u32*)ws, tag, RLX_AGENT);
      for (int i = 0; i < 6; ++i) __builtin_amdgcn_s_sleep(127);
      wait_flags(ws + 64, 256 + t, tag);
      wait_flags(ws + 64, t < 224 ? t : 0, tag);
      BAR();
      u64 mn = ~0ull, mxv = 0, sum = 0;
      for (int i = 0; i < 8; ++i) {
        const int wv = t + 256 * i, blk = wv >> 2;
        if (blk >= 224 && blk < 256) continue;
        const GAS u64* p = (const GAS u64*)(ws + WS_DBG) + wv * 2;
        const u64 s0 = __hip_atomic_load(p, RLX_AGENT), sk = __hip_atomic_load(p + 1, RLX_AGENT);
        if (blk < 224) mn = s0 < mn ? s0 : mn;
        if (((READOUT >> 5) & 1) == (blk >= 256)) { mxv = sk > mxv ? sk : mxv; sum += sk & 0xffffffffu; }
      }
      LAS u64* l = (LAS u64*)lds;
      l[t] = mn; l[256 + t] = mxv; l[512 + t] = sum;
      BAR();
      sum = 0;
      for (int i = 0; i < 256; ++i) { mn = l[i] < mn ? l[i] : mn; mxv = l[256 + i] > mxv ? l[256 + i] : mxv; sum += l[512 + i]; }
      const u64 avg = sum / (((READOUT >> 5) & 1) ? 1024 : 896) - (mn & 0xffffffffu);
      const u64 until = mn + 6000 + (RK == 0 ? 0 : (READOUT & 64) ? avg : mxv - mn);
      for (int it = 0; it < 1000000 && __builtin_amdgcn_s_memrealtime() < until; ++it) __builtin_amdgcn_s_sleep(1);
    }
  }
}

extern "C" void kernel_launch(void* const* d_in, const int* in_sizes, int n_in, void* d_out, int out_size, void* d_ws,
                              size_t ws_size, hipStream_t stream) {
  fused_kernel<<<512, 256, 0, stream>>>(d_in[0], d_in[1], d_in[2], d_in[3], d_in[4], d_in[5], d_in[6], d_in[7], d_in[8], d_in[9], d_out, d_ws);
}
```

```cpp
#define UEARLY 3
#include <hip/hip_runtime.h>
#include <stdint.h>

typedef unsigned short u16;
typedef unsigned u32;
typedef unsigned long long u64;
typedef short s16x4 __attribute__((ext_vector_type(4)));
typedef __bf16 bf16x8 __attribute__((ext_vector_type(8)));
typedef __bf16 bf16x2 __attribute__((ext_vector_type(2)));
typedef _Float16 f16x8 __attribute__((ext_vector_type(8)));
typedef _Float16 f16x2 __attribute__((ext_vector_type(2)));
typedef float f32x16 __attribute__((ext_vector_type(16)));
typedef float f32x4 __attribute__((ext_vector_type(4)));
typedef float f32x2 __attribute__((ext_vector_type(2)));
typedef u32 u32x4 __attribute__((ext_vector_type(4)));
typedef u32 u32x2 __attribute__((ext_vector_type(2)));
#define LAS __attribute__((address_space(3)))
#define GAS __attribute__((address_space(1)))
#define DI __device__ __forceinline__
#if defined(__HIP_DEVICE_COMPILE__)
#define NOPK __attribute__((target("no-packed-fp32-ops")))
#else
#define NOPK
#endif
#define RLX_AGENT __ATOMIC_RELAXED, __HIP_MEMORY_SCOPE_AGENT

#define BMAP 1
#define PREFETCH 1
#define CODE_BEFORE 0
#define CODE_LINES 220
#define QEARLY 3
#define GATH_AT 18
#define WO_EARLY 0
#define WO_MID 12
#define WO_G 32
#define WO_E 8
#define KVSTEP 1
#define PUB_FIRST 0
#define UPOLL_AT 8
#define TMAP 1
#define BPRIO1 2
#define ABASE 0
#define ACONV 0
#define L2PF 0
#define NTA 1
#define NTX 0
#define W0FIRST 1
#define ORD 4
#define PF2AT 1
#define UMID 0
#define WFIRST 0
#define HEADC 1
#define ASLEEP 0
#define TDELAY 0
#define ROT 1
#define APRIO 3
#define ATAIL 1
#define BPRIO 2

constexpr u32 WS_FLAG = 4096;
constexpr u32 WS_MB1 = 73728;
constexpr u32 WS_MB2 = WS_MB1 + 32768;
constexpr u32 WS_MBU = WS_MB2 + 32768;
constexpr u32 WS_MBY = WS_MBU + 57344;
constexpr u32 WS_KVP = 1u << 18;
constexpr u32 WS_OB = WS_KVP + (1u << 20);
constexpr u32 WS_UX = WS_OB + (2u << 20);
constexpr u32 WS_YX = WS_UX + (2u << 20);
constexpr u32 WS_WQ = WS_YX + (2u << 20);
constexpr u32 WS_WO = WS_WQ + (1u << 19);
constexpr u32 WS_MBQ = WS_WO + 458752;
constexpr u32 WS_END = WS_WO + (1u << 19);
constexpr u32 WS_DBG = WS_END;
constexpr int SPIN_CAP = 1 << 15;

#define STAMP(i) do {} while (0)
#define STAMP_DECL
#define COUNT(i) do {} while (0)
#define STAMP_FLUSH() do {} while (0)

DI float bflo(u32 v) { return __uint_as_float(v << 16); }
DI float bfhi(u32 v) { return __uint_as_float(v & 0xffff0000u); }
DI u32 pkbf(float a, float b) { f32x2 p = {a, b}; return __builtin_bit_cast(u32, __builtin_convertvector(p, bf16x2)); }
DI u32 pkh(f32x2 p) { return __builtin_bit_cast(u32, __builtin_convertvector(p, f16x2)); }
DI float rbf(float a) { return bflo(pkbf(a, a)); }
DI u32 umax3(u32 a, u32 b, u32 c) { u32 m = a > b ? a : b; return m > c ? m : c; }
DI u32 sum4(u32 m, u32x4 v) { return (m + v.x + v.y) + v.z + v.w; }
DI u32 row_sum(u32 v) {
  v += __builtin_amdgcn_update_dpp(0u, v, 0x111, 0xf, 0xf, true); v += __builtin_amdgcn_update_dpp(0u, v, 0x112, 0xf, 0xf, true);
  v += __builtin_amdgcn_update_dpp(0u, v, 0x114, 0xf, 0xf, true); v += __builtin_amdgcn_update_dpp(0u, v, 0x118, 0xf, 0xf, true);
  return v;
}
DI u32 wave_sum(u32 v) {
  v = row_sum(v);
  return (__builtin_amdgcn_readlane(v, 15) + __builtin_amdgcn_readlane(v, 31)) + (__builtin_amdgcn_readlane(v, 47) + __builtin_amdgcn_readlane(v, 63));
}
DI u32 wave_umax(u32 v) {
  u32 t;
  t = __builtin_amdgcn_update_dpp(0u, v, 0x111, 0xf, 0xf, false); v = v > t ? v : t;
  t = __builtin_amdgcn_update_dpp(0u, v, 0x112, 0xf, 0xf, false); v = v > t ? v : t;
  t = __builtin_amdgcn_update_dpp(0u, v, 0x114, 0xf, 0xf, false); v = v > t ? v : t;
  t = __builtin_amdgcn_update_dpp(0u, v, 0x118, 0xf, 0xf, false); v = v > t ? v : t;
  const u32 a = __builtin_amdgcn_readlane(v, 15), b = __builtin_amdgcn_readlane(v, 31), c = __builtin_amdgcn_readlane(v, 47), d = __builtin_amdgcn_readlane(v, 63);
  const u32 ab = a > b ? a : b, cd = c > d ? c : d;
  return ab > cd ? ab : cd;
}

DI u32 request_epoch(const char* ws) { u32 e; asm volatile("s_load_dword %0, %1, 0x0" : "=s"(e) : "s"(ws) : "memory"); return e; }
DI u32 take_tag(u32 e) { u32 r; asm volatile("s_waitcnt lgkmcnt(0)\n\ts_and_b32 %0, %1, -2" : "=s"(r) : "s"(e) : "memory", "scc"); return r * 1664525u + 2027808447u; }
DI void put_flag(char* ws, u32 tag) { if (threadIdx.x == 0) __hip_atomic_store((GAS u32*)(ws + WS_FLAG) + blockIdx.x * 32, tag, RLX_AGENT); }
#define PSCOPE __HIP_MEMORY_SCOPE_AGENT
DI u32 poll32(char* ws, u32 off, bool on) { u32 z = 0, r = 0; asm volatile("" : "+v"(z)); if (on) r = __hip_atomic_fetch_add((GAS u32*)(ws + off), z, __ATOMIC_RELAXED, PSCOPE); return r; }
DI u64 poll64(char* ws, u32 off, bool on) { u64 z = 0, r = 0; asm volatile("" : "+v"(z)); if (on) r = __hip_atomic_fetch_add((GAS u64*)(ws + off), z, __ATOMIC_RELAXED, PSCOPE); return r; }
#define SBAR() __builtin_amdgcn_sched_barrier(0)
#define PRIO(n) do { SBAR(); __builtin_amdgcn_s_setprio(n); SBAR(); } while (0)
#define PIN(x) asm volatile("" : "+v"(x))
DI f32x16 mfma32(u32x4 a, u32x4 b, f32x16 c) { return __builtin_amdgcn_mfma_f32_32x32x16_bf16(__builtin_bit_cast(bf16x8, a), __builtin_bit_cast(bf16x8, b), c, 0, 0, 0); }
DI f32x16 mfma32h(u32x4 a, u32x4 b, f32x16 c) { return __builtin_amdgcn_mfma_f32_32x32x16_f16(__builtin_bit_cast(f16x8, a), __builtin_bit_cast(f16x8, b), c, 0, 0, 0); }
DI f32x4 mfma16h(u32x4 a, u32x4 b, f32x4 c) { return __builtin_amdgcn_mfma_f32_16x16x32_f16(__builtin_bit_cast(f16x8, a), __builtin_bit_cast(f16x8, b), c, 0, 0, 0); }
DI f32x4 mfma16(u32x4 a, u32x4 b, f32x4 c) { return __builtin_amdgcn_mfma_f32_16x16x32_bf16(__builtin_bit_cast(bf16x8, a), __builtin_bit_cast(bf16x8, b), c, 0, 0, 0); }
DI u32x2 tr_read(LAS char* p) { return __builtin_bit_cast(u32x2, __builtin_amdgcn_ds_read_tr16_b64_v4i16((LAS s16x4*)p)); }
#define RSRC(p, n) __builtin_amdgcn_make_buffer_rsrc((void*)(p), 0, (n), 0x00020000)
#define DMA16(rs, voff, soff, dst, aux) __builtin_amdgcn_raw_ptr_buffer_load_lds((rs), (LAS void*)(dst), 16, (voff), (soff), 0, (aux))
#define WAITVM(n) do { asm volatile("" ::: "memory"); __builtin_amdgcn_s_waitcnt(0x0F70 | ((n) & 15) | (((n) >> 4) << 14)); asm volatile("" ::: "memory"); } while (0)
#define VMCNT(n) asm volatile("s_waitcnt vmcnt(" #n ")" ::: "memory")
#define LGKM0() asm volatile("s_waitcnt lgkmcnt(0)" ::: "memory")
#define BAR() do { asm volatile("s_waitcnt lgkmcnt(0)" ::: "memory"); __builtin_amdgcn_s_barrier(); asm volatile("" ::: "memory"); } while (0)
#define ST_SC1(v, rs, off) __builtin_amdgcn_raw_buffer_store_b128((v), (rs), (off), 0, 16)
#define LD_SC1(rs, off) __builtin_amdgcn_raw_buffer_load_b128((rs), (off), 0, 16)
#define LDS_SC1(rs, off, soff) __builtin_amdgcn_raw_buffer_load_b128((rs), (off), (soff), 16)
#define ST1_SC1(v, rs, off) __builtin_amdgcn_raw_buffer_store_b32((v), (rs), (off), 0, 16)
#define LD1_SC1(rs, off) __builtin_amdgcn_raw_buffer_load_b32((rs), (off), 0, 16)

typedef unsigned short u16x2 __attribute__((ext_vector_type(2)));
DI u32 phi1(float a, float b) {
  const u32 t = pkbf(a, b);
  const f32x2 x = {bflo(t), bfhi(t)};
  const f32x2 y = x * 1.4426950408889634f;
  const float e0 = __builtin_amdgcn_fmed3f(__builtin_amdgcn_exp2f(y.x), 0.f, 1.f);
  const float e1 = __builtin_amdgcn_fmed3f(__builtin_amdgcn_exp2f(y.y), 0.f, 1.f);
  const f32x2 x1 = x + 1.0f;
  return pkbf(__builtin_fmaxf(x1.x, e0), __builtin_fmaxf(x1.y, e1));
}
DI void phi_eps(u32x4 (&p)[2]) {
  u16x2 m = {0xffff, 0xffff};
#pragma unroll
  for (int j = 0; j < 8; ++j) { const u32 e = p[j >> 2][j & 3]; m = __builtin_elementwise_min(m, __builtin_bit_cast(u16x2, e)); }
  if (__any(m.x < 0x3A80 || m.y < 0x3A80)) {
#pragma unroll
    for (int j = 0; j < 8; ++j) { const u32 r = p[j >> 2][j & 3]; p[j >> 2][j & 3] = pkbf(bflo(r) + 1e-6f, bfhi(r) + 1e-6f); }
  }
}

DI void wait_flags(const char* ws, int idx, u32 tag) {
  const GAS u32* f = (const GAS u32*)(ws + WS_FLAG) + idx * 32;
#pragma nounroll
  for (int it = 0; it < SPIN_CAP; ++it) {
    const u32 v = __hip_atomic_load(f, RLX_AGENT);
    if (__all(v == tag)) break;
    __builtin_amdgcn_s_sleep(16);
  }
}

template <bool HEAD>
DI void role_b(LAS char* lds, const char* __restrict__ x, const char* __restrict__ wqkv, char* __restrict__ out, char* ws, u32 epoch) {
  const int b = blockIdx.x - 256, tid = threadIdx.x, lane = tid & 63;
  const int w = __builtin_amdgcn_readfirstlane(tid >> 6);
  STAMP_DECL;
  STAMP(0);
  const int rb = 4 * ((b & 7) >> 1) + (b >> 6), h = (b & 1) * 8 + ((b >> 3) & 7);
  const int start = (int)((0x1065432106543210ull >> (4 * ((h & 7) + 2 * (rb & 3)))) & 7);
  const __amdgpu_buffer_rsrc_t rs = RSRC(ws, WS_END), rx = RSRC(x, 2048 * 896), rw = RSRC(wqkv, 448 * 2688);
  constexpr int WR = 49152, KVI = WR + 4096, OST = 16384, OTR = WR;
#define XS(i) ((i) * 16384)
#define QS(cc) ((cc) < 2 ? WR + 24576 + (cc) * 4096 : (cc) < 6 ? XS(1) + ((cc) - 2) * 1024 : WR)
#define QD(cc) ((cc) >= 2 && (cc) < 6 ? 4096 : 1024)
  const int wsb = (56 * h) & ~15;
  const int coff = (h & 1) * 4;
  const int wone = (h & 1) ? 0 : 28;
  const u32 xv0 = (128 * rb + 32 * w + (lane >> 3)) * 896 + (((lane & 7) ^ (lane >> 4)) << 4);
  const u32 xv1 = (xv0 ^ 64) + 8 * 896;
  const u32 wv = (16 * w + (lane >> 2)) * 2688 + wsb + (lane & 3) * 16;
  const u32 l16 = lane * 16;
  int ch_[7];
  auto chunk = [&](int cc) { return ch_[cc]; };
  const int gi = h + rb;
  const u32 ordc = ORD == 1 ? 0x6543210u : ORD == 2 ? ((gi & 1) ? 0x5461032u : 0x6543210u) : ORD == 3 ? ((gi & 1) ? 0x6452301u : 0x6543210u)
                 : ((gi & 2) ? ((gi & 1) ? 0x4652103u : 0x5461032u) : ((gi & 1) ? 0x4650321u : 0x6543210u));
  auto next_chunk = [&](int cc) { int c = (ordc >> (4 * cc)) & 7; asm volatile("" : "+s"(c)); ch_[cc] = c; };
  auto issue_x = [&](int cc, int i) { DMA16(rx, (i & 1) ? xv1 : xv0, chunk(cc) * 128 + (i >> 1) * (16 * 896), lds + XS(cc % 3) + w * 4096 + i * 1024, NTX); };
  auto issue_w = [&](int cc, int i) { DMA16(rw, wv, chunk(cc) * (64 * 2688) + 896 * (i + 1), lds + WR + (cc & 3) * 8192 + i * 4096 + w * 1024, 0); };
  bool qok = true;
  auto issue_q = [&](int cc) { DMA16(rs, l16, qok ? WS_WQ + (h * 28 + 4 * chunk(cc) + w) * 1024 : WS_END, lds + QS(cc) + w * QD(cc), 16); };
#pragma unroll
  for (int cc = 0; cc < 2; ++cc) {
    next_chunk(cc);
    if (HEAD == (cc < HEADC)) { issue_w(cc, 0); issue_w(cc, 1); issue_x(cc, 0); issue_x(cc, 1); issue_x(cc, 2); issue_x(cc, 3); }
    SBAR();
  }
  if (HEAD) return;
  next_chunk(2); next_chunk(3); next_chunk(4); next_chunk(5); next_chunk(6);
  __builtin_amdgcn_s_setprio(BPRIO1);
  const int r32 = lane & 31, half = lane >> 5;
  const int xrd = w * 4096 + r32 * 128 + ((half ^ ((r32 >> 1) & 7)) << 4);
  LAS char* whi = lds + WR + (8 * half + ((lane >> 2) & 3)) * 64 + ((lane >> 4) & 1) * 32 + (lane & 3) * 8;
  PIN(whi);
  const int rt = h >> 2, ct = (h + ((h & 8) ? 2 : 1)) & 3;
  u32 wo_src = WS_WO + (4 * ct + w < 14 ? 4 * ct + w : 13) * 32768, kvp_src = WS_KVP + h * 65536 + w * 16384;
  u32 ob_dst = WS_OB + ((rb * 4 + w) * 16 + h) * 2048, ob_src = WS_OB + ((rb * 4 + rt) * 16 + 4 * w) * 2048;
  asm volatile("" : "+s"(wo_src), "+s"(kvp_src), "+s"(ob_dst), "+s"(ob_src));
  u32 tell1 = lane < 4 ? WS_MB1 + (h * 16 + 4 * w + lane) * 128 + rb * 4 : tid == 4 ? WS_FLAG + blockIdx.x * 128 : WS_END;
  u32 tell2 = lane < 4 ? WS_MB2 + (rb * 16 + 4 * w + lane) * 128 + h * 4 : WS_END;
  u32 mb1 = WS_MB1 + (h * 16 + rb) * 128 + ((lane + rb) & 15) * 4, mb2 = WS_MB2 + (rb * 16 + h) * 128 + ((4 * w + lane) & 15) * 4;
  u32 kvp_dst = kvp_src + rb * 1024 + l16, kvs[16];
#pragma unroll
  for (int p = 1; p < 16; ++p) { kvs[p] = kvp_src + ((p + rb) & 15) * 1024; asm volatile("" : "+s"(kvs[p])); }
  const int dw = 8 * w + 4 * half - coff;
  u32 rowmask = dw < 0 || dw >= 28 ? 0u : ~0u, padlo = (h & 1) && !half ? 0u : ~0u, padhi = !(h & 1) && half ? 0u : ~0u;
  const int oi = (r32 & 3) + 4 * (r32 >> 3);
  LAS char* kvrd = lds + KVI + ((oi < 14 ? 14 * ((r32 >> 2) & 1) + oi + coff : wone) + 32 * half) * 32;
  LAS char* qlo = lds + l16; LAS char* qhi = lds + WR + l16;
  PIN(tell1); PIN(tell2); PIN(mb1); PIN(mb2); PIN(kvp_dst); PIN(rowmask); PIN(padlo); PIN(padhi); PIN(kvrd); PIN(qhi);
  const bool ck = lane == wone + 1;
  u32 mbq = WS_MBQ + (h * 16 + rb) * 128 + (4 * (lane & 7) + w) * 4, mail = 0, tag = 0;
  PIN(mbq);
  u32x4 xf[28], wf[2][8];
  f32x16 ak = {}, av = {};
  auto rd = [&](int cc, int i) {
    if ((i & 1) == 0) xf[cc * 4 + (i >> 1)] = *(LAS u32x4*)(lds + XS(cc % 3) + (xrd ^ ((i >> 1) << 5)));
    LAS char* p = whi + (cc & 3) * 8192 + (i >> 1) * 1024 + (i & 1) * 4096;
    const u32x2 lo = tr_read(p), hi = tr_read(p + 256);
    wf[cc & 1][i] = (u32x4){lo.x, lo.y, hi.x, hi.y};
  };
#pragma unroll
  for (int cc = 0; cc < 8; ++cc) {
    if (cc < 7) {
      if (cc == 0) VMCNT(6); else if (cc < 5) VMCNT(8); else if (cc == 5) VMCNT(7); else VMCNT(1);
      BAR();
      if (cc == 5) __builtin_amdgcn_s_setprio(BPRIO);
      if (cc == 6) tag = take_tag(epoch);
      if (cc == 0) { STAMP(1); }
      if (cc == 2) { STAMP(23); } if (cc == 4) { STAMP(24); } if (cc == 5) { STAMP(25); } if (cc == 6) { STAMP(26); }
      if (cc >= 5) {
#pragma unroll
        for (int i = 0; i < 8; ++i) rd(cc, i);
        SBAR();
      }
#pragma unroll
      for (int i = 0; i < 8; ++i) {
        if (cc > 0) {
          if (i & 1) av = mfma32(xf[(cc - 1) * 4 + (i >> 1)], wf[(cc - 1) & 1][i], av);
          else ak = mfma32(xf[(cc - 1) * 4 + (i >> 1)], wf[(cc - 1) & 1][i], ak);
        }
        if (cc < 5) rd(cc, i);
        if (cc == 0) { if (i < 2) issue_w(2, i); else if (i < 6) issue_x(2, i - 2); else issue_w(3, i - 6); }
        else if (cc <= 4 && i < 4) issue_x(cc + 2, i);
        else if (cc <= 3 && i < 6) issue_w(cc + 3, i - 4);
        else if (cc == 4 && i == 4) mail = poll32(ws, mbq, lane < 7);
        else if (cc == 6 && i >= 4) {
          if (i == 4) { WAITVM(0); qok = __ballot(mail == tag) == 0x7full; }
          issue_q(2 * (i - 4)); if (i < 7) issue_q(2 * (i - 4) + 1);
        }
        SBAR();
      }
#pragma unroll
      for (int s = 3; s >= 0; --s) { PIN(wf[cc & 1][2 * s + 1]); PIN(wf[cc & 1][2 * s]); PIN(xf[cc * 4 + s]); }
      if (cc > 0) { PIN(ak); PIN(av); }
    } else {
#pragma unroll
      for (int s = 0; s < 4; ++s) ak = mfma32(xf[24 + s], wf[0][2 * s], ak);
#pragma unroll
      for (int s = 0; s < 4; ++s) av = mfma32(xf[24 + s], wf[0][2 * s + 1], av);
      SBAR();
    }
  }
  STAMP(2);
  u32x4 wof[32], d[16], pq[2];
  auto load_wo = [&](int k) { wof[k] = __builtin_amdgcn_raw_buffer_load_b128(rs, l16 + (k & 3) * 1024, wo_src + (k >> 2) * 4096, 16); };
  {
    u32x4 pk[2], pv[2];
#pragma unroll
    for (int j = 0; j < 8; ++j) {
      pk[j >> 2][j & 3] = phi1(ak[2 * j], ak[2 * j + 1]);
      if (j < WO_E) { PIN(pk[j >> 2][j & 3]); load_wo(j); SBAR(); }
    }
    phi_eps(pk);
    STAMP(11);
#pragma unroll
    for (int j = 0; j < 8; ++j) { const u32 v = pkbf(av[2 * j], av[2 * j + 1]); pv[j >> 2][j & 3] = r32 == wone ? 0x3F803F80u : v; }
    f32x16 kv = {};
    kv = mfma32(pk[0], pv[0], kv); kv = mfma32(pk[1], pv[1], kv);
    if (!qok) {
#pragma nounroll
      for (int it = 0; it < SPIN_CAP && __ballot(poll32(ws, mbq, lane < 7) == tag) != 0x7full; ++it) {}
      qok = true;
#pragma unroll
      for (int cc = 0; cc < 7; ++cc) issue_q(cc);
      WAITVM(0);
      COUNT(13);
    }
    WAITVM(WO_E);
    ST1_SC1(tag, rs, tell1);
#pragma unroll
    for (int g = 0; g < 4; ++g) *(LAS f32x4*)(lds + XS(2) + w * 4096 + g * 1024 + l16) = (f32x4){kv[4 * g], kv[4 * g + 1], kv[4 * g + 2], kv[4 * g + 3]};
    STAMP(12);
    BAR();
    f32x4 s4 = *(LAS f32x4*)(lds + XS(2) + tid * 16);
#pragma unroll
    for (int ww = 1; ww < 4; ++ww) s4 += *(LAS f32x4*)(lds + XS(2) + ww * 4096 + tid * 16);
    u32x4 v = __builtin_bit_cast(u32x4, s4);
    const u32 part = v.y + v.z + v.w;
    const u32 c = tag - wave_sum(ck ? part : part + v.x);
    if (ck) v.x = c;
    ST_SC1(v, rs, kvp_dst);
    d[0] = v;
  }
  u32 go, need = 0xfffeu;
  mail = poll32(ws, mb1, lane < 16);
  STAMP(3);
#define QP(cc) (QS(cc) >= WR ? qhi + (QS(cc) - WR) : qlo + QS(cc))
  auto load_kv = [&](int p, bool on = true) { d[p] = __builtin_amdgcn_raw_buffer_load_b128(rs, l16, on ? kvs[p] : WS_END, 16); };
  {
    f32x16 aq = {};
    u32x4 qf[2][4];
    u32 qsum = 0;
    auto read_q = [&](int cc) {
#pragma unroll
      for (int s = 0; s < 4; ++s) qf[cc & 1][s] = *(LAS u32x4*)(QP(cc) + s * QD(cc));
      SBAR();
    };
    read_q(0);
#pragma unroll
    for (int cc = 0; cc < 7; ++cc) {
      if (cc < 6) read_q(cc + 1);
#pragma unroll
      for (int s = 0; s < 4; ++s) {
        aq = mfma32(qf[cc & 1][s], xf[cc * 4 + s], aq);
        qsum = sum4(qsum, qf[cc & 1][s]);
        if (s & 1) load_wo(WO_E + 2 * cc + (s >> 1));
        PIN(aq); PIN(qsum);
        SBAR();
      }
    }
    qok = wave_sum(qsum) == 28u * tag;
    STAMP(14);
    go = need & (u32)__ballot(mail == tag); need &= ~go;
    mail = poll32(ws, mb1, lane < 16 && need != 0);
    STAMP(15);
#pragma unroll
    for (int j = 0; j < 8; ++j) {
      if (j) load_kv(2 * j, go >> (2 * j) & 1);
      load_kv(2 * j + 1, go >> (2 * j + 1) & 1);
      pq[j >> 2][j & 3] = phi1(aq[2 * j], aq[2 * j + 1]);
      PIN(pq[j >> 2][j & 3]);
      SBAR();
    }
    phi_eps(pq);
  }
  STAMP(5);
  PIN(mail);
  STAMP(16);
#pragma nounroll
  for (int i2 = 0; i2 < SPIN_CAP && need != 0; ++i2) {
    go = need & (u32)__ballot(mail == tag); need &= ~go;
#pragma unroll
    for (int p = 1; p < 16; ++p) if (go >> p & 1) load_kv(p);
    if (need != 0) { mail = poll32(ws, mb1, lane < 16); WAITVM(0); }
    COUNT(14);
  }
#pragma unroll
  for (int k = WO_E + 14; k < WO_G; ++k) load_wo(k);
  WAITVM(WO_G - WO_E - 14);
  STAMP(18);
#pragma nounroll
  for (int it = 0; it < SPIN_CAP; ++it) {
    u32 m = 0;
#pragma unroll
    for (int p = 0; p < 16; ++p) m = sum4(m, d[p]);
    if (wave_sum(m) == 16u * tag) break;
    asm volatile("" ::: "memory");
#pragma unroll
    for (int p = 1; p < 16; ++p) load_kv(p);
    WAITVM(0);
    COUNT(11);
  }
  mail = poll32(ws, mb2, lane < 4);
  SBAR();
  {
    f32x4 s4 = __builtin_bit_cast(f32x4, d[0]);
#pragma unroll
    for (int p = 1; p < 16; ++p) s4 += __builtin_bit_cast(f32x4, d[p]);
    *(LAS u32x2*)(lds + KVI + lane * 32 + w * 8) = (u32x2){pkbf(s4.x, s4.y) & rowmask, pkbf(s4.z, s4.w) & rowmask};
  }
  STAMP(6);
  BAR();
  {
    const u32x4 a0 = *(LAS u32x4*)kvrd, a1 = *(LAS u32x4*)(kvrd + 16);
    if (!qok) {
      f32x16 aq = {};
#pragma nounroll
      for (int k2 = 0; k2 < 28; ++k2) {
        const int ks = 4 * ((ordc >> (k2 & 28)) & 7) + (k2 & 3);
        u32x4 f;
#pragma nounroll
        for (int it = 0; it < SPIN_CAP; ++it) {
          asm volatile("" ::: "memory");
          f = LDS_SC1(rs, l16, WS_WQ + (h * 28 + ks) * 1024);
          if (wave_sum(sum4(0u, f)) == tag) break;
        }
        aq = mfma32(f, __builtin_amdgcn_raw_buffer_load_b128(rx, (128 * rb + 32 * w + r32) * 896 + half * 16, ks * 32, 0), aq);
      }
#pragma unroll
      for (int j = 0; j < 8; ++j) pq[j >> 2][j & 3] = phi1(aq[2 * j], aq[2 * j + 1]);
      phi_eps(pq);
      COUNT(15);
    }
    ST1_SC1(tag, rs, tell2);
    pq[0][0] &= padlo; pq[0][1] &= padlo; pq[1][2] &= padhi; pq[1][3] &= padhi;
    f32x16 nm = {};
    nm = mfma32(a0, pq[0], nm); nm = mfma32(a1, pq[1], nm);
    const float den = rbf(rbf(nm[14]) + 1e-6f);
    const float rc = __builtin_amdgcn_rcpf(den);
    u32 o[8];
#pragma unroll
    for (int j = 0; j < 7; ++j) { const u32 t = pkbf(nm[2 * j], nm[2 * j + 1]); o[j] = pkbf(bflo(t) * rc, bfhi(t) * rc); }
    o[7] = tag - ((o[0] + o[1] + o[2]) + (o[3] + o[4] + o[5]) + o[6]);
    __builtin_amdgcn_raw_buffer_store_b128((u32x4){o[0], o[1], o[2], o[3]}, rs, l16, ob_dst, 16);
    __builtin_amdgcn_raw_buffer_store_b128((u32x4){o[4], o[5], o[6], o[7]}, rs, l16 + 1024, ob_dst, 16);
    SBAR();
  }
  STAMP(7);
  u32x4 g[8];
  auto load_o = [&](int i, bool on = true) {
    const u32 so = on ? ob_src + (i >> 1) * 4096 : WS_END;
    g[2 * i] = __builtin_amdgcn_raw_buffer_load_b128(rs, l16 + (i & 1) * 2048, so, 16);
    g[2 * i + 1] = __builtin_amdgcn_raw_buffer_load_b128(rs, l16 + (i & 1) * 2048 + 1024, so, 16);
  };
  WAITVM(3);
  go = (u32)__ballot(mail == tag); need = 15u & ~go;
  mail = poll32(ws, mb2, lane < 4 && need != 0);
#pragma unroll
  for (int i = 0; i < 4; ++i) load_o(i, go >> i & 1);
  SBAR();
  STAMP(19);
#pragma nounroll
  for (int it = 0; it < SPIN_CAP; ++it) {
    u32 m = 0;
#pragma unroll
    for (int k = 0; k < 32; ++k) m = sum4(m, wof[k]);
    if (__all(m == 16u * tag)) break;
    asm volatile("" ::: "memory");
#pragma unroll
    for (int k = 0; k < 32; ++k) load_wo(k);
    WAITVM(0);
  }
#pragma unroll
  for (int k = 1; k < 32; k += 2) { wof[k].w = 0u; PIN(wof[k]); }
  STAMP(20);
#pragma nounroll
  for (int it = 0; it < SPIN_CAP; ++it) {
#pragma nounroll
    for (int i2 = 0; i2 < SPIN_CAP && need != 0; ++i2) {
      go = need & (u32)__ballot(mail == tag); need &= ~go;
      if (need != 0) mail = poll32(ws, mb2, lane < 4);
#pragma unroll
      for (int i = 0; i < 4; ++i) if (go >> i & 1) load_o(i);
      COUNT(12);
    }
#pragma unroll
    for (int i = 0; i < 4; ++i) need |= (u32)!__all(sum4(sum4(0u, g[2 * i]), g[2 * i + 1]) == tag) << i;
    if (need == 0) break;
    mail = tag;
    asm volatile("" ::: "memory");
    COUNT(13);
  }
#pragma unroll
  for (int p = 0; p < 8; ++p) {
    if (p & 1) g[p].w = 0u;
    *(LAS u32x4*)(lds + OST + (8 * w + p) * 1024 + l16) = g[p];
  }
  STAMP(8);
  BAR();
  STAMP(9);
  f32x16 acc = {};
  {
    u32x4 of[32];
#pragma unroll
    for (int k = 0; k < 6; ++k) of[k] = *(LAS u32x4*)(lds + OST + k * 1024 + l16);
    SBAR();
#pragma unroll
    for (int k = 0; k < 32; ++k) {
      acc = mfma32(wof[k], of[k], acc);
      if (k + 6 < 32) of[k + 6] = *(LAS u32x4*)(lds + OST + (k + 6) * 1024 + l16);
      SBAR();
    }
  }
  STAMP(22);
  {
    LAS char* p = lds + OTR + r32 * 272 + w * 64 + half * 32;
    *(LAS u32x4*)p = (u32x4){pkbf(acc[0], acc[1]), pkbf(acc[2], acc[3]), pkbf(acc[4], acc[5]), pkbf(acc[6], acc[7])};
    *(LAS u32x4*)(p + 16) = (u32x4){pkbf(acc[8], acc[9]), pkbf(acc[10], acc[11]), pkbf(acc[12], acc[13]), pkbf(acc[14], acc[15])};
  }
  BAR();
  if (ct < 3 || (tid & 15) < 8) {
#pragma unroll
    for (int i = 0; i < 2; ++i)
      *(GAS u32x4*)(out + (u32)((128 * rb + 32 * rt + 16 * i + (tid >> 4)) * 1792 + 896 + ct * 256 + (tid & 15) * 16)) =
          *(LAS u32x4*)(lds + OTR + (16 * i + (tid >> 4)) * 272 + (tid & 15) * 16);
  }
  STAMP(10);
  STAMP_FLUSH();
}

DI u32 hmul1(u32 a, u32 b) { const f16x2 x = __builtin_bit_cast(f16x2, a), y = __builtin_bit_cast(f16x2, b); return __builtin_bit_cast(u32, (f16x2){x.x, x.x} * y); }
DI u32 hmul2(u32 a, u32 b, u32 t) {
  const f16x2 x = __builtin_bit_cast(f16x2, a), y = __builtin_bit_cast(f16x2, b);
  return __builtin_bit_cast(u32, __builtin_elementwise_fma((f16x2){x.y, x.y}, (f16x2){-y.y, y.x}, __builtin_bit_cast(f16x2, t)));
}
DI f32x2 rot(f32x2 b) { return (f32x2){-b.y, b.x}; }
DI f32x2 cm1(f32x2 a, f32x2 b) { return (f32x2){a.x, a.x} * b; }
DI f32x2 cm2(f32x2 a, f32x2 br, f32x2 t) { return __builtin_elementwise_fma((f32x2){a.y, a.y}, br, t); }
DI f32x2 cexp_p(float a2, float f, float p) {
  const float m = __builtin_amdgcn_exp2f(a2 * p), ph = __builtin_amdgcn_fractf(f * p);
  return (f32x2){m, m} * (f32x2){__builtin_amdgcn_cosf(ph), __builtin_amdgcn_sinf(ph)};
}
DI void role_a(LAS char* lds, const char* __restrict__ u, const char* __restrict__ Dsk, const char* __restrict__ wqkv, const char* __restrict__ wo, char* __restrict__ out, char* ws, u32 epoch,
               u32 pA, u32 pI, u32 pR, u32 pC, u32 ldt) {
  const int a = blockIdx.x, cb = a >> 5, jp = a & 31, tid = threadIdx.x, lane = tid & 63;
  const int w = __builtin_amdgcn_readfirstlane(tid >> 6);
  STAMP_DECL;
  STAMP(0);
  __builtin_amdgcn_s_setprio(APRIO);
  constexpr int SROW = 528, STG = 32 * SROW, UC = 0, UCS = 4160, RV = 36864, RVS = 4608, EX = RV + 2 * RVS, UIM = 4 * STG;
  const int PKS = UIM + 8192 + (w ? 1024 + w * 1024 : 0);
  const __amdgpu_buffer_rsrc_t rs = RSRC(ws, WS_END);
  const int ch = w >> 1, hf = w & 1, d = 2 * a + ch;
  const int r32 = lane & 31, half = lane >> 5;
  const int pkh_ = w == 0 ? (a & 15) : 2 * (a & 7) + (w - 1);
  {
    const __amdgpu_buffer_rsrc_t rp = RSRC(w == 0 ? wo : wqkv, w == 0 ? 448 * 896 : w < 3 ? 448 * 2688 : 0);
    const u32 vo = (lane >> 2) * (w == 0 ? 896 : 2688) + (lane & 3) * 16;
    const u32 so = w == 0 ? 28 * 896 * pkh_ + (a >> 4) * 64 : 16 * 2688 * (a >> 3) + ((56 * pkh_) & ~15);
    DMA16(rp, vo, so, lds + PKS, 0);
    DMA16(rp, vo, w == 0 ? so + 16 * 896 : 448 * 2688, lds + (w == 0 ? PKS + 1024 : UIM + 8192 + 5120), 0);
  }
  u32x4 dk;
  auto load_tile = [&]() {
#pragma unroll
    for (int i = 0; i < 2; ++i)
      DMA16(RSRC(u, 2048 * 896), (tid >> 3) * 896 + (((tid & 7) ^ ((tid >> 5) & 7)) << 4), (64 * jp + 32 * i) * 896 + cb * 128, lds + UIM + (32 * i + 8 * w) * 128, NTA ? 2 : 0);
    dk = *(const GAS u32x4*)(Dsk + cb * 128 + (tid & 7) * 16);
  };
  if (UEARLY == 1) load_tile();
  PRIO(ABASE);
  int tix[8];
#pragma unroll
  for (int j = 0; j < 8; ++j) {
    const int p = (int)rbf((float)(2047 - 8 * tid - j));
    const int low = p & 7, blk = p >> 8;
    const int k4 = (blk < 4 ? blk : 4) * 4;
    const int sh = (0x32210 >> k4) & 15;
    const int base = blk + ((0xCBA70 >> k4) & 15);
    tix[j] = ((base + (low >> sh)) * 32 + ((p >> 3) & 31)) * 4;
    PIN(tix[j]);
  }
  const u32 tag = take_tag(epoch);
  auto publish_weights = [&]() {
  PRIO(APRIO);
  if (w == 0) {
    const int q = (lane >> 2) & 3, gam = 4 * ((lane >> 4) & 1) + (lane & 3), ng = 4 * (gam & 1) + (gam >> 1);
    LAS char* p = lds + PKS + (14 * half + q) * 64 + 8 * ng;
    const u32x2 r0 = tr_read(p), r1 = tr_read(p + 256), r2 = tr_read(p + 512), r3 = tr_read(p + 768);
    const u32 off = WS_WO + ((a >> 4) * 16 + pkh_) * 2048 + lane * 16;
    ST_SC1(((u32x4){r0.x, r0.y, r1.x, r1.y}), rs, off);
    ST_SC1(((u32x4){r2.x, r2.y, r3.x, tag - ((r0.x + r0.y + r1.x) + (r1.y + r2.x + r2.y) + r3.x)}), rs, off + 1024);
  } else if (w < 3) {
    LAS char* p = lds + PKS + (8 * half + ((lane >> 2) & 3)) * 64 + ((lane >> 4) & 1) * 32 + (lane & 3) * 8;
    const u32x2 r0 = tr_read(p), r1 = tr_read(p + 256);
    u32x4 v = {r0.x, r0.y, r1.x, r1.y};
    const bool ck = lane == ((pkh_ & 1) ? 1 : 29);
    if (ck) v = (u32x4){0u, 0u, 0u, 0u};
    const u32 c = tag - wave_sum(sum4(0u, v));
    if (ck) v.x = c;
    ST_SC1(v, rs, WS_WQ + (pkh_ * 28 + (a >> 3)) * 1024 + lane * 16);
    if (lane < 16) ST1_SC1(tag, rs, WS_MBQ + (pkh_ * 16 + lane) * 128 + (a >> 3) * 4);
  }
  PRIO(ABASE);
  };
  auto publish_u = [&]() {
  PRIO(APRIO);
  BAR();
#pragma unroll
  for (int i = 0; i < 2; ++i) {
    const int jj = 16 * i + (tid >> 4), n = tid & 15;
    LAS char* p = lds + UIM + 4 * n * 128 + (((jj >> 2) ^ (n & 7)) << 4) + (jj & 3) * 4;
    const u32x4 v = {*(LAS u32*)p, *(LAS u32*)(p + 128), *(LAS u32*)(p + 256), *(LAS u32*)(p + 384)};
    ST_SC1(v, rs, WS_UX + (cb * 32 + jj) * 8192 + jp * 256 + n * 16);
    const u32 ck = tag - row_sum(sum4(0u, v));
    __builtin_amdgcn_raw_buffer_store_b64((u32x2){tag, ck}, rs, n == 15 ? WS_MBU + (cb * 32 + jj) * 256 + jp * 8 : tid + i == 0 ? WS_FLAG + a * 128 : WS_END, 0, 16);
  }
  PRIO(ABASE);
  };
  if (WFIRST) { WAITVM(0); publish_weights(); if (UEARLY == 1) publish_u(); } else if (UEARLY == 1) WAITVM(5); else WAITVM(2);
  PIN(pA); PIN(pI); PIN(pR); PIN(pC); PIN(ldt);
  if (UEARLY == 2) { PRIO(APRIO); load_tile(); PRIO(ABASE); }
  STAMP(1);
  const float dt = rbf(expf(ch ? bfhi(ldt) : bflo(ldt)));
  const u32 mx = wave_umax(umax3(umax3(pR & 0x7fffu, (pR >> 16) & 0x7fffu, pC & 0x7fffu), (pC >> 16) & 0x7fffu, 0u));
  int ex = mx >> 7; ex = ex < 13 ? 13 : ex > 254 ? 254 : ex;
  const float sc = __uint_as_float((u32)(266 - ex) << 23), isc = __uint_as_float((u32)(ex - 12) << 23);
  LAS char* stg = lds + w * STG;
  u32x4 ud[3];
  const __amdgpu_buffer_rsrc_t ru = RSRC(ws + WS_UX + a * 8192, 8192);
  const int npc = w < 3 ? 9 : 8;
  u64 mail = 0;
  const u32 mbu = WS_MBU + a * 256 + (8 * w + lane) * 8;
  auto load_u = [&]() {
    PRIO(APRIO);
    ud[0] = LD_SC1(ru, w * 2048 + lane * 16); ud[1] = LD_SC1(ru, w * 2048 + 1024 + lane * 16);
    ud[2] = LD_SC1(ru, lane < 16 ? w * 2048 + 2048 + lane * 16 : 8192);
    PRIO(ABASE);
  };
  bool have_u = false;
  auto try_u = [&]() {
    if (!have_u) { if (__popcll(__ballot((u32)mail == tag)) == npc) { load_u(); have_u = true; } else { PRIO(APRIO); mail = poll64(ws, mbu, lane < npc); PRIO(ABASE); } }
  };
  {
    f32x2 y1[2], yq[2], z8[2], z32[2], p[2], q[2], c2[2], c4[2], c6[2];
#pragma unroll
    for (int m = 0; m < 2; ++m) {
      const float la = m ? bfhi(pA) : bflo(pA), ai = m ? bfhi(pI) : bflo(pI), cr = m ? bfhi(pR) : bflo(pR), ci = m ? bfhi(pC) : bflo(pC);
      const float a2 = -rbf(expf(la)) * dt * 1.4426950408889634f, f = ai * dt * 0.15915494309189535f;
      y1[m] = cexp_p(a2, -f, 1.f); yq[m] = cexp_p(a2, -f, 256.f); z8[m] = cexp_p(a2, f, 8.f);
      p[m] = q[m] = (f32x2){cr, -ci} * sc;
    }
    LAS char* sw = stg + lane * 8;
#define PUT(r, v) *(LAS u32x2*)(sw + (r) * SROW) = (u32x2){pkh(v[0]), pkh(v[1])}
#define MUL(v, b) do { v[0] = cm2(v[0], rot(b[0]), cm1(v[0], b[0])); v[1] = cm2(v[1], rot(b[1]), cm1(v[1], b[1])); } while (0)
#define CPY(dst, src) do { dst[0] = src[0]; dst[1] = src[1]; } while (0)
    SBAR();
    PUT(0, p); *(LAS u32x2*)(sw + 21 * SROW) = (u32x2){0x3C00u, 0x3C00u};
    MUL(p, y1); MUL(q, yq); PUT(1, p); PUT(8, q); SBAR();
    MUL(p, y1); MUL(q, yq); PUT(2, p); PUT(12, q); CPY(c2, p); SBAR();
    MUL(p, y1); MUL(q, yq); PUT(3, p); PUT(14, q); SBAR();
    MUL(p, y1); MUL(q, yq); PUT(4, p); PUT(16, q); CPY(c4, p); SBAR();
    MUL(p, y1); MUL(q, yq); PUT(5, p); PUT(17, q); SBAR();
    MUL(p, y1); MUL(q, yq); PUT(6, p); PUT(18, q); CPY(c6, p); SBAR();
    MUL(p, y1); MUL(q, yq); PUT(7, p); PUT(19, q); SBAR();
    if (WFIRST && UEARLY == 1) { PRIO(APRIO); mail = poll64(ws, mbu, lane < npc); PRIO(ABASE); }
    MUL(c4, yq); MUL(q, yq); PUT(10, c4); PUT(20, q); SBAR();
    MUL(c4, yq); MUL(c2, yq); PUT(13, c4); PUT(9, c2); SBAR();
    MUL(c4, yq); MUL(c6, yq); PUT(15, c4); PUT(11, c6); SBAR();
    CPY(p, z8); PUT(22, p); MUL(p, z8); PUT(23, p); SBAR();
    MUL(p, z8); PUT(24, p); MUL(p, z8); PUT(25, p); CPY(z32, p); SBAR();
    MUL(p, z32); PUT(26, p); MUL(p, z32); PUT(27, p); SBAR();
    MUL(p, z32); PUT(28, p); MUL(p, z32); PUT(29, p); SBAR();
    MUL(p, z32); PUT(30, p); MUL(p, z32); PUT(31, p); SBAR();
#undef PUT
#undef MUL
#undef CPY
  }
  STAMP(2);
  if (!WFIRST) { WAITVM(0); if (UEARLY == 3) { PRIO(APRIO); load_tile(); } publish_weights(); if (UEARLY == 1 || UEARLY == 2) publish_u(); } else if (UEARLY == 1) try_u();
  STAMP(13);
  {
    const int c16 = lane & 15, quad = lane >> 4;
    LAS char* py0 = stg + c16 * SROW + quad * 16;
    LAS char* py1 = stg + (16 + (c16 < 4 ? c16 : 4)) * SROW + quad * 16;
    LAS char* pt1 = stg + (21 + (c16 & 3)) * SROW + quad * 16;
    LAS char* pt2 = stg + (c16 < 4 ? 21 : 24 + (c16 >> 2)) * SROW + quad * 16;
    LAS char* pt3 = stg + (28 + (c16 >> 2)) * SROW + quad * 16;
    f32x4 ka[4] = {};
    u32x4 fr[2][5];
    auto rdk = [&](int s) {
      fr[s & 1][0] = *(LAS u32x4*)(pt1 + s * 64); fr[s & 1][1] = *(LAS u32x4*)(pt2 + s * 64); fr[s & 1][2] = *(LAS u32x4*)(pt3 + s * 64);
      fr[s & 1][3] = *(LAS u32x4*)(py0 + s * 64); fr[s & 1][4] = *(LAS u32x4*)(py1 + s * 64);
    };
    rdk(0);
#pragma unroll
    for (int s = 0; s < 8; ++s) {
      if (s < 7) rdk(s + 1);
      SBAR();
      u32x4 x0, x1;
#pragma unroll
      for (int i = 0; i < 4; ++i) { x0[i] = hmul1(fr[s & 1][0][i], fr[s & 1][1][i]); x1[i] = hmul1(fr[s & 1][0][i], fr[s & 1][2][i]); }
#pragma unroll
      for (int i = 0; i < 4; ++i) { x0[i] = hmul2(fr[s & 1][0][i], fr[s & 1][1][i], x0[i]); x1[i] = hmul2(fr[s & 1][0][i], fr[s & 1][2][i], x1[i]); }
      ka[0] = mfma16h(fr[s & 1][3], x0, ka[0]); ka[2] = mfma16h(fr[s & 1][4], x0, ka[2]);
      ka[1] = mfma16h(fr[s & 1][3], x1, ka[1]); ka[3] = mfma16h(fr[s & 1][4], x1, ka[3]);
      SBAR();
    }
    if (UEARLY == 3) {} else if (UEARLY) try_u(); else { __builtin_amdgcn_s_sleep(TDELAY); PRIO(APRIO); load_tile(); PRIO(ABASE); }
    STAMP(3);
    const float fac = 2.f * dt * isc;
    LAS float* tb = (LAS float*)stg + 4 * quad * 32 + c16;
#pragma unroll
    for (int g = 0; g < 4; ++g) { tb[g * 32] = ka[0][g] * fac; tb[g * 32 + 16] = ka[1][g] * fac; }
    if (quad < 2) {
#pragma unroll
      for (int g = 0; g < 4; ++g) { tb[(16 + g) * 32] = ka[2][g] * fac; tb[(16 + g) * 32 + 16] = ka[3][g] * fac; }
    }
  }
  STAMP(4);
  if (UEARLY == 3) { WAITVM(0); publish_u(); } else BAR();
  {
    float kk[4][8];
#pragma unroll
    for (int c = 0; c < 4; ++c)
#pragma unroll
      for (int j = 0; j < 8; ++j) kk[c][j] = *(LAS float*)(lds + c * STG + tix[j]);
    SBAR();
#pragma unroll
    for (int c = 0; c < 2; ++c) {
      u32x4 v;
#pragma unroll
      for (int j = 0; j < 4; ++j) v[j] = pkbf(kk[2 * c][2 * j] + kk[2 * c + 1][2 * j], kk[2 * c][2 * j + 1] + kk[2 * c + 1][2 * j + 1]);
      *(LAS u32x4*)(lds + RV + c * RVS + tid * 16) = v;
    }
  }
  STAMP(11);
  if (UEARLY) {
    try_u();
    BAR();
  } else { WAITVM(0); publish_u(); }
  STAMP(12);
  u32 sk[8];
#pragma unroll
  for (int i = 0; i < 2; ++i) {
    const u32x4 us = *(LAS u32x4*)(lds + UIM + (32 * i + (tid >> 3)) * 128 + (((tid & 7) ^ ((tid >> 5) & 7)) << 4));
#pragma unroll
    for (int k = 0; k < 4; ++k) { sk[4 * i + k] = pkbf(bflo(us[k]) * bflo(dk[k]), bfhi(us[k]) * bfhi(dk[k])); PIN(sk[4 * i + k]); }
  }
  if (!UEARLY) { PRIO(APRIO); mail = poll64(ws, mbu, lane < npc); PRIO(ABASE); }
  STAMP(14);
  if (tid < 64) *(LAS u32x4*)(lds + RV + half * RVS + 4096 + r32 * 16) = (u32x4){0u, 0u, 0u, 0u};
  SBAR();
#pragma nounroll
  for (int it = 0; it < SPIN_CAP; ++it) {
    if (!have_u) {
#pragma nounroll
      for (int i2 = 0; i2 < SPIN_CAP && __popcll(__ballot((u32)mail == tag)) != npc; ++i2) { __builtin_amdgcn_s_sleep(ASLEEP); mail = poll64(ws, mbu, lane < npc); COUNT(14); }
      load_u();
    }
    if (wave_sum(sum4(sum4(sum4((u32)(mail >> 32), ud[0]), ud[1]), ud[2])) == npc * tag) break;
    have_u = false;
    asm volatile("" ::: "memory");
    COUNT(11);
  }
  STAMP(15);
#pragma unroll
  for (int i = 0; i < 2; ++i) {
    const u32 nx = __builtin_amdgcn_update_dpp(__builtin_amdgcn_readlane(ud[i + 1].x, 0), ud[i].x, 0x130, 0xf, 0xf, false);
    const u32 dw[5] = {ud[i].x, ud[i].y, ud[i].z, ud[i].w, nx};
    LAS char* p = lds + UC + (128 * w + 64 * i + lane + 4) * 8;
#pragma unroll
    for (int c = 0; c < 2; ++c) {
      u32 pr[4];
#pragma unroll
      for (int k = 0; k < 4; ++k) pr[k] = __builtin_amdgcn_perm(dw[k + 1], dw[k], c ? 0x07060302u : 0x05040100u);
      *(LAS u32x2*)(p + (4 * c) * UCS) = (u32x2){pr[0], pr[2]};
      *(LAS u32x2*)(p + (4 * c + 1) * UCS) = (u32x2){pr[1], pr[3]};
      *(LAS u32*)(p + (4 * c + 2) * UCS - 4) = pr[0]; *(LAS u32*)(p + (4 * c + 2) * UCS) = pr[2];
      *(LAS u32*)(p + (4 * c + 3) * UCS - 4) = pr[1]; *(LAS u32*)(p + (4 * c + 3) * UCS) = pr[3];
    }
  }
  if (w == 0) {
    const u32 u0 = __builtin_amdgcn_readlane(ud[0].x, 0);
    const int cs = lane >> 3, ix = lane & 7, last = (cs & 2) ? 6 : 7;
    const u32 v = ((cs & 1) && ix == last) ? (cs & 4 ? u0 & 0xffff0000u : u0 << 16) : 0u;
    if (ix <= last) *(LAS u32*)(lds + UC + cs * UCS + ix * 4) = v;
  }
  STAMP(16);
  BAR();
  STAMP(5);
  PRIO(ACONV);
  {
    const int c16 = lane & 15, hq = lane >> 4;
    LAS char* pa = lds + RV + ch * RVS + 2 * (2032 - 256 * 7 + 128 * hf - 16 * c16 + 8 * hq);
    LAS char* pb = lds + UC + (4 * ch + ((c16 + 1) & 3)) * UCS + 2 * (128 * hf + 8 * hq + 4 * ((c16 + 1) >> 2));
    f32x4 acc[8] = {};
    u32x4 fa[2][8], fb[2][8];
    auto ldf = [&](int rho, int m) {
      fa[rho & 1][m] = *(LAS u32x4*)(pa + 64 * rho + 512 * (7 - m));
      const u32x2 lo = *(volatile LAS u32x2*)(pb + 64 * rho + 512 * m), hi = *(volatile LAS u32x2*)(pb + 64 * rho + 512 * m + 8);
      fb[rho & 1][m] = (u32x4){lo.x, lo.y, hi.x, hi.y};
    };
#pragma unroll
    for (int m = 0; m < 8; ++m) ldf(0, m);
#pragma unroll
    for (int rho = 0; rho < 4; ++rho) {
      SBAR();
#pragma unroll
      for (int g = 0; g < 8; ++g) {
#pragma unroll
        for (int aa = 0; aa <= g; ++aa) {
          if (aa + g < 8) acc[aa + g] = mfma16(fa[rho & 1][g], fb[rho & 1][aa], acc[aa + g]);
          if (aa < g && aa + g < 8) acc[aa + g] = mfma16(fa[rho & 1][aa], fb[rho & 1][g], acc[aa + g]);
        }
        if (rho < 3) { ldf(rho + 1, g); SBAR(); }
      }
    }
    STAMP(6);
    PRIO(ATAIL);
#pragma unroll
    for (int t = 0; t < 8; ++t) *(LAS f32x4*)(lds + EX + w * 8192 + t * 1024 + lane * 16) = acc[t];
    STAMP(17);
    BAR();
    STAMP(18);
#pragma unroll
    for (int i = 0; i < 2; ++i) {
      LAS char* p = lds + EX + (2 * w + i) * 1024 + lane * 16;
      const f32x4 y0 = *(LAS f32x4*)p + *(LAS f32x4*)(p + 8192), y1 = *(LAS f32x4*)(p + 16384) + *(LAS f32x4*)(p + 24576);
      const u32x4 v = {pkbf(y0.x, y1.x), pkbf(y0.y, y1.y), pkbf(y0.z, y1.z), pkbf(y0.w, y1.w)};
      ST_SC1(v, rs, WS_YX + (cb * 32 + 4 * (2 * w + i) + hq) * 8192 + jp * 256 + c16 * 16);
      const u32 ck = tag - row_sum(sum4(0u, v));
      if (c16 == 15) __builtin_amdgcn_raw_buffer_store_b64((u32x2){tag, ck}, rs, WS_MBY + (cb * 32 + 4 * (2 * w + i) + hq) * 256 + jp * 8, 0, 16);
    }
  }
  STAMP(7);
  {
    u32x4 y[2];
    const u32 mby = WS_MBY + a * 256 + (8 * w + lane) * 8;
#pragma nounroll
    for (int it = 0; it < SPIN_CAP; ++it) {
      __builtin_amdgcn_s_sleep(ASLEEP);
      mail = poll64(ws, mby, lane < 8);
      if (__ballot((u32)mail == tag) == 0xffull) break;
      COUNT(13);
    }
    STAMP(19);
#pragma nounroll
    for (int it = 0; it < SPIN_CAP; ++it) {
      asm volatile("" ::: "memory");
      y[0] = LD_SC1(rs, WS_YX + a * 8192 + w * 2048 + lane * 16); y[1] = LD_SC1(rs, WS_YX + a * 8192 + w * 2048 + 1024 + lane * 16);
      if (wave_sum(sum4(sum4((u32)(mail >> 32), y[0]), y[1])) == 8u * tag) break;
      COUNT(12);
    }
    STAMP(8);
#pragma unroll
    for (int i = 0; i < 2; ++i)
#pragma unroll
      for (int g = 0; g < 4; ++g) *(LAS u32*)(lds + (16 * g + (lane & 15)) * 144 + (8 * w + 4 * i + (lane >> 4)) * 4) = y[i][g];
    BAR();
#pragma unroll
    for (int i = 0; i < 2; ++i) {
      const u32x4 v = *(LAS u32x4*)(lds + (32 * i + (tid >> 3)) * 144 + (tid & 7) * 16);
      u32x4 o;
#pragma unroll
      for (int k = 0; k < 4; ++k) o[k] = pkbf(bflo(v[k]) + bflo(sk[4 * i + k]), bfhi(v[k]) + bfhi(sk[4 * i + k]));
      *(GAS u32x4*)(out + (u32)((64 * jp + 32 * i + (tid >> 3)) * 1792 + cb * 128 + (tid & 7) * 16)) = o;
    }
  }
  STAMP(9);
  STAMP_FLUSH();
}

__global__ void NOPK __launch_bounds__(256, 2)
fused_kernel(const void*, const void*, const void*, const void*, const void*, const void*, const void*, const void*, const void*,
             const void*, void*, void*) {
  __shared__ __attribute__((aligned(16))) char smem[81920];
  LAS char* lds = (LAS char*)smem;
  if ((blockIdx.x >> 5) == 7) {
    u64 pc;
    asm volatile("s_getpc_b64 %0" : "=s"(pc));
    if (threadIdx.x < CODE_LINES / 4)
      __builtin_amdgcn_global_load_lds((const GAS void*)((pc & ~127ull) + (threadIdx.x * 4 + ((blockIdx.x >> 3) & 3)) * 128), (LAS void*)lds, 4, 0, 0);
  }
  const __attribute__((address_space(4))) u64* ka = (const __attribute__((address_space(4))) u64*)__builtin_amdgcn_kernarg_segment_ptr();
  asm volatile("" : "+s"(ka));
  const char* u = (const char*)ka[0]; const GAS u32* logA = (const GAS u32*)ka[1]; const GAS u32* Aim = (const GAS u32*)ka[2];
  const GAS u32* Cre = (const GAS u32*)ka[3]; const GAS u32* Cim = (const GAS u32*)ka[4]; const GAS u32* logdt = (const GAS u32*)ka[5];
  const char* Dsk = (const char*)ka[6]; const char* x = (const char*)ka[7]; const char* wqkv = (const char*)ka[8];
  const char* wo = (const char*)ka[9]; char* out = (char*)ka[10]; char* ws = (char*)ka[11];
  asm volatile("" :: "s"(u), "s"(logA), "s"(Aim), "s"(Cre), "s"(Cim), "s"(logdt), "s"(Dsk), "s"(x), "s"(wqkv), "s"(wo), "s"(out), "s"(ws));
  const u32 epoch = request_epoch(ws);
  u32 pA, pI, pR, pC, ldt;
  int bid = blockIdx.x;
  if (bid >= 256) role_b<true>(lds, x, wqkv, out, ws, epoch);
  else if (bid < 224) {
    const int pi = bid * 256 + threadIdx.x;
    pA = __builtin_nontemporal_load(logA + pi); pI = __builtin_nontemporal_load(Aim + pi);
    pR = __builtin_nontemporal_load(Cre + pi); pC = __builtin_nontemporal_load(Cim + pi); ldt = logdt[bid];
  }
  asm volatile("" : "+s"(bid) :: "memory");
  if (bid >= 256) role_b<false>(lds, x, wqkv, out, ws, epoch);
  else if (bid < 224) role_a(lds, u, Dsk, wqkv, wo, out, ws, epoch, pA, pI, pR, pC, ldt);
  else {
    const int t = threadIdx.x;
    if (blockIdx.x == 224) {
      const u32 tag = take_tag(epoch);
      __builtin_amdgcn_s_sleep(127);
      wait_flags(ws, 256 + t, tag);
      wait_flags(ws, t < 224 ? t : 0, tag);
      BAR();
      if (t == 0) __hip_atomic_store((GAS u32*)ws, tag, RLX_AGENT);
    }
  }
}

extern "C" void kernel_launch(void* const* d_in, const int* in_sizes, int n_in, void* d_out, int out_size, void* d_ws,
                              size_t ws_size, hipStream_t stream) {
  fused_kernel<<<512, 256, 0, stream>>>(d_in[0], d_in[1], d_in[2], d_in[3], d_in[4], d_in[5], d_in[6], d_in[7], d_in[8], d_in[9], d_out, d_ws);
}
```

```cpp
#include <hip/hip_runtime.h>
#include <stdint.h>

typedef unsigned short u16;
typedef unsigned u32;
typedef unsigned long long u64;
typedef short s16x4 __attribute__((ext_vector_type(4)));
typedef __bf16 bf16x8 __attribute__((ext_vector_type(8)));
typedef __bf16 bf16x2 __attribute__((ext_vector_type(2)));
typedef _Float16 f16x8 __attribute__((ext_vector_type(8)));
typedef _Float16 f16x2 __attribute__((ext_vector_type(2)));
typedef float f32x16 __attribute__((ext_vector_type(16)));
typedef float f32x4 __attribute__((ext_vector_type(4)));
typedef float f32x2 __attribute__((ext_vector_type(2)));
typedef u32 u32x4 __attribute__((ext_vector_type(4)));
typedef u32 u32x2 __attribute__((ext_vector_type(2)));
#define LAS __attribute__((address_space(3)))
#define GAS __attribute__((address_space(1)))
#define DI __device__ __forceinline__
#if defined(__HIP_DEVICE_COMPILE__)
#define NOPK __attribute__((target("no-packed-fp32-ops")))
#else
#define NOPK
#endif
#define RLX_AGENT __ATOMIC_RELAXED, __HIP_MEMORY_SCOPE_AGENT

#define BMAP 1
#define PREFETCH 1
#define CODE_BEFORE 0
#define CODE_LINES 208
#define QEARLY 3
#define GATH_AT 18
#define WO_EARLY 0
#define WO_MID 12
#define WO_G 32
#define WO_E 4
#define KVSTEP 1
#define PUB_FIRST 0
#define UPOLL_AT 8
#define TMAP 1
#define BPRIO1 2
#define ABASE 0
#define ACONV 0
#define L2PF 0
#define NTA 1
#define NTX 0
#define W0FIRST 1
#define ORD 4
#define PF2AT 1
#define UMID 0
#define WFIRST 0
#define HEADC 1
#define ASLEEP 0
#define TDELAY 0
#define UEARLY 3
#define EPW 255
#define OUTAUX 16
#define TELL2_LATE 1
#define ROT 1
#define APRIO 3
#define ATAIL 1
#define BPRIO 2

constexpr u32 WS_MB1 = 73728;
constexpr u32 WS_MB2 = WS_MB1 + 32768;
constexpr u32 WS_MBU = WS_MB2 + 32768;
constexpr u32 WS_MBY = WS_MBU + 57344;
constexpr u32 WS_KVP = 1u << 18;
constexpr u32 WS_OB = WS_KVP + (1u << 20);
constexpr u32 WS_UX = WS_OB + (2u << 20);
constexpr u32 WS_YX = WS_UX + (2u << 20);
constexpr u32 WS_WQ = WS_YX + (2u << 20);
constexpr u32 WS_WO = WS_WQ + (1u << 19);
constexpr u32 WS_MBQ = WS_WO + 458752;
constexpr u32 WS_END = WS_WO + (1u << 19);
constexpr u32 WS_DBG = WS_END;
constexpr int SPIN_CAP = 1 << 15;

#define STAMP(i) do {} while (0)
#define STAMP_DECL
#define COUNT(i) do {} while (0)
#define STAMP_FLUSH() do {} while (0)

DI float bflo(u32 v) { return __uint_as_float(v << 16); }
DI float bfhi(u32 v) { return __uint_as_float(v & 0xffff0000u); }
DI u32 pkbf(float a, float b) { f32x2 p = {a, b}; return __builtin_bit_cast(u32, __builtin_convertvector(p, bf16x2)); }
DI u32 pkh(f32x2 p) { return __builtin_bit_cast(u32, __builtin_convertvector(p, f16x2)); }
DI float rbf(float a) { return bflo(pkbf(a, a)); }
DI u32 umax3(u32 a, u32 b, u32 c) { u32 m = a > b ? a : b; return m > c ? m : c; }
DI u32 sum4(u32 m, u32x4 v) { return (m + v.x + v.y) + v.z + v.w; }
DI u32 row_sum(u32 v) {
  v += __builtin_amdgcn_update_dpp(0u, v, 0x111, 0xf, 0xf, true); v += __builtin_amdgcn_update_dpp(0u, v, 0x112, 0xf, 0xf, true);
  v += __builtin_amdgcn_update_dpp(0u, v, 0x114, 0xf, 0xf, true); v += __builtin_amdgcn_update_dpp(0u, v, 0x118, 0xf, 0xf, true);
  return v;
}
DI u32 row_sum2(u32 a, u32 b) {
  a += __builtin_amdgcn_update_dpp(0u, a, 0x111, 0xf, 0xf, true); b += __builtin_amdgcn_update_dpp(0u, b, 0x101, 0xf, 0xf, true);
  a += __builtin_amdgcn_update_dpp(0u, a, 0x112, 0xf, 0xf, true); b += __builtin_amdgcn_update_dpp(0u, b, 0x102, 0xf, 0xf, true);
  a += __builtin_amdgcn_update_dpp(0u, a, 0x114, 0xf, 0xf, true); b += __builtin_amdgcn_update_dpp(0u, b, 0x104, 0xf, 0xf, true);
  a += __builtin_amdgcn_update_dpp(0u, a, 0x118, 0xf, 0xf, true); b += __builtin_amdgcn_update_dpp(0u, b, 0x108, 0xf, 0xf, true);
  return (threadIdx.x & 8) ? a : b;
}
DI u32 wave_sum(u32 v) {
  v = row_sum(v);
  return (__builtin_amdgcn_readlane(v, 15) + __builtin_amdgcn_readlane(v, 31)) + (__builtin_amdgcn_readlane(v, 47) + __builtin_amdgcn_readlane(v, 63));
}
DI u32 wave_umax(u32 v) {
  u32 t;
  t = __builtin_amdgcn_update_dpp(0u, v, 0x111, 0xf, 0xf, false); v = v > t ? v : t;
  t = __builtin_amdgcn_update_dpp(0u, v, 0x112, 0xf, 0xf, false); v = v > t ? v : t;
  t = __builtin_amdgcn_update_dpp(0u, v, 0x114, 0xf, 0xf, false); v = v > t ? v : t;
  t = __builtin_amdgcn_update_dpp(0u, v, 0x118, 0xf, 0xf, false); v = v > t ? v : t;
  const u32 a = __builtin_amdgcn_readlane(v, 15), b = __builtin_amdgcn_readlane(v, 31), c = __builtin_amdgcn_readlane(v, 47), d = __builtin_amdgcn_readlane(v, 63);
  const u32 ab = a > b ? a : b, cd = c > d ? c : d;
  return ab > cd ? ab : cd;
}

DI u32 request_epoch(const char* ws) { u32 e; asm volatile("s_load_dword %0, %1, 0x0" : "=s"(e) : "s"(ws) : "memory"); return e; }
DI u32 take_tag(u32 e) { u32 r; asm volatile("s_waitcnt lgkmcnt(0)\n\ts_and_b32 %0, %1, -2" : "=s"(r) : "s"(e) : "memory", "scc"); return r * 1664525u + 2027808447u; }
#define PSCOPE __HIP_MEMORY_SCOPE_AGENT
DI u32 poll32(char* ws, u32 off, bool on) { u32 z = 0, r = 0; asm volatile("" : "+v"(z)); if (on) r = __hip_atomic_fetch_add((GAS u32*)(ws + off), z, __ATOMIC_RELAXED, PSCOPE); return r; }
DI u64 poll64(char* ws, u32 off, bool on) { u64 z = 0, r = 0; asm volatile("" : "+v"(z)); if (on) r = __hip_atomic_fetch_add((GAS u64*)(ws + off), z, __ATOMIC_RELAXED, PSCOPE); return r; }
#define SBAR() __builtin_amdgcn_sched_barrier(0)
#define PRIO(n) do { SBAR(); __builtin_amdgcn_s_setprio(n); SBAR(); } while (0)
#define PIN(x) asm volatile("" : "+v"(x))
DI f32x16 mfma32(u32x4 a, u32x4 b, f32x16 c) { return __builtin_amdgcn_mfma_f32_32x32x16_bf16(__builtin_bit_cast(bf16x8, a), __builtin_bit_cast(bf16x8, b), c, 0, 0, 0); }
DI f32x16 mfma32h(u32x4 a, u32x4 b, f32x16 c) { return __builtin_amdgcn_mfma_f32_32x32x16_f16(__builtin_bit_cast(f16x8, a), __builtin_bit_cast(f16x8, b), c, 0, 0, 0); }
DI f32x4 mfma16h(u32x4 a, u32x4 b, f32x4 c) { return __builtin_amdgcn_mfma_f32_16x16x32_f16(__builtin_bit_cast(f16x8, a), __builtin_bit_cast(f16x8, b), c, 0, 0, 0); }
DI f32x4 mfma16(u32x4 a, u32x4 b, f32x4 c) { return __builtin_amdgcn_mfma_f32_16x16x32_bf16(__builtin_bit_cast(bf16x8, a), __builtin_bit_cast(bf16x8, b), c, 0, 0, 0); }
DI u32x2 tr_read(LAS char* p) { return __builtin_bit_cast(u32x2, __builtin_amdgcn_ds_read_tr16_b64_v4i16((LAS s16x4*)p)); }
#define RSRC(p, n) __builtin_amdgcn_make_buffer_rsrc((void*)(p), 0, (n), 0x00020000)
#define DMA16(rs, voff, soff, dst, aux) __builtin_amdgcn_raw_ptr_buffer_load_lds((rs), (LAS void*)(dst), 16, (voff), (soff), 0, (aux))
#define WAITVM(n) do { asm volatile("" ::: "memory"); __builtin_amdgcn_s_waitcnt(0x0F70 | ((n) & 15) | (((n) >> 4) << 14)); asm volatile("" ::: "memory"); } while (0)
#define VMCNT(n) asm volatile("s_waitcnt vmcnt(" #n ")" ::: "memory")
#define LGKM0() asm volatile("s_waitcnt lgkmcnt(0)" ::: "memory")
#define BAR() do { asm volatile("s_waitcnt lgkmcnt(0)" ::: "memory"); __builtin_amdgcn_s_barrier(); asm volatile("" ::: "memory"); } while (0)
#define ST_SC1(v, rs, off) __builtin_amdgcn_raw_buffer_store_b128((v), (rs), (off), 0, 16)
#define LD_SC1(rs, off) __builtin_amdgcn_raw_buffer_load_b128((rs), (off), 0, 16)
#define LDS_SC1(rs, off, soff) __builtin_amdgcn_raw_buffer_load_b128((rs), (off), (soff), 16)
#define ST1_SC1(v, rs, off) __builtin_amdgcn_raw_buffer_store_b32((v), (rs), (off), 0, 16)
#define LD1_SC1(rs, off) __builtin_amdgcn_raw_buffer_load_b32((rs), (off), 0, 16)

typedef unsigned short u16x2 __attribute__((ext_vector_type(2)));
DI u32 phi1(float a, float b) {
  const u32 t = pkbf(a, b);
  const f32x2 x = {bflo(t), bfhi(t)};
  const f32x2 y = x * 1.4426950408889634f;
  const float e0 = __builtin_amdgcn_fmed3f(__builtin_amdgcn_exp2f(y.x), 0.f, 1.f);
  const float e1 = __builtin_amdgcn_fmed3f(__builtin_amdgcn_exp2f(y.y), 0.f, 1.f);
  const f32x2 x1 = x + 1.0f;
  return pkbf(__builtin_fmaxf(x1.x, e0), __builtin_fmaxf(x1.y, e1));
}
DI u32 pkmin(u32 a, u32 b) { return __builtin_bit_cast(u32, __builtin_elementwise_min(__builtin_bit_cast(u16x2, a), __builtin_bit_cast(u16x2, b))); }
DI void phi_eps(u32x4 (&p)[2], u32 mn) {
  if (__any((mn & 0xffffu) < 0x3A80u || mn < 0x3A800000u)) {
#pragma unroll
    for (int j = 0; j < 8; ++j) { const u32 r = p[j >> 2][j & 3]; p[j >> 2][j & 3] = pkbf(bflo(r) + 1e-6f, bfhi(r) + 1e-6f); }
  }
}

template <bool HEAD>
DI void role_b(LAS char* lds, const char* __restrict__ x, const char* __restrict__ wqkv, char* __restrict__ out, char* ws, u32 epoch) {
  const int b = blockIdx.x - 256, tid = threadIdx.x, lane = tid & 63;
  const int w = __builtin_amdgcn_readfirstlane(tid >> 6);
  STAMP_DECL;
  STAMP(0);
  const int rb = 4 * ((b & 7) >> 1) + (b >> 6), h = (b & 1) * 8 + ((b >> 3) & 7);
  const int start = (int)((0x1065432106543210ull >> (4 * ((h & 7) + 2 * (rb & 3)))) & 7);
  const __amdgpu_buffer_rsrc_t rs = RSRC(ws, WS_END), rx = RSRC(x, 2048 * 896), rw = RSRC(wqkv, 448 * 2688);
  constexpr int WR = 49152, KVI = WR + 4096, OST = 16384, OTR = WR;
#define XS(i) ((i) * 16384)
#define QS(cc) ((cc) < 2 ? WR + 24576 + (cc) * 4096 : (cc) < 6 ? XS(1) + ((cc) - 2) * 1024 : WR)
#define QD(cc) ((cc) >= 2 && (cc) < 6 ? 4096 : 1024)
  const int wsb = (56 * h) & ~15;
  const int coff = (h & 1) * 4;
  const int wone = (h & 1) ? 0 : 28;
  const u32 xv0 = (128 * rb + 32 * w + (lane >> 3)) * 896 + (((lane & 7) ^ (lane >> 4)) << 4);
  const u32 xv1 = (xv0 ^ 64) + 8 * 896;
  const u32 wv = (16 * w + (lane >> 2)) * 2688 + wsb + (lane & 3) * 16;
  const u32 l16 = lane * 16;
  int ch_[7];
  auto chunk = [&](int cc) { return ch_[cc]; };
  const int gi = h + rb;
  const u32 ordc = ORD == 1 ? 0x6543210u : ORD == 2 ? ((gi & 1) ? 0x5461032u : 0x6543210u) : ORD == 3 ? ((gi & 1) ? 0x6452301u : 0x6543210u)
                 : ((gi & 2) ? ((gi & 1) ? 0x4652103u : 0x5461032u) : ((gi & 1) ? 0x4650321u : 0x6543210u));
  auto next_chunk = [&](int cc) { int c = (ordc >> (4 * cc)) & 7; asm volatile("" : "+s"(c)); ch_[cc] = c; };
  auto issue_x = [&](int cc, int i) { DMA16(rx, (i & 1) ? xv1 : xv0, chunk(cc) * 128 + (i >> 1) * (16 * 896), lds + XS(cc % 3) + w * 4096 + i * 1024, NTX); };
  auto issue_w = [&](int cc, int i) { DMA16(rw, wv, chunk(cc) * (64 * 2688) + 896 * (i + 1), lds + WR + (cc & 3) * 8192 + i * 4096 + w * 1024, 0); };
  bool qok = true;
  u32 qso[7];
  auto issue_q = [&](int cc) { DMA16(rs, l16, qok ? qso[cc] : WS_END, lds + QS(cc) + w * QD(cc), 16); };
#pragma unroll
  for (int cc = 0; cc < 2; ++cc) {
    next_chunk(cc);
    if (HEAD == (cc < HEADC)) { issue_w(cc, 0); issue_w(cc, 1); issue_x(cc, 0); issue_x(cc, 1); issue_x(cc, 2); issue_x(cc, 3); }
    SBAR();
  }
  if (HEAD) return;
  next_chunk(2); next_chunk(3); next_chunk(4); next_chunk(5); next_chunk(6);
  __builtin_amdgcn_s_setprio(BPRIO1);
  const int r32 = lane & 31, half = lane >> 5;
  const int xrd = w * 4096 + r32 * 128 + ((half ^ ((r32 >> 1) & 7)) << 4);
  LAS char* whi = lds + WR + (8 * half + ((lane >> 2) & 3)) * 64 + ((lane >> 4) & 1) * 32 + (lane & 3) * 8;
  PIN(whi);
  const int rt = h >> 2, ct = (h + ((h & 8) ? 2 : 1)) & 3;
  u32 wo_src = WS_WO + (4 * ct + w < 14 ? 4 * ct + w : 13) * 32768, kvp_src = WS_KVP + h * 65536 + w * 16384;
  u32 ob_dst = WS_OB + ((rb * 4 + w) * 16 + h) * 2048 + l16, ob_src = WS_OB + ((rb * 4 + rt) * 16 + 4 * w) * 2048;
  asm volatile("" : "+s"(wo_src), "+s"(kvp_src), "+s"(ob_src));
  PIN(ob_dst);
  u32 tell1 = lane < 4 ? WS_MB1 + (h * 16 + 4 * w + lane) * 128 + rb * 4 : WS_END;
  u32 tell2 = lane < 4 ? WS_MB2 + (rb * 16 + 4 * w + lane) * 128 + h * 4 : WS_END;
  u32 mb1 = WS_MB1 + (h * 16 + rb) * 128 + ((lane + rb) & 15) * 4, mb2 = WS_MB2 + (rb * 16 + h) * 128 + ((4 * w + lane) & 15) * 4;
  u32 kvp_dst = kvp_src + rb * 1024 + l16, kvs[16];
#pragma unroll
  for (int p = 1; p < 16; ++p) { kvs[p] = kvp_src + ((p + rb) & 15) * 1024; asm volatile("" : "+s"(kvs[p])); }
  const int dw = 8 * w + 4 * half - coff;
  u32 rowmask = dw < 0 || dw >= 28 ? 0u : ~0u;
  const bool padlo = (h & 1) && !half, padhi = !(h & 1) && half;
  const int oi = (r32 & 3) + 4 * (r32 >> 3);
  LAS char* kvrd = lds + KVI + ((oi < 14 ? 14 * ((r32 >> 2) & 1) + oi + coff : wone) + 32 * half) * 32;
  LAS char* qlo = lds + l16; LAS char* qhi = lds + WR + l16;
  PIN(tell1); PIN(tell2); PIN(mb1); PIN(mb2); PIN(kvp_dst); PIN(rowmask); PIN(kvrd); PIN(qhi);
  const bool ck = lane == wone + 1;
  u32 mbq = WS_MBQ + (h * 16 + rb) * 128 + (4 * (lane & 7) + w) * 4, mail = 0, tag = 0;
  PIN(mbq);
  u32x4 xf[28], wf[2][8];
  f32x16 ak = {}, av = {};
  auto rd = [&](int cc, int i) {
    if ((i & 1) == 0) xf[cc * 4 + (i >> 1)] = *(LAS u32x4*)(lds + XS(cc % 3) + (xrd ^ ((i >> 1) << 5)));
    LAS char* p = whi + (cc & 3) * 8192 + (i >> 1) * 1024 + (i & 1) * 4096;
    const u32x2 lo = tr_read(p), hi = tr_read(p + 256);
    wf[cc & 1][i] = (u32x4){lo.x, lo.y, hi.x, hi.y};
  };
#pragma unroll
  for (int cc = 0; cc < 6; ++cc) {
    if (cc == 0) VMCNT(6); else if (cc < 5) VMCNT(8); else VMCNT(7);
    BAR();
    if (cc == 5) __builtin_amdgcn_s_setprio(BPRIO);
    if (cc == 0) { STAMP(1); tag = take_tag(epoch); }
    if (cc == 2) { STAMP(23); } if (cc == 4) { STAMP(24); } if (cc == 5) { STAMP(25); }
    if (cc == 5) {
#pragma unroll
      for (int i = 0; i < 8; ++i) { rd(cc, i); if (i < 7) { qso[i] = WS_WQ + (h * 28 + 4 * chunk(i) + w) * 1024; asm volatile("" : "+s"(qso[i])); } }
      SBAR();
    }
#pragma unroll
    for (int i = 0; i < 8; ++i) {
      if (cc > 0) {
        if (i & 1) av = mfma32(xf[(cc - 1) * 4 + (i >> 1)], wf[(cc - 1) & 1][i], av);
        else ak = mfma32(xf[(cc - 1) * 4 + (i >> 1)], wf[(cc - 1) & 1][i], ak);
      }
      if (cc < 5) rd(cc, i);
      if (cc == 0) { if (i < 2) issue_w(2, i); else if (i < 6) issue_x(2, i - 2); else issue_w(3, i - 6); }
      else if (cc <= 4 && i < 4) issue_x(cc + 2, i);
      else if (cc <= 3 && i < 6) issue_w(cc + 3, i - 4);
      else if (cc == 4 && i == 4) mail = poll32(ws, mbq, lane < 7);
      SBAR();
    }
#pragma unroll
    for (int s = 3; s >= 0; --s) { PIN(wf[cc & 1][2 * s + 1]); PIN(wf[cc & 1][2 * s]); PIN(xf[cc * 4 + s]); }
    if (cc > 0) { PIN(ak); PIN(av); }
  }
  VMCNT(1);
  BAR();
  STAMP(26);
  rd(6, 0); rd(6, 2); rd(6, 4); rd(6, 6);
  SBAR();
#pragma unroll
  for (int s = 0; s < 4; ++s) ak = mfma32(xf[20 + s], wf[1][2 * s], ak);
  PIN(ak);
#pragma unroll
  for (int s = 3; s >= 0; --s) { PIN(wf[0][2 * s]); PIN(xf[24 + s]); }
  rd(6, 1); rd(6, 3); rd(6, 5); rd(6, 7);
  SBAR();
#pragma unroll
  for (int s = 0; s < 4; ++s) ak = mfma32(xf[24 + s], wf[0][2 * s], ak);
  PIN(ak);
  SBAR();
  WAITVM(0);
  PIN(mail);
  qok = __ballot(mail == tag) == 0x7full;
  STAMP(2);
  u32x4 wof[32], d[16], pq[2];
  auto load_wo = [&](int k) { wof[k] = __builtin_amdgcn_raw_buffer_load_b128(rs, l16 + (k & 3) * 1024, wo_src + (k >> 2) * 4096, 16); };
  {
    u32x4 pk[2], pv[2];
    u32 mn = ~0u;
#pragma unroll
    for (int j = 0; j < 8; j += 2) {
      av = mfma32(xf[20 + j], wf[j < 4][2 * (j & 3) + 1], av);
      issue_q(j); if (j < 6) issue_q(j + 1);
      SBAR();
      pk[j >> 2][j & 3] = phi1(ak[2 * j], ak[2 * j + 1]);
      av = mfma32(xf[21 + j], wf[j < 4][2 * (j & 3) + 3], av);
      pk[j >> 2][(j & 3) + 1] = phi1(ak[2 * j + 2], ak[2 * j + 3]);
      mn = pkmin(pkmin(mn, pk[j >> 2][j & 3]), pk[j >> 2][(j & 3) + 1]);
      PIN(mn); PIN(av);
      SBAR();
    }
    phi_eps(pk, mn);
    STAMP(11);
#pragma unroll
    for (int j = 0; j < 8; ++j) { const u32 v = pkbf(av[2 * j], av[2 * j + 1]); pv[j >> 2][j & 3] = r32 == wone ? 0x3F803F80u : v; }
    f32x16 kv = {};
    kv = mfma32(pk[0], pv[0], kv); kv = mfma32(pk[1], pv[1], kv);
    ST1_SC1(tag, rs, tell1);
#pragma unroll
    for (int g = 0; g < 4; ++g) *(LAS f32x4*)(lds + XS(2) + w * 4096 + g * 1024 + l16) = (f32x4){kv[4 * g], kv[4 * g + 1], kv[4 * g + 2], kv[4 * g + 3]};
    STAMP(12);
    BAR();
    f32x4 s4 = *(LAS f32x4*)(lds + XS(2) + tid * 16);
#pragma unroll
    for (int ww = 1; ww < 4; ++ww) s4 += *(LAS f32x4*)(lds + XS(2) + ww * 4096 + tid * 16);
    u32x4 v = __builtin_bit_cast(u32x4, s4);
    const u32 part = v.y + v.z + v.w;
    const u32 c = tag - wave_sum(ck ? part : part + v.x);
    if (ck) v.x = c;
    ST_SC1(v, rs, kvp_dst);
    d[0] = v;
  }
  if (!qok) {
#pragma nounroll
    for (int it = 0; it < SPIN_CAP && __ballot(poll32(ws, mbq, lane < 7) == tag) != 0x7full; ++it) {}
    qok = true;
#pragma unroll
    for (int cc = 0; cc < 7; ++cc) issue_q(cc);
    WAITVM(0);
    COUNT(13);
  }
  u32 go, need = 0xfffeu;
  mail = poll32(ws, mb1, lane < 16);
#pragma unroll
  for (int k = 0; k < WO_E; ++k) load_wo(k);
  WAITVM(WO_E + 3);
  BAR();
  STAMP(3);
#define QP(cc) (QS(cc) >= WR ? qhi + (QS(cc) - WR) : qlo + QS(cc))
  auto load_kv = [&](int p, bool on = true) { d[p] = __builtin_amdgcn_raw_buffer_load_b128(rs, l16, on ? kvs[p] : WS_END, 16); };
  {
    f32x16 aq = {};
    u32x4 qf[2][4];
    u32 qsum = 0;
    auto read_q = [&](int cc) {
#pragma unroll
      for (int s = 0; s < 4; ++s) qf[cc & 1][s] = *(LAS u32x4*)(QP(cc) + s * QD(cc));
      SBAR();
    };
    read_q(0);
#pragma unroll
    for (int cc = 0; cc < 7; ++cc) {
      if (cc < 6) read_q(cc + 1);
#pragma unroll
      for (int s = 0; s < 4; ++s) {
        aq = mfma32(qf[cc & 1][s], xf[cc * 4 + s], aq);
        qsum = sum4(qsum, qf[cc & 1][s]);
        if (s & 1) load_wo(WO_E + 2 * cc + (s >> 1));
        PIN(aq); PIN(qsum);
        SBAR();
      }
    }
    if (wave_sum(qsum) != 28u * tag) {
      aq = (f32x16){};
#pragma nounroll
      for (int k2 = 0; k2 < 28; ++k2) {
        const int ks = 4 * ((ordc >> (k2 & 28)) & 7) + (k2 & 3);
        u32x4 f;
#pragma nounroll
        for (int it = 0; it < SPIN_CAP; ++it) {
          asm volatile("" ::: "memory");
          f = LDS_SC1(rs, l16, WS_WQ + (h * 28 + ks) * 1024);
          if (wave_sum(sum4(0u, f)) == tag) break;
        }
        aq = mfma32(f, __builtin_amdgcn_raw_buffer_load_b128(rx, (128 * rb + 32 * w + r32) * 896 + half * 16, ks * 32, 0), aq);
      }
      COUNT(15);
    }
    STAMP(14);
    go = need & (u32)__ballot(mail == tag); need &= ~go;
    mail = poll32(ws, mb1, lane < 16 && need != 0);
    STAMP(15);
    u32 mn = ~0u;
#pragma unroll
    for (int j = 0; j < 8; ++j) {
      if (j) load_kv(2 * j, go >> (2 * j) & 1);
      load_kv(2 * j + 1, go >> (2 * j + 1) & 1);
      u32 r = phi1(aq[2 * j], aq[2 * j + 1]);
      if (j < 2) r = padlo ? 0x3F803F80u : r; else if (j >= 6) r = padhi ? 0x3F803F80u : r;
      pq[j >> 2][j & 3] = r;
      mn = pkmin(mn, r);
      PIN(mn);
      SBAR();
    }
    phi_eps(pq, mn);
    PIN(pq[0]); PIN(pq[1]);
  }
  STAMP(5);
  PIN(mail);
  STAMP(16);
#pragma nounroll
  for (int i2 = 0; i2 < SPIN_CAP && need != 0; ++i2) {
    go = need & (u32)__ballot(mail == tag); need &= ~go;
#pragma unroll
    for (int p = 1; p < 16; ++p) if (go >> p & 1) load_kv(p);
    if (need != 0) { mail = poll32(ws, mb1, lane < 16); WAITVM(0); }
    COUNT(14);
  }
#pragma unroll
  for (int k = WO_E + 14; k < WO_G; ++k) load_wo(k);
  WAITVM(WO_G - WO_E - 14);
  STAMP(18);
#pragma nounroll
  for (int it = 0; it < SPIN_CAP; ++it) {
    u32 m = 0;
#pragma unroll
    for (int p = 0; p < 16; ++p) m = sum4(m, d[p]);
    if (wave_sum(m) == 16u * tag) break;
    asm volatile("" ::: "memory");
#pragma unroll
    for (int p = 1; p < 16; ++p) load_kv(p);
    WAITVM(0);
    COUNT(11);
  }
  mail = poll32(ws, mb2, lane < 4);
  SBAR();
  {
    f32x4 s4 = __builtin_bit_cast(f32x4, d[0]);
#pragma unroll
    for (int p = 1; p < 16; ++p) s4 += __builtin_bit_cast(f32x4, d[p]);
    *(LAS u32x2*)(lds + KVI + lane * 32 + w * 8) = (u32x2){pkbf(s4.x, s4.y) & rowmask, pkbf(s4.z, s4.w) & rowmask};
  }
  STAMP(6);
  BAR();
  {
    const u32x4 a0 = *(LAS u32x4*)kvrd, a1 = *(LAS u32x4*)(kvrd + 16);
    if (!TELL2_LATE) ST1_SC1(tag, rs, tell2);
    f32x16 nm = {};
    nm = mfma32(a0, pq[0], nm); nm = mfma32(a1, pq[1], nm);
    const float den = rbf(rbf(nm[14]) + 1e-6f);
    const float rc = __builtin_amdgcn_rcpf(den);
    u32 o[8];
#pragma unroll
    for (int j = 0; j < 7; ++j) { const u32 t = pkbf(nm[2 * j], nm[2 * j + 1]); o[j] = pkbf(bflo(t) * rc, bfhi(t) * rc); }
    o[7] = (tag - ((o[0] + o[1] + o[2]) + (o[3] + o[4] + o[5]) + o[6])) & ~0x40004000u;
    __builtin_amdgcn_raw_buffer_store_b128((u32x4){o[0], o[1], o[2], o[3]}, rs, ob_dst, 0, 16);
    __builtin_amdgcn_raw_buffer_store_b128((u32x4){o[4], o[5], o[6], o[7]}, rs, ob_dst + 1024, 0, 16);
    if (TELL2_LATE) ST1_SC1(tag, rs, tell2);
    SBAR();
  }
  STAMP(7);
  auto load_o = [&](int i) {
    DMA16(rs, l16, ob_src + i * 2048, lds + OST + (8 * w + 2 * i) * 1024, 16);
    DMA16(rs, l16, ob_src + i * 2048 + 1024, lds + OST + (8 * w + 2 * i + 1) * 1024, 16);
  };
#pragma unroll
  for (int k = WO_G; k < 32; ++k) load_wo(k);
  WAITVM(35 - WO_G);
  PIN(mail);
  need = 15u;
  bool wo_done = false;
#pragma nounroll
  for (int i2 = 0; i2 < SPIN_CAP; ++i2) {
    go = need & (u32)__ballot(mail == tag); need &= ~go;
    if (WO_G < 32 && !wo_done) WAITVM(0);
#pragma unroll
    for (int i = 0; i < 4; ++i) if (go >> i & 1) load_o(i);
    mail = poll32(ws, mb2, lane < 4 && need != 0);
    if (!wo_done) {
#pragma nounroll
      for (int it = 0; it < SPIN_CAP; ++it) {
        u32 m = 0;
#pragma unroll
        for (int k = 0; k < 32; ++k) m = sum4(m, wof[k]);
        if (__all(m == 16u * tag)) break;
        asm volatile("" ::: "memory");
#pragma unroll
        for (int k = 0; k < 32; ++k) load_wo(k);
        WAITVM(0);
      }
#pragma unroll
      for (int k = 1; k < 32; k += 2) { wof[k].w = 0u; PIN(wof[k]); }
      wo_done = true;
    }
    WAITVM(0);
    COUNT(12);
    if (need == 0) break;
  }
  STAMP(8);
  f32x16 acc;
#pragma nounroll
  for (int it = 0; it < SPIN_CAP; ++it) {
    BAR();
    STAMP(9);
    u32x4 of[32];
    u32 m = 0;
#pragma unroll
    for (int k = 0; k < 6; ++k) of[k] = *(LAS u32x4*)(lds + OST + k * 1024 + l16);
    acc = (f32x16){};
    SBAR();
#pragma unroll
    for (int k = 0; k < 32; ++k) {
      acc = mfma32(wof[k], of[k], acc);
      if (k + 6 < 32) of[k + 6] = *(LAS u32x4*)(lds + OST + (k + 6) * 1024 + l16);
      m = sum4(m, of[k]);
      PIN(m);
      SBAR();
    }
    if (__all(((16u * tag - m) & 0x3FF83FFFu) == 0u)) break;
    BAR();
    load_o(0); load_o(1); load_o(2); load_o(3);
    WAITVM(0);
    COUNT(13);
  }
  STAMP(22);
  {
    LAS char* p = lds + OTR + r32 * 272 + w * 64 + half * 32;
    *(LAS u32x4*)p = (u32x4){pkbf(acc[0], acc[1]), pkbf(acc[2], acc[3]), pkbf(acc[4], acc[5]), pkbf(acc[6], acc[7])};
    *(LAS u32x4*)(p + 16) = (u32x4){pkbf(acc[8], acc[9]), pkbf(acc[10], acc[11]), pkbf(acc[12], acc[13]), pkbf(acc[14], acc[15])};
  }
  BAR();
  if (ct < 3 || (tid & 15) < 8) {
#pragma unroll
    for (int i = 0; i < 2; ++i)
      __builtin_amdgcn_raw_buffer_store_b128(*(LAS u32x4*)(lds + OTR + (16 * i + (tid >> 4)) * 272 + (tid & 15) * 16), RSRC(out, 2048 * 1792),
                                             (128 * rb + 32 * rt + 16 * i + (tid >> 4)) * 1792 + 896 + ct * 256 + (tid & 15) * 16, 0, OUTAUX);
  }
  if (b == EPW && tid == 0) __hip_atomic_store((GAS u32*)ws, tag, RLX_AGENT);
  STAMP(10);
  STAMP_FLUSH();
}

DI u32 hmul1(u32 a, u32 b) { const f16x2 x = __builtin_bit_cast(f16x2, a), y = __builtin_bit_cast(f16x2, b); return __builtin_bit_cast(u32, (f16x2){x.x, x.x} * y); }
DI u32 hmul2(u32 a, u32 b, u32 t) {
  const f16x2 x = __builtin_bit_cast(f16x2, a), y = __builtin_bit_cast(f16x2, b);
  return __builtin_bit_cast(u32, __builtin_elementwise_fma((f16x2){x.y, x.y}, (f16x2){-y.y, y.x}, __builtin_bit_cast(f16x2, t)));
}
DI u32 hmul(u32 a, u32 b) { return hmul2(a, b, hmul1(a, b)); }
DI f32x2 csq(f32x2 a) { return (f32x2){__builtin_fmaf(a.x, a.x, -a.y * a.y), (a.x + a.x) * a.y}; }
DI float exp_acc(float x) {
  const float hi = x * 1.4426950408889634f, lo = __builtin_fmaf(x, 1.9259629911266175e-8f, __builtin_fmaf(x, 1.4426950408889634f, -hi));
  const float e = __builtin_amdgcn_exp2f(hi);
  return __builtin_fmaf(e, lo * 0.6931471805599453f, e);
}
DI f32x2 rot(f32x2 b) { return (f32x2){-b.y, b.x}; }
DI f32x2 cm1(f32x2 a, f32x2 b) { return (f32x2){a.x, a.x} * b; }
DI f32x2 cm2(f32x2 a, f32x2 br, f32x2 t) { return __builtin_elementwise_fma((f32x2){a.y, a.y}, br, t); }
DI f32x2 cexp_p(float a2, float f, float p) {
  const float m = __builtin_amdgcn_exp2f(a2 * p), ph = __builtin_amdgcn_fractf(f * p);
  return (f32x2){m, m} * (f32x2){__builtin_amdgcn_cosf(ph), __builtin_amdgcn_sinf(ph)};
}
DI void role_a(LAS char* lds, const char* __restrict__ u, const char* __restrict__ Dsk, const char* __restrict__ wqkv, const char* __restrict__ wo, char* __restrict__ out, char* ws, u32 epoch,
               u32 pA, u32 pI, u32 pR, u32 pC, u32 ldt) {
  const int a = blockIdx.x, cb = a >> 5, jp = a & 31, tid = threadIdx.x, lane = tid & 63;
  const int w = __builtin_amdgcn_readfirstlane(tid >> 6);
  STAMP_DECL;
  STAMP(0);
  __builtin_amdgcn_s_setprio(APRIO);
  constexpr int SROW = 528, STG = 32 * SROW, UC = 0, UCS = 4160, RV = 36864, RVS = 4608, EX = RV + 2 * RVS, UIM = 4 * STG;
  const int PKS = UIM + 8192 + (w ? 1024 + w * 1024 : 0);
  const __amdgpu_buffer_rsrc_t rs = RSRC(ws, WS_END);
  const int ch = w >> 1, hf = w & 1, d = 2 * a + ch;
  const int r32 = lane & 31, half = lane >> 5;
  const int pkh_ = w == 0 ? (a & 15) : 2 * (a & 7) + (w - 1);
  {
    const __amdgpu_buffer_rsrc_t rp = RSRC(w == 0 ? wo : wqkv, w == 0 ? 448 * 896 : w < 3 ? 448 * 2688 : 0);
    const u32 vo = (lane >> 2) * (w == 0 ? 896 : 2688) + (lane & 3) * 16;
    const u32 so = w == 0 ? 28 * 896 * pkh_ + (a >> 4) * 64 : 16 * 2688 * (a >> 3) + ((56 * pkh_) & ~15);
    DMA16(rp, vo, so, lds + PKS, 0);
    DMA16(rp, vo, w == 0 ? so + 16 * 896 : 448 * 2688, lds + (w == 0 ? PKS + 1024 : UIM + 8192 + 5120), 0);
  }
  u32x4 dk;
  auto load_tile = [&]() {
#pragma unroll
    for (int i = 0; i < 2; ++i)
      DMA16(RSRC(u, 2048 * 896), (tid >> 3) * 896 + (((tid & 7) ^ ((tid >> 5) & 7)) << 4), (64 * jp + 32 * i) * 896 + cb * 128, lds + UIM + (32 * i + 8 * w) * 128, NTA ? 2 : 0);
    dk = *(const GAS u32x4*)(Dsk + cb * 128 + (tid & 7) * 16);
  };
  PRIO(ABASE);
  int tix[8];
#pragma unroll
  for (int j = 0; j < 8; ++j) {
    const int p = (int)rbf((float)(2047 - 8 * tid - j));
    const int low = p & 7, blk = p >> 8;
    const int k4 = (blk < 4 ? blk : 4) * 4;
    const int sh = (0x32210 >> k4) & 15;
    const int base = blk + ((0xCBA70 >> k4) & 15);
    tix[j] = ((base + (low >> sh)) * 32 + ((p >> 3) & 31)) * 4;
    PIN(tix[j]);
  }
  const u32 tag = take_tag(epoch);
  auto publish_weights = [&]() {
  PRIO(APRIO);
  BAR();
  if (w == 0) {
    const int q = (lane >> 2) & 3, gam = 4 * ((lane >> 4) & 1) + (lane & 3), ng = 4 * (gam & 1) + (gam >> 1);
    LAS char* p = lds + PKS + (14 * half + q) * 64 + 8 * ng;
    const u32x2 r0 = tr_read(p), r1 = tr_read(p + 256), r2 = tr_read(p + 512), r3 = tr_read(p + 768);
    const u32 off = WS_WO + ((a >> 4) * 16 + pkh_) * 2048 + lane * 16;
    ST_SC1(((u32x4){r0.x, r0.y, r1.x, r1.y}), rs, off);
    ST_SC1(((u32x4){r2.x, r2.y, r3.x, tag - ((r0.x + r0.y + r1.x) + (r1.y + r2.x + r2.y) + r3.x)}), rs, off + 1024);
  } else if (w < 3) {
    LAS char* p = lds + PKS + (8 * half + ((lane >> 2) & 3)) * 64 + ((lane >> 4) & 1) * 32 + (lane & 3) * 8;
    const u32x2 r0 = tr_read(p), r1 = tr_read(p + 256);
    u32x4 v = {r0.x, r0.y, r1.x, r1.y};
    const bool ck = lane == ((pkh_ & 1) ? 1 : 29);
    if (ck) v = (u32x4){0u, 0u, 0u, 0u};
    const u32 c = tag - wave_sum(sum4(0u, v));
    if (ck) v.x = c;
    ST_SC1(v, rs, WS_WQ + (pkh_ * 28 + (a >> 3)) * 1024 + lane * 16);
    if (lane < 16) ST1_SC1(tag, rs, WS_MBQ + (pkh_ * 16 + lane) * 128 + (a >> 3) * 4);
  }
  PRIO(ABASE);
  };
  auto publish_u = [&]() {
  PRIO(APRIO);
  BAR();
  {
    const int n = tid & 15;
    u32x4 v[2];
#pragma unroll
    for (int i = 0; i < 2; ++i) {
      const int jj = 16 * i + (tid >> 4);
      LAS char* p = lds + UIM + 4 * n * 128 + (((jj >> 2) ^ (n & 7)) << 4) + (jj & 3) * 4;
      v[i] = (u32x4){*(LAS u32*)p, *(LAS u32*)(p + 128), *(LAS u32*)(p + 256), *(LAS u32*)(p + 384)};
    }
    const u32 off = WS_UX + (cb * 32 + (tid >> 4)) * 8192 + jp * 256 + n * 16;
    ST_SC1(v[0], rs, off); ST_SC1(v[1], rs, off + 16 * 8192);
    const u32 ck = tag - row_sum2(sum4(0u, v[0]), sum4(0u, v[1]));
    __builtin_amdgcn_raw_buffer_store_b64((u32x2){tag, ck}, rs, n == 15 || n == 0 ? WS_MBU + (cb * 32 + (tid >> 4) + (n ? 0 : 16)) * 256 + jp * 8 : WS_END, 0, 16);
  }
  PRIO(ABASE);
  };
  WAITVM(2);
  PIN(pA); PIN(pI); PIN(pR); PIN(pC); PIN(ldt);
  STAMP(1);
  const float dt = rbf(exp_acc(ch ? bfhi(ldt) : bflo(ldt)));
  const u32 mx = wave_umax(umax3(umax3(pR & 0x7fffu, (pR >> 16) & 0x7fffu, pC & 0x7fffu), (pC >> 16) & 0x7fffu, 0u));
  int ex = mx >> 7; ex = ex < 13 ? 13 : ex > 254 ? 254 : ex;
  const float sc = __uint_as_float((u32)(266 - ex) << 23), isc = __uint_as_float((u32)(ex - 12) << 23);
  LAS char* stg = lds + w * STG;
  u32x4 ud[3];
  const __amdgpu_buffer_rsrc_t ru = RSRC(ws + WS_UX + a * 8192, 8192);
  const int npc = w < 3 ? 9 : 8;
  u64 mail = 0;
  const u32 mbu = WS_MBU + a * 256 + (8 * w + lane) * 8;
  auto load_u = [&]() {
    PRIO(APRIO);
    ud[0] = LD_SC1(ru, w * 2048 + lane * 16); ud[1] = LD_SC1(ru, w * 2048 + 1024 + lane * 16);
    ud[2] = LD_SC1(ru, lane < 16 ? w * 2048 + 2048 + lane * 16 : 8192);
    PRIO(ABASE);
  };
  bool have_u = false;
  {
    u32x2 c0, y1, y2, y4, q1, q2, q4, q8, z8, z16, z32, z64, z128;
#pragma unroll
    for (int m = 0; m < 2; ++m) {
      const float la = m ? bfhi(pA) : bflo(pA), ai = m ? bfhi(pI) : bflo(pI), cr = m ? bfhi(pR) : bflo(pR), ci = m ? bfhi(pC) : bflo(pC);
      const float a2 = -rbf(exp_acc(la)) * dt * 1.4426950408889634f, f = ai * dt * 0.15915494309189535f;
      f32x2 a = cexp_p(a2, -f, 1.f), b = cexp_p(a2, -f, 256.f), c = cexp_p(a2, f, 8.f);
      c0[m] = pkh((f32x2){cr * sc, -ci * sc});
      y1[m] = pkh(a); a = csq(a); y2[m] = pkh(a); a = csq(a); y4[m] = pkh(a);
      q1[m] = pkh(b); b = csq(b); q2[m] = pkh(b); b = csq(b); q4[m] = pkh(b); b = csq(b); q8[m] = pkh(b);
      z8[m] = pkh(c); c = csq(c); z16[m] = pkh(c); c = csq(c); z32[m] = pkh(c); c = csq(c); z64[m] = pkh(c); c = csq(c); z128[m] = pkh(c);
    }
#define OPQ(v) asm("" : "+v"(v))
    OPQ(c0); OPQ(y1); OPQ(y2); OPQ(y4); OPQ(q1); OPQ(q2); OPQ(q4); OPQ(q8); OPQ(z8); OPQ(z16); OPQ(z32); OPQ(z64); OPQ(z128);
    LAS char* sw = stg + lane * 8;
    auto put = [&](int r, u32x2 v) { *(LAS u32x2*)(sw + r * SROW) = v; };
    auto mul = [&](u32x2 x, u32x2 y) { const u32 x0 = x.x, x1 = x.y, y0 = y.x, y1_ = y.y; u32x2 r = {hmul(x0, y0), hmul(x1, y1_)}; OPQ(r); return r; };
    put(0, c0); put(21, (u32x2){0x3C00u, 0x3C00u}); put(22, z8); put(23, z16); put(25, z32); put(26, z64); put(28, z128);
    const u32x2 c2 = mul(c0, y2), c4 = mul(c0, y4), c512 = mul(c0, q2), c1024 = mul(c0, q4), z96 = mul(z32, z64);
    put(2, c2); put(4, c4); put(12, c512); put(16, c1024); put(27, z96);
    const u32x2 c6 = mul(c4, y2), c516 = mul(c4, q2), c1536 = mul(c1024, q2);
    put(6, c6); put(13, c516); put(18, c1536);
    put(1, mul(c0, y1)); put(3, mul(c2, y1)); put(5, mul(c4, y1)); put(7, mul(c6, y1));
    put(8, mul(c0, q1)); put(9, mul(c2, q1)); put(10, mul(c4, q1)); put(11, mul(c6, q1));
    put(14, mul(c512, q1)); put(15, mul(c516, q1)); put(17, mul(c1024, q1)); put(19, mul(c1536, q1)); put(20, mul(c0, q8));
    put(24, mul(z8, z16)); put(29, mul(z32, z128)); put(30, mul(z64, z128)); put(31, mul(z96, z128));
  }
  STAMP(2);
  WAITVM(0);
  if (UEARLY == 3) { PRIO(APRIO); load_tile(); }
  publish_weights();
  STAMP(13);
  {
    const int c16 = lane & 15, quad = lane >> 4;
    LAS char* py0 = stg + c16 * SROW + quad * 16;
    LAS char* py1 = stg + (16 + (c16 < 4 ? c16 : 4)) * SROW + quad * 16;
    LAS char* pt1 = stg + (21 + (c16 & 3)) * SROW + quad * 16;
    LAS char* pt2 = stg + (c16 < 4 ? 21 : 24 + (c16 >> 2)) * SROW + quad * 16;
    LAS char* pt3 = stg + (28 + (c16 >> 2)) * SROW + quad * 16;
    f32x4 ka[4] = {};
    u32x4 ft[2][3], fy[2][2], xx[2][2];
    auto rdt = [&](int s) { ft[s & 1][0] = *(LAS u32x4*)(pt1 + s * 64); ft[s & 1][1] = *(LAS u32x4*)(pt2 + s * 64); ft[s & 1][2] = *(LAS u32x4*)(pt3 + s * 64); };
    auto rdy = [&](int s) { fy[s & 1][0] = *(LAS u32x4*)(py0 + s * 64); fy[s & 1][1] = *(LAS u32x4*)(py1 + s * 64); };
    auto prod = [&](int s, int i) {
      u32 t1 = ft[s & 1][0][i], t2 = ft[s & 1][1][i], t3 = ft[s & 1][2][i];
      asm("" : "+v"(t2), "+v"(t3));
      const u32 p0 = hmul1(t1, t2), p1 = hmul1(t1, t3);
      xx[s & 1][0][i] = hmul2(t1, t2, p0); xx[s & 1][1][i] = hmul2(t1, t3, p1);
    };
    rdt(0); rdt(1); rdy(0);
    prod(0, 0); prod(0, 1); prod(0, 2); prod(0, 3);
#pragma unroll
    for (int s = 0; s < 8; ++s) {
      if (s < 6) rdt(s + 2);
      if (s < 7) rdy(s + 1);
      SBAR();
#pragma unroll
      for (int i = 0; i < 4; ++i) {
        ka[i] = mfma16h(fy[s & 1][i >> 1], xx[s & 1][i & 1], ka[i]);
        if (s < 7) prod(s + 1, i);
        SBAR();
      }
    }
    if (UEARLY == 0) { __builtin_amdgcn_s_sleep(TDELAY); PRIO(APRIO); load_tile(); PRIO(ABASE); }
    STAMP(3);
    const float fac = 2.f * dt * isc;
    LAS float* tb = (LAS float*)stg + 4 * quad * 32 + c16;
#pragma unroll
    for (int g = 0; g < 4; ++g) { tb[g * 32] = ka[0][g] * fac; tb[g * 32 + 16] = ka[1][g] * fac; }
    if (quad < 2) {
#pragma unroll
      for (int g = 0; g < 4; ++g) { tb[(16 + g) * 32] = ka[2][g] * fac; tb[(16 + g) * 32 + 16] = ka[3][g] * fac; }
    }
  }
  STAMP(4);
  if (UEARLY == 3) { WAITVM(0); publish_u(); } else BAR();
  {
    float kk[4][8];
#pragma unroll
    for (int c = 0; c < 4; ++c)
#pragma unroll
      for (int j = 0; j < 8; ++j) kk[c][j] = *(LAS float*)(lds + c * STG + tix[j]);
    SBAR();
#pragma unroll
    for (int c = 0; c < 2; ++c) {
      u32x4 v;
#pragma unroll
      for (int j = 0; j < 4; ++j) v[j] = pkbf(kk[2 * c][2 * j] + kk[2 * c + 1][2 * j], kk[2 * c][2 * j + 1] + kk[2 * c + 1][2 * j + 1]);
      *(LAS u32x4*)(lds + RV + c * RVS + tid * 16) = v;
    }
  }
  STAMP(11);
  if (UEARLY) { PRIO(APRIO); mail = poll64(ws, mbu, lane < npc); PRIO(ABASE); BAR(); } else { WAITVM(0); publish_u(); }
  STAMP(12);
  u32 sk[8];
#pragma unroll
  for (int i = 0; i < 2; ++i) {
    const u32x4 us = *(LAS u32x4*)(lds + UIM + (32 * i + (tid >> 3)) * 128 + (((tid & 7) ^ ((tid >> 5) & 7)) << 4));
#pragma unroll
    for (int k = 0; k < 4; ++k) { sk[4 * i + k] = pkbf(bflo(us[k]) * bflo(dk[k]), bfhi(us[k]) * bfhi(dk[k])); PIN(sk[4 * i + k]); }
  }
  if (!UEARLY) { PRIO(APRIO); mail = poll64(ws, mbu, lane < npc); PRIO(ABASE); }
  STAMP(14);
  if (tid < 64) *(LAS u32x4*)(lds + RV + half * RVS + 4096 + r32 * 16) = (u32x4){0u, 0u, 0u, 0u};
  SBAR();
#pragma nounroll
  for (int it = 0; it < SPIN_CAP; ++it) {
    if (!have_u) {
#pragma nounroll
      for (int i2 = 0; i2 < SPIN_CAP && __popcll(__ballot((u32)mail == tag)) != npc; ++i2) { __builtin_amdgcn_s_sleep(ASLEEP); mail = poll64(ws, mbu, lane < npc); COUNT(14); }
      load_u();
    }
    if (wave_sum(sum4(sum4(sum4((u32)(mail >> 32), ud[0]), ud[1]), ud[2])) == npc * tag) break;
    have_u = false;
    asm volatile("" ::: "memory");
    COUNT(11);
  }
  STAMP(15);
#pragma unroll
  for (int i = 0; i < 2; ++i) {
    const u32 nx = __builtin_amdgcn_update_dpp(__builtin_amdgcn_readlane(ud[i + 1].x, 0), ud[i].x, 0x130, 0xf, 0xf, false);
    const u32 dw[5] = {ud[i].x, ud[i].y, ud[i].z, ud[i].w, nx};
    LAS char* p = lds + UC + (128 * w + 64 * i + lane + 4) * 8;
#pragma unroll
    for (int c = 0; c < 2; ++c) {
      u32 pr[4];
#pragma unroll
      for (int k = 0; k < 4; ++k) pr[k] = __builtin_amdgcn_perm(dw[k + 1], dw[k], c ? 0x07060302u : 0x05040100u);
      *(LAS u32x2*)(p + (4 * c) * UCS) = (u32x2){pr[0], pr[2]};
      *(LAS u32x2*)(p + (4 * c + 1) * UCS) = (u32x2){pr[1], pr[3]};
      *(LAS u32*)(p + (4 * c + 2) * UCS - 4) = pr[0]; *(LAS u32*)(p + (4 * c + 2) * UCS) = pr[2];
      *(LAS u32*)(p + (4 * c + 3) * UCS - 4) = pr[1]; *(LAS u32*)(p + (4 * c + 3) * UCS) = pr[3];
    }
  }
  if (w == 0) {
    const u32 u0 = __builtin_amdgcn_readlane(ud[0].x, 0);
    const int cs = lane >> 3, ix = lane & 7, last = (cs & 2) ? 6 : 7;
    const u32 v = ((cs & 1) && ix == last) ? (cs & 4 ? u0 & 0xffff0000u : u0 << 16) : 0u;
    if (ix <= last) *(LAS u32*)(lds + UC + cs * UCS + ix * 4) = v;
  }
  STAMP(16);
  BAR();
  STAMP(5);
  PRIO(ACONV);
  {
    const int c16 = lane & 15, hq = lane >> 4;
    LAS char* pa = lds + RV + ch * RVS + 2 * (2032 - 256 * 7 + 128 * hf - 16 * c16 + 8 * hq);
    LAS char* pb = lds + UC + (4 * ch + ((c16 + 1) & 3)) * UCS + 2 * (128 * hf + 8 * hq + 4 * ((c16 + 1) >> 2));
    f32x4 acc[8] = {};
    u32x4 fa[2][8], fb[2][8];
    auto ldf = [&](int rho, int m) {
      fa[rho & 1][m] = *(LAS u32x4*)(pa + 64 * rho + 512 * (7 - m));
      const u32x2 lo = *(volatile LAS u32x2*)(pb + 64 * rho + 512 * m), hi = *(volatile LAS u32x2*)(pb + 64 * rho + 512 * m + 8);
      fb[rho & 1][m] = (u32x4){lo.x, lo.y, hi.x, hi.y};
    };
#pragma unroll
    for (int m = 0; m < 8; ++m) ldf(0, m);
    u32 ydst = WS_YX + (cb * 32 + 8 * w + hq) * 8192 + jp * 256 + c16 * 16;
    u32 ymb = c16 == 15 || c16 == 0 ? WS_MBY + (cb * 32 + 8 * w + hq + (c16 ? 0 : 4)) * 256 + jp * 8 : WS_END;
    LAS char* exw = lds + EX + w * 8192 + lane * 16;
    LAS char* exr = lds + EX + w * 2048 + lane * 16;
    PIN(ydst); PIN(ymb); PIN(exw); PIN(exr);
#pragma unroll
    for (int rho = 0; rho < 3; ++rho) {
      SBAR();
#pragma unroll
      for (int g = 0; g < 8; ++g) {
#pragma unroll
        for (int aa = 0; aa <= g; ++aa) {
          if (aa + g < 8) acc[aa + g] = mfma16(fa[rho & 1][g], fb[rho & 1][aa], acc[aa + g]);
          if (aa < g && aa + g < 8) acc[aa + g] = mfma16(fa[rho & 1][aa], fb[rho & 1][g], acc[aa + g]);
        }
        ldf(rho + 1, g); SBAR();
      }
    }
#pragma unroll
    for (int t = 0; t < 8; ++t)
#pragma unroll
      for (int aa = 0; aa <= t; ++aa) {
        acc[t] = mfma16(fa[1][t - aa], fb[1][aa], acc[t]);
        SBAR();
#pragma unroll
        for (int f = 0; f < 7; ++f) if (t * (t + 1) / 2 + aa == f * (f + 3) / 2 + 8) { *(LAS f32x4*)(exw + f * 1024) = acc[f]; SBAR(); }
      }
    *(LAS f32x4*)(exw + 7168) = acc[7];
    STAMP(6);
    PRIO(ATAIL);
    BAR();
    STAMP(18);
    f32x4 e[8];
#pragma unroll
    for (int k = 0; k < 8; ++k) e[k] = *(LAS f32x4*)(exr + (k >> 2) * 1024 + (k & 3) * 8192);
    u32x4 v[2];
#pragma unroll
    for (int i = 0; i < 2; ++i) {
      const f32x4 y0 = e[4 * i] + e[4 * i + 1], y1 = e[4 * i + 2] + e[4 * i + 3];
      v[i] = (u32x4){pkbf(y0.x, y1.x), pkbf(y0.y, y1.y), pkbf(y0.z, y1.z), pkbf(y0.w, y1.w)};
      ST_SC1(v[i], rs, ydst + i * 32768);
    }
    const u32 ck = tag - row_sum2(sum4(0u, v[0]), sum4(0u, v[1]));
    __builtin_amdgcn_raw_buffer_store_b64((u32x2){tag, ck}, rs, ymb, 0, 16);
  }
  STAMP(7);
  {
    u32x4 y[2];
    const u32 mby = WS_MBY + a * 256 + (8 * w + lane) * 8;
    mail = poll64(ws, mby, lane < 8);
    float sf[16];
#pragma unroll
    for (int k = 0; k < 8; ++k) { sf[2 * k] = bflo(sk[k]); sf[2 * k + 1] = bfhi(sk[k]); PIN(sf[2 * k]); PIN(sf[2 * k + 1]); }
    u32 ysrc = WS_YX + a * 8192 + w * 2048 + lane * 16, odst = (64 * jp + (tid >> 3)) * 1792 + cb * 128 + (tid & 7) * 16;
    LAS char* tw = lds + (lane & 15) * 144 + (8 * w + (lane >> 4)) * 4;
    LAS char* tr = lds + (tid >> 3) * 144 + (tid & 7) * 16;
    PIN(ysrc); PIN(odst); PIN(tw); PIN(tr);
#pragma nounroll
    for (int it = 0; it < SPIN_CAP && __ballot((u32)mail == tag) != 0xffull; ++it) { mail = poll64(ws, mby, lane < 8); COUNT(13); }
    STAMP(19);
#pragma nounroll
    for (int it = 0; it < SPIN_CAP; ++it) {
      asm volatile("" ::: "memory");
      y[0] = LD_SC1(rs, ysrc); y[1] = LD_SC1(rs, ysrc + 1024);
#pragma unroll
      for (int i = 0; i < 2; ++i)
#pragma unroll
        for (int g = 0; g < 4; ++g) *(LAS u32*)(tw + g * 2304 + i * 16) = y[i][g];
      if (wave_sum(sum4(sum4((u32)(mail >> 32), y[0]), y[1])) == 8u * tag) break;
      COUNT(12);
    }
    STAMP(8);
    BAR();
#pragma unroll
    for (int i = 0; i < 2; ++i) {
      const u32x4 v = *(LAS u32x4*)(tr + i * 4608);
      u32x4 o;
#pragma unroll
      for (int k = 0; k < 4; ++k) o[k] = pkbf(bflo(v[k]) + sf[8 * i + 2 * k], bfhi(v[k]) + sf[8 * i + 2 * k + 1]);
      __builtin_amdgcn_raw_buffer_store_b128(o, RSRC(out, 2048 * 1792), odst + i * (32 * 1792), 0, OUTAUX);
    }
  }
  STAMP(9);
  STAMP_FLUSH();
}

__global__ void NOPK __launch_bounds__(256, 2)
fused_kernel(const void*, const void*, const void*, const void*, const void*, const void*, const void*, const void*, const void*,
             const void*, void*, void*) {
  __shared__ __attribute__((aligned(16))) char smem[81920];
  LAS char* lds = (LAS char*)smem;
  if ((blockIdx.x >> 5) == 7) {
    u64 pc;
    asm volatile("s_getpc_b64 %0" : "=s"(pc));
    if (threadIdx.x < CODE_LINES / 4)
      __builtin_amdgcn_global_load_lds((const GAS void*)((pc & ~127ull) + (threadIdx.x * 4 + ((blockIdx.x >> 3) & 3)) * 128), (LAS void*)lds, 4, 0, 0);
  }
  const __attribute__((address_space(4))) u64* ka = (const __attribute__((address_space(4))) u64*)__builtin_amdgcn_kernarg_segment_ptr();
  asm volatile("" : "+s"(ka));
  const char* u = (const char*)ka[0]; const GAS u32* logA = (const GAS u32*)ka[1]; const GAS u32* Aim = (const GAS u32*)ka[2];
  const GAS u32* Cre = (const GAS u32*)ka[3]; const GAS u32* Cim = (const GAS u32*)ka[4]; const GAS u32* logdt = (const GAS u32*)ka[5];
  const char* Dsk = (const char*)ka[6]; const char* x = (const char*)ka[7]; const char* wqkv = (const char*)ka[8];
  const char* wo = (const char*)ka[9]; char* out = (char*)ka[10]; char* ws = (char*)ka[11];
  asm volatile("" :: "s"(u), "s"(logA), "s"(Aim), "s"(Cre), "s"(Cim), "s"(logdt), "s"(Dsk), "s"(x), "s"(wqkv), "s"(wo), "s"(out), "s"(ws));
  const u32 epoch = request_epoch(ws);
  int bid = blockIdx.x;
  if (bid >= 256) role_b<true>(lds, x, wqkv, out, ws, epoch);
  asm volatile("" : "+s"(bid) :: "memory");
  if (bid >= 256) role_b<false>(lds, x, wqkv, out, ws, epoch);
  else if (bid < 224) {
    const int pi = bid * 256 + threadIdx.x;
    const u32 pA = __builtin_nontemporal_load(logA + pi), pI = __builtin_nontemporal_load(Aim + pi);
    const u32 pR = __builtin_nontemporal_load(Cre + pi), pC = __builtin_nontemporal_load(Cim + pi), ldt = logdt[bid];
    SBAR();
    role_a(lds, u, Dsk, wqkv, wo, out, ws, epoch, pA, pI, pR, pC, ldt);
  }
}

extern "C" void kernel_launch(void* const* d_in, const int* in_sizes, int n_in, void* d_out, int out_size, void* d_ws,
                              size_t ws_size, hipStream_t stream) {
  fused_kernel<<<512, 256, 0, stream>>>(d_in[0], d_in[1], d_in[2], d_in[3], d_in[4], d_in[5], d_in[6], d_in[7], d_in[8], d_in[9], d_out, d_ws);
}
```

```cpp
#define READOUT 128
#include <hip/hip_runtime.h>
#include <stdint.h>

typedef unsigned short u16;
typedef unsigned u32;
typedef unsigned long long u64;
typedef short s16x4 __attribute__((ext_vector_type(4)));
typedef __bf16 bf16x8 __attribute__((ext_vector_type(8)));
typedef __bf16 bf16x2 __attribute__((ext_vector_type(2)));
typedef _Float16 f16x8 __attribute__((ext_vector_type(8)));
typedef _Float16 f16x2 __attribute__((ext_vector_type(2)));
typedef float f32x16 __attribute__((ext_vector_type(16)));
typedef float f32x4 __attribute__((ext_vector_type(4)));
typedef float f32x2 __attribute__((ext_vector_type(2)));
typedef u32 u32x4 __attribute__((ext_vector_type(4)));
typedef u32 u32x2 __attribute__((ext_vector_type(2)));
#define LAS __attribute__((address_space(3)))
#define GAS __attribute__((address_space(1)))
#define DI __device__ __forceinline__
#if defined(__HIP_DEVICE_COMPILE__)
#define NOPK __attribute__((target("no-packed-fp32-ops")))
#else
#define NOPK
#endif
#define RLX_AGENT __ATOMIC_RELAXED, __HIP_MEMORY_SCOPE_AGENT

#define BMAP 1
#define PREFETCH 1
#define CODE_BEFORE 0
#define CODE_LINES 208
#define QEARLY 3
#define GATH_AT 18
#define WO_EARLY 0
#define WO_MID 12
#define WO_G 32
#define WO_E 4
#define KVSTEP 1
#define PUB_FIRST 0
#define UPOLL_AT 8
#define TMAP 1
#define BPRIO1 2
#define ABASE 0
#define ACONV 0
#define L2PF 0
#define NTA 1
#define NTX 0
#define W0FIRST 1
#define ORD 4
#define PF2AT 1
#define UMID 0
#define WFIRST 0
#define HEADC 1
#define ASLEEP 0
#define TDELAY 0
#define UEARLY 3
#define EPW 255
#define OUTAUX 16
#define TELL2_LATE 1
#define ROT 1
#define APRIO 3
#define ATAIL 1
#define BPRIO 2

constexpr u32 WS_MB1 = 73728;
constexpr u32 WS_MB2 = WS_MB1 + 32768;
constexpr u32 WS_MBU = WS_MB2 + 32768;
constexpr u32 WS_MBY = WS_MBU + 57344;
constexpr u32 WS_KVP = 1u << 18;
constexpr u32 WS_OB = WS_KVP + (1u << 20);
constexpr u32 WS_UX = WS_OB + (2u << 20);
constexpr u32 WS_YX = WS_UX + (2u << 20);
constexpr u32 WS_WQ = WS_YX + (2u << 20);
constexpr u32 WS_WO = WS_WQ + (1u << 19);
constexpr u32 WS_MBQ = WS_WO + 458752;
constexpr u32 WS_END = WS_WO + (1u << 19);
constexpr u32 WS_DBG = WS_END;
constexpr int SPIN_CAP = 1 << 15;

#define RK (READOUT & 31)
#define STAMP(i) do { if ((i) == 0) st0_ = __builtin_amdgcn_s_memrealtime(); else if ((i) == RK) stk_ = __builtin_amdgcn_s_memrealtime(); } while (0)
#define STAMP_DECL u64 st0_ = 0, stk_ = 0
#define COUNT(i) do {} while (0)
#define STAMP_FLUSH() do { if (lane == 0) { GAS u64* p_ = (GAS u64*)(ws + WS_DBG) + (blockIdx.x * 4 + w) * 2; __hip_atomic_store(p_, st0_, RLX_AGENT); __hip_atomic_store(p_ + 1, stk_, RLX_AGENT); } } while (0)

DI float bflo(u32 v) { return __uint_as_float(v << 16); }
DI float bfhi(u32 v) { return __uint_as_float(v & 0xffff0000u); }
DI u32 pkbf(float a, float b) { f32x2 p = {a, b}; return __builtin_bit_cast(u32, __builtin_convertvector(p, bf16x2)); }
DI u32 pkh(f32x2 p) { return __builtin_bit_cast(u32, __builtin_convertvector(p, f16x2)); }
DI float rbf(float a) { return bflo(pkbf(a, a)); }
DI u32 umax3(u32 a, u32 b, u32 c) { u32 m = a > b ? a : b; return m > c ? m : c; }
DI u32 sum4(u32 m, u32x4 v) { return (m + v.x + v.y) + v.z + v.w; }
DI u32 row_sum(u32 v) {
  v += __builtin_amdgcn_update_dpp(0u, v, 0x111, 0xf, 0xf, true); v += __builtin_amdgcn_update_dpp(0u, v, 0x112, 0xf, 0xf, true);
  v += __builtin_amdgcn_update_dpp(0u, v, 0x114, 0xf, 0xf, true); v += __builtin_amdgcn_update_dpp(0u, v, 0x118, 0xf, 0xf, true);
  return v;
}
DI u32 row_sum2(u32 a, u32 b) {
  a += __builtin_amdgcn_update_dpp(0u, a, 0x111, 0xf, 0xf, true); b += __builtin_amdgcn_update_dpp(0u, b, 0x101, 0xf, 0xf, true);
  a += __builtin_amdgcn_update_dpp(0u, a, 0x112, 0xf, 0xf, true); b += __builtin_amdgcn_update_dpp(0u, b, 0x102, 0xf, 0xf, true);
  a += __builtin_amdgcn_update_dpp(0u, a, 0x114, 0xf, 0xf, true); b += __builtin_amdgcn_update_dpp(0u, b, 0x104, 0xf, 0xf, true);
  a += __builtin_amdgcn_update_dpp(0u, a, 0x118, 0xf, 0xf, true); b += __builtin_amdgcn_update_dpp(0u, b, 0x108, 0xf, 0xf, true);
  return (threadIdx.x & 8) ? a : b;
}
DI u32 wave_sum(u32 v) {
  v = row_sum(v);
  return (__builtin_amdgcn_readlane(v, 15) + __builtin_amdgcn_readlane(v, 31)) + (__builtin_amdgcn_readlane(v, 47) + __builtin_amdgcn_readlane(v, 63));
}
DI u32 wave_umax(u32 v) {
  u32 t;
  t = __builtin_amdgcn_update_dpp(0u, v, 0x111, 0xf, 0xf, false); v = v > t ? v : t;
  t = __builtin_amdgcn_update_dpp(0u, v, 0x112, 0xf, 0xf, false); v = v > t ? v : t;
  t = __builtin_amdgcn_update_dpp(0u, v, 0x114, 0xf, 0xf, false); v = v > t ? v : t;
  t = __builtin_amdgcn_update_dpp(0u, v, 0x118, 0xf, 0xf, false); v = v > t ? v : t;
  const u32 a = __builtin_amdgcn_readlane(v, 15), b = __builtin_amdgcn_readlane(v, 31), c = __builtin_amdgcn_readlane(v, 47), d = __builtin_amdgcn_readlane(v, 63);
  const u32 ab = a > b ? a : b, cd = c > d ? c : d;
  return ab > cd ? ab : cd;
}

DI u32 request_epoch(const char* ws) { u32 e; asm volatile("s_load_dword %0, %1, 0x0" : "=s"(e) : "s"(ws) : "memory"); return e; }
DI u32 take_tag(u32 e) { u32 r; asm volatile("s_waitcnt lgkmcnt(0)\n\ts_and_b32 %0, %1, -2" : "=s"(r) : "s"(e) : "memory", "scc"); return r * 1664525u + 2027808447u; }
#define PSCOPE __HIP_MEMORY_SCOPE_AGENT
DI u32 poll32(char* ws, u32 off, bool on) { u32 z = 0, r = 0; asm volatile("" : "+v"(z)); if (on) r = __hip_atomic_fetch_add((GAS u32*)(ws + off), z, __ATOMIC_RELAXED, PSCOPE); return r; }
DI u64 poll64(char* ws, u32 off, bool on) { u64 z = 0, r = 0; asm volatile("" : "+v"(z)); if (on) r = __hip_atomic_fetch_add((GAS u64*)(ws + off), z, __ATOMIC_RELAXED, PSCOPE); return r; }
#define SBAR() __builtin_amdgcn_sched_barrier(0)
#define PRIO(n) do { SBAR(); __builtin_amdgcn_s_setprio(n); SBAR(); } while (0)
#define PIN(x) asm volatile("" : "+v"(x))
DI f32x16 mfma32(u32x4 a, u32x4 b, f32x16 c) { return __builtin_amdgcn_mfma_f32_32x32x16_bf16(__builtin_bit_cast(bf16x8, a), __builtin_bit_cast(bf16x8, b), c, 0, 0, 0); }
DI f32x16 mfma32h(u32x4 a, u32x4 b, f32x16 c) { return __builtin_amdgcn_mfma_f32_32x32x16_f16(__builtin_bit_cast(f16x8, a), __builtin_bit_cast(f16x8, b), c, 0, 0, 0); }
DI f32x4 mfma16h(u32x4 a, u32x4 b, f32x4 c) { return __builtin_amdgcn_mfma_f32_16x16x32_f16(__builtin_bit_cast(f16x8, a), __builtin_bit_cast(f16x8, b), c, 0, 0, 0); }
DI f32x4 mfma16(u32x4 a, u32x4 b, f32x4 c) { return __builtin_amdgcn_mfma_f32_16x16x32_bf16(__builtin_bit_cast(bf16x8, a), __builtin_bit_cast(bf16x8, b), c, 0, 0, 0); }
DI u32x2 tr_read(LAS char* p) { return __builtin_bit_cast(u32x2, __builtin_amdgcn_ds_read_tr16_b64_v4i16((LAS s16x4*)p)); }
#define RSRC(p, n) __builtin_amdgcn_make_buffer_rsrc((void*)(p), 0, (n), 0x00020000)
#define DMA16(rs, voff, soff, dst, aux) __builtin_amdgcn_raw_ptr_buffer_load_lds((rs), (LAS void*)(dst), 16, (voff), (soff), 0, (aux))
#define WAITVM(n) do { asm volatile("" ::: "memory"); __builtin_amdgcn_s_waitcnt(0x0F70 | ((n) & 15) | (((n) >> 4) << 14)); asm volatile("" ::: "memory"); } while (0)
#define VMCNT(n) asm volatile("s_waitcnt vmcnt(" #n ")" ::: "memory")
#define LGKM0() asm volatile("s_waitcnt lgkmcnt(0)" ::: "memory")
#define BAR() do { asm volatile("s_waitcnt lgkmcnt(0)" ::: "memory"); __builtin_amdgcn_s_barrier(); asm volatile("" ::: "memory"); } while (0)
#define ST_SC1(v, rs, off) __builtin_amdgcn_raw_buffer_store_b128((v), (rs), (off), 0, 16)
#define LD_SC1(rs, off) __builtin_amdgcn_raw_buffer_load_b128((rs), (off), 0, 16)
#define LDS_SC1(rs, off, soff) __builtin_amdgcn_raw_buffer_load_b128((rs), (off), (soff), 16)
#define ST1_SC1(v, rs, off) __builtin_amdgcn_raw_buffer_store_b32((v), (rs), (off), 0, 16)
#define LD1_SC1(rs, off) __builtin_amdgcn_raw_buffer_load_b32((rs), (off), 0, 16)

typedef unsigned short u16x2 __attribute__((ext_vector_type(2)));
DI u32 phi1(float a, float b) {
  const u32 t = pkbf(a, b);
  const f32x2 x = {bflo(t), bfhi(t)};
  const f32x2 y = x * 1.4426950408889634f;
  const float e0 = __builtin_amdgcn_fmed3f(__builtin_amdgcn_exp2f(y.x), 0.f, 1.f);
  const float e1 = __builtin_amdgcn_fmed3f(__builtin_amdgcn_exp2f(y.y), 0.f, 1.f);
  const f32x2 x1 = x + 1.0f;
  return pkbf(__builtin_fmaxf(x1.x, e0), __builtin_fmaxf(x1.y, e1));
}
DI u32 pkmin(u32 a, u32 b) { return __builtin_bit_cast(u32, __builtin_elementwise_min(__builtin_bit_cast(u16x2, a), __builtin_bit_cast(u16x2, b))); }
DI void phi_eps(u32x4 (&p)[2], u32 mn) {
  if (__any((mn & 0xffffu) < 0x3A80u || mn < 0x3A800000u)) {
#pragma unroll
    for (int j = 0; j < 8; ++j) { const u32 r = p[j >> 2][j & 3]; p[j >> 2][j & 3] = pkbf(bflo(r) + 1e-6f, bfhi(r) + 1e-6f); }
  }
}

template <bool HEAD>
DI void role_b(LAS char* lds, const char* __restrict__ x, const char* __restrict__ wqkv, char* __restrict__ out, char* ws, u32 epoch) {
  const int b = blockIdx.x - 256, tid = threadIdx.x, lane = tid & 63;
  const int w = __builtin_amdgcn_readfirstlane(tid >> 6);
  STAMP_DECL;
  STAMP(0);
  const int rb = 4 * ((b & 7) >> 1) + (b >> 6), h = (b & 1) * 8 + ((b >> 3) & 7);
  const int start = (int)((0x1065432106543210ull >> (4 * ((h & 7) + 2 * (rb & 3)))) & 7);
  const __amdgpu_buffer_rsrc_t rs = RSRC(ws, WS_END), rx = RSRC(x, 2048 * 896), rw = RSRC(wqkv, 448 * 2688);
  constexpr int WR = 49152, KVI = WR + 4096, OST = 16384, OTR = WR;
#define XS(i) ((i) * 16384)
#define QS(cc) ((cc) < 2 ? WR + 24576 + (cc) * 4096 : (cc) < 6 ? XS(1) + ((cc) - 2) * 1024 : WR)
#define QD(cc) ((cc) >= 2 && (cc) < 6 ? 4096 : 1024)
  const int wsb = (56 * h) & ~15;
  const int coff = (h & 1) * 4;
  const int wone = (h & 1) ? 0 : 28;
  const u32 xv0 = (128 * rb + 32 * w + (lane >> 3)) * 896 + (((lane & 7) ^ (lane >> 4)) << 4);
  const u32 xv1 = (xv0 ^ 64) + 8 * 896;
  const u32 wv = (16 * w + (lane >> 2)) * 2688 + wsb + (lane & 3) * 16;
  const u32 l16 = lane * 16;
  int ch_[7];
  auto chunk = [&](int cc) { return ch_[cc]; };
  const int gi = h + rb;
  const u32 ordc = ORD == 1 ? 0x6543210u : ORD == 2 ? ((gi & 1) ? 0x5461032u : 0x6543210u) : ORD == 3 ? ((gi & 1) ? 0x6452301u : 0x6543210u)
                 : ((gi & 2) ? ((gi & 1) ? 0x4652103u : 0x5461032u) : ((gi & 1) ? 0x4650321u : 0x6543210u));
  auto next_chunk = [&](int cc) { int c = (ordc >> (4 * cc)) & 7; asm volatile("" : "+s"(c)); ch_[cc] = c; };
  auto issue_x = [&](int cc, int i) { DMA16(rx, (i & 1) ? xv1 : xv0, chunk(cc) * 128 + (i >> 1) * (16 * 896), lds + XS(cc % 3) + w * 4096 + i * 1024, NTX); };
  auto issue_w = [&](int cc, int i) { DMA16(rw, wv, chunk(cc) * (64 * 2688) + 896 * (i + 1), lds + WR + (cc & 3) * 8192 + i * 4096 + w * 1024, 0); };
  bool qok = true;
  u32 qso[7];
  auto issue_q = [&](int cc) { DMA16(rs, l16, qok ? qso[cc] : WS_END, lds + QS(cc) + w * QD(cc), 16); };
#pragma unroll
  for (int cc = 0; cc < 2; ++cc) {
    next_chunk(cc);
    if (HEAD == (cc < HEADC)) { issue_w(cc, 0); issue_w(cc, 1); issue_x(cc, 0); issue_x(cc, 1); issue_x(cc, 2); issue_x(cc, 3); }
    SBAR();
  }
  if (HEAD) return;
  next_chunk(2); next_chunk(3); next_chunk(4); next_chunk(5); next_chunk(6);
  __builtin_amdgcn_s_setprio(BPRIO1);
  const int r32 = lane & 31, half = lane >> 5;
  const int xrd = w * 4096 + r32 * 128 + ((half ^ ((r32 >> 1) & 7)) << 4);
  LAS char* whi = lds + WR + (8 * half + ((lane >> 2) & 3)) * 64 + ((lane >> 4) & 1) * 32 + (lane & 3) * 8;
  PIN(whi);
  const int rt = h >> 2, ct = (h + ((h & 8) ? 2 : 1)) & 3;
  u32 wo_src = WS_WO + (4 * ct + w < 14 ? 4 * ct + w : 13) * 32768, kvp_src = WS_KVP + h * 65536 + w * 16384;
  u32 ob_dst = WS_OB + ((rb * 4 + w) * 16 + h) * 2048 + l16, ob_src = WS_OB + ((rb * 4 + rt) * 16 + 4 * w) * 2048;
  asm volatile("" : "+s"(wo_src), "+s"(kvp_src), "+s"(ob_src));
  PIN(ob_dst);
  u32 tell1 = lane < 4 ? WS_MB1 + (h * 16 + 4 * w + lane) * 128 + rb * 4 : WS_END;
  u32 tell2 = lane < 4 ? WS_MB2 + (rb * 16 + 4 * w + lane) * 128 + h * 4 : WS_END;
  u32 mb1 = WS_MB1 + (h * 16 + rb) * 128 + ((lane + rb) & 15) * 4, mb2 = WS_MB2 + (rb * 16 + h) * 128 + ((4 * w + lane) & 15) * 4;
  u32 kvp_dst = kvp_src + rb * 1024 + l16, kvs[16];
#pragma unroll
  for (int p = 1; p < 16; ++p) { kvs[p] = kvp_src + ((p + rb) & 15) * 1024; asm volatile("" : "+s"(kvs[p])); }
  const int dw = 8 * w + 4 * half - coff;
  u32 rowmask = dw < 0 || dw >= 28 ? 0u : ~0u;
  const bool padlo = (h & 1) && !half, padhi = !(h & 1) && half;
  const int oi = (r32 & 3) + 4 * (r32 >> 3);
  LAS char* kvrd = lds + KVI + ((oi < 14 ? 14 * ((r32 >> 2) & 1) + oi + coff : wone) + 32 * half) * 32;
  LAS char* qlo = lds + l16; LAS char* qhi = lds + WR + l16;
  PIN(tell1); PIN(tell2); PIN(mb1); PIN(mb2); PIN(kvp_dst); PIN(rowmask); PIN(kvrd); PIN(qhi);
  const bool ck = lane == wone + 1;
  u32 mbq = WS_MBQ + (h * 16 + rb) * 128 + (4 * (lane & 7) + w) * 4, mail = 0, tag = 0;
  PIN(mbq);
  u32x4 xf[28], wf[2][8];
  f32x16 ak = {}, av = {};
  auto rd = [&](int cc, int i) {
    if ((i & 1) == 0) xf[cc * 4 + (i >> 1)] = *(LAS u32x4*)(lds + XS(cc % 3) + (xrd ^ ((i >> 1) << 5)));
    LAS char* p = whi + (cc & 3) * 8192 + (i >> 1) * 1024 + (i & 1) * 4096;
    const u32x2 lo = tr_read(p), hi = tr_read(p + 256);
    wf[cc & 1][i] = (u32x4){lo.x, lo.y, hi.x, hi.y};
  };
#pragma unroll
  for (int cc = 0; cc < 6; ++cc) {
    if (cc == 0) VMCNT(6); else if (cc < 5) VMCNT(8); else VMCNT(7);
    BAR();
    if (cc == 5) __builtin_amdgcn_s_setprio(BPRIO);
    if (cc == 0) { STAMP(1); tag = take_tag(epoch); }
    if (cc == 2) { STAMP(23); } if (cc == 4) { STAMP(24); } if (cc == 5) { STAMP(25); }
    if (cc == 5) {
#pragma unroll
      for (int i = 0; i < 8; ++i) { rd(cc, i); if (i < 7) { qso[i] = WS_WQ + (h * 28 + 4 * chunk(i) + w) * 1024; asm volatile("" : "+s"(qso[i])); } }
      SBAR();
    }
#pragma unroll
    for (int i = 0; i < 8; ++i) {
      if (cc > 0) {
        if (i & 1) av = mfma32(xf[(cc - 1) * 4 + (i >> 1)], wf[(cc - 1) & 1][i], av);
        else ak = mfma32(xf[(cc - 1) * 4 + (i >> 1)], wf[(cc - 1) & 1][i], ak);
      }
      if (cc < 5) rd(cc, i);
      if (cc == 0) { if (i < 2) issue_w(2, i); else if (i < 6) issue_x(2, i - 2); else issue_w(3, i - 6); }
      else if (cc <= 4 && i < 4) issue_x(cc + 2, i);
      else if (cc <= 3 && i < 6) issue_w(cc + 3, i - 4);
      else if (cc == 4 && i == 4) mail = poll32(ws, mbq, lane < 7);
      SBAR();
    }
#pragma unroll
    for (int s = 3; s >= 0; --s) { PIN(wf[cc & 1][2 * s + 1]); PIN(wf[cc & 1][2 * s]); PIN(xf[cc * 4 + s]); }
    if (cc > 0) { PIN(ak); PIN(av); }
  }
  VMCNT(1);
  BAR();
  STAMP(26);
  rd(6, 0); rd(6, 2); rd(6, 4); rd(6, 6);
  SBAR();
#pragma unroll
  for (int s = 0; s < 4; ++s) ak = mfma32(xf[20 + s], wf[1][2 * s], ak);
  PIN(ak);
#pragma unroll
  for (int s = 3; s >= 0; --s) { PIN(wf[0][2 * s]); PIN(xf[24 + s]); }
  rd(6, 1); rd(6, 3); rd(6, 5); rd(6, 7);
  SBAR();
#pragma unroll
  for (int s = 0; s < 4; ++s) ak = mfma32(xf[24 + s], wf[0][2 * s], ak);
  PIN(ak);
  SBAR();
  WAITVM(0);
  PIN(mail);
  qok = __ballot(mail == tag) == 0x7full;
  STAMP(2);
  u32x4 wof[32], d[16], pq[2];
  auto load_wo = [&](int k) { wof[k] = __builtin_amdgcn_raw_buffer_load_b128(rs, l16 + (k & 3) * 1024, wo_src + (k >> 2) * 4096, 16); };
  {
    u32x4 pk[2], pv[2];
    u32 mn = ~0u;
#pragma unroll
    for (int j = 0; j < 8; j += 2) {
      av = mfma32(xf[20 + j], wf[j < 4][2 * (j & 3) + 1], av);
      issue_q(j); if (j < 6) issue_q(j + 1);
      SBAR();
      pk[j >> 2][j & 3] = phi1(ak[2 * j], ak[2 * j + 1]);
      av = mfma32(xf[21 + j], wf[j < 4][2 * (j & 3) + 3], av);
      pk[j >> 2][(j & 3) + 1] = phi1(ak[2 * j + 2], ak[2 * j + 3]);
      mn = pkmin(pkmin(mn, pk[j >> 2][j & 3]), pk[j >> 2][(j & 3) + 1]);
      PIN(mn); PIN(av);
      SBAR();
    }
    phi_eps(pk, mn);
    STAMP(11);
#pragma unroll
    for (int j = 0; j < 8; ++j) { const u32 v = pkbf(av[2 * j], av[2 * j + 1]); pv[j >> 2][j & 3] = r32 == wone ? 0x3F803F80u : v; }
    f32x16 kv = {};
    kv = mfma32(pk[0], pv[0], kv); kv = mfma32(pk[1], pv[1], kv);
    ST1_SC1(tag, rs, tell1);
#pragma unroll
    for (int g = 0; g < 4; ++g) *(LAS f32x4*)(lds + XS(2) + w * 4096 + g * 1024 + l16) = (f32x4){kv[4 * g], kv[4 * g + 1], kv[4 * g + 2], kv[4 * g + 3]};
    STAMP(12);
    BAR();
    f32x4 s4 = *(LAS f32x4*)(lds + XS(2) + tid * 16);
#pragma unroll
    for (int ww = 1; ww < 4; ++ww) s4 += *(LAS f32x4*)(lds + XS(2) + ww * 4096 + tid * 16);
    u32x4 v = __builtin_bit_cast(u32x4, s4);
    const u32 part = v.y + v.z + v.w;
    const u32 c = tag - wave_sum(ck ? part : part + v.x);
    if (ck) v.x = c;
    ST_SC1(v, rs, kvp_dst);
    d[0] = v;
  }
  if (!qok) {
#pragma nounroll
    for (int it = 0; it < SPIN_CAP && __ballot(poll32(ws, mbq, lane < 7) == tag) != 0x7full; ++it) {}
    qok = true;
#pragma unroll
    for (int cc = 0; cc < 7; ++cc) issue_q(cc);
    WAITVM(0);
    COUNT(13);
  }
  u32 go, need = 0xfffeu;
  mail = poll32(ws, mb1, lane < 16);
#pragma unroll
  for (int k = 0; k < WO_E; ++k) load_wo(k);
  WAITVM(WO_E + 3);
  BAR();
  STAMP(3);
#define QP(cc) (QS(cc) >= WR ? qhi + (QS(cc) - WR) : qlo + QS(cc))
  auto load_kv = [&](int p, bool on = true) { d[p] = __builtin_amdgcn_raw_buffer_load_b128(rs, l16, on ? kvs[p] : WS_END, 16); };
  {
    f32x16 aq = {};
    u32x4 qf[2][4];
    u32 qsum = 0;
    auto read_q = [&](int cc) {
#pragma unroll
      for (int s = 0; s < 4; ++s) qf[cc & 1][s] = *(LAS u32x4*)(QP(cc) + s * QD(cc));
      SBAR();
    };
    read_q(0);
#pragma unroll
    for (int cc = 0; cc < 7; ++cc) {
      if (cc < 6) read_q(cc + 1);
#pragma unroll
      for (int s = 0; s < 4; ++s) {
        aq = mfma32(qf[cc & 1][s], xf[cc * 4 + s], aq);
        qsum = sum4(qsum, qf[cc & 1][s]);
        if (s & 1) load_wo(WO_E + 2 * cc + (s >> 1));
        PIN(aq); PIN(qsum);
        SBAR();
      }
    }
    if (wave_sum(qsum) != 28u * tag) {
      aq = (f32x16){};
#pragma nounroll
      for (int k2 = 0; k2 < 28; ++k2) {
        const int ks = 4 * ((ordc >> (k2 & 28)) & 7) + (k2 & 3);
        u32x4 f;
#pragma nounroll
        for (int it = 0; it < SPIN_CAP; ++it) {
          asm volatile("" ::: "memory");
          f = LDS_SC1(rs, l16, WS_WQ + (h * 28 + ks) * 1024);
          if (wave_sum(sum4(0u, f)) == tag) break;
        }
        aq = mfma32(f, __builtin_amdgcn_raw_buffer_load_b128(rx, (128 * rb + 32 * w + r32) * 896 + half * 16, ks * 32, 0), aq);
      }
      COUNT(15);
    }
    STAMP(14);
    go = need & (u32)__ballot(mail == tag); need &= ~go;
    mail = poll32(ws, mb1, lane < 16 && need != 0);
    STAMP(15);
    u32 mn = ~0u;
#pragma unroll
    for (int j = 0; j < 8; ++j) {
      if (j) load_kv(2 * j, go >> (2 * j) & 1);
      load_kv(2 * j + 1, go >> (2 * j + 1) & 1);
      u32 r = phi1(aq[2 * j], aq[2 * j + 1]);
      if (j < 2) r = padlo ? 0x3F803F80u : r; else if (j >= 6) r = padhi ? 0x3F803F80u : r;
      pq[j >> 2][j & 3] = r;
      mn = pkmin(mn, r);
      PIN(mn);
      SBAR();
    }
    phi_eps(pq, mn);
    PIN(pq[0]); PIN(pq[1]);
  }
  STAMP(5);
  PIN(mail);
  STAMP(16);
#pragma nounroll
  for (int i2 = 0; i2 < SPIN_CAP && need != 0; ++i2) {
    go = need & (u32)__ballot(mail == tag); need &= ~go;
#pragma unroll
    for (int p = 1; p < 16; ++p) if (go >> p & 1) load_kv(p);
    if (need != 0) { mail = poll32(ws, mb1, lane < 16); WAITVM(0); }
    COUNT(14);
  }
#pragma unroll
  for (int k = WO_E + 14; k < WO_G; ++k) load_wo(k);
  WAITVM(WO_G - WO_E - 14);
  STAMP(18);
#pragma nounroll
  for (int it = 0; it < SPIN_CAP; ++it) {
    u32 m = 0;
#pragma unroll
    for (int p = 0; p < 16; ++p) m = sum4(m, d[p]);
    if (wave_sum(m) == 16u * tag) break;
    asm volatile("" ::: "memory");
#pragma unroll
    for (int p = 1; p < 16; ++p) load_kv(p);
    WAITVM(0);
    COUNT(11);
  }
  mail = poll32(ws, mb2, lane < 4);
  SBAR();
  {
    f32x4 s4 = __builtin_bit_cast(f32x4, d[0]);
#pragma unroll
    for (int p = 1; p < 16; ++p) s4 += __builtin_bit_cast(f32x4, d[p]);
    *(LAS u32x2*)(lds + KVI + lane * 32 + w * 8) = (u32x2){pkbf(s4.x, s4.y) & rowmask, pkbf(s4.z, s4.w) & rowmask};
  }
  STAMP(6);
  BAR();
  {
    const u32x4 a0 = *(LAS u32x4*)kvrd, a1 = *(LAS u32x4*)(kvrd + 16);
    if (!TELL2_LATE) ST1_SC1(tag, rs, tell2);
    f32x16 nm = {};
    nm = mfma32(a0, pq[0], nm); nm = mfma32(a1, pq[1], nm);
    const float den = rbf(rbf(nm[14]) + 1e-6f);
    const float rc = __builtin_amdgcn_rcpf(den);
    u32 o[8];
#pragma unroll
    for (int j = 0; j < 7; ++j) { const u32 t = pkbf(nm[2 * j], nm[2 * j + 1]); o[j] = pkbf(bflo(t) * rc, bfhi(t) * rc); }
    o[7] = (tag - ((o[0] + o[1] + o[2]) + (o[3] + o[4] + o[5]) + o[6])) & ~0x40004000u;
    __builtin_amdgcn_raw_buffer_store_b128((u32x4){o[0], o[1], o[2], o[3]}, rs, ob_dst, 0, 16);
    __builtin_amdgcn_raw_buffer_store_b128((u32x4){o[4], o[5], o[6], o[7]}, rs, ob_dst + 1024, 0, 16);
    if (TELL2_LATE) ST1_SC1(tag, rs, tell2);
    SBAR();
  }
  STAMP(7);
  auto load_o = [&](int i) {
    DMA16(rs, l16, ob_src + i * 2048, lds + OST + (8 * w + 2 * i) * 1024, 16);
    DMA16(rs, l16, ob_src + i * 2048 + 1024, lds + OST + (8 * w + 2 * i + 1) * 1024, 16);
  };
#pragma unroll
  for (int k = WO_G; k < 32; ++k) load_wo(k);
  WAITVM(35 - WO_G);
  PIN(mail);
  need = 15u;
  bool wo_done = false;
#pragma nounroll
  for (int i2 = 0; i2 < SPIN_CAP; ++i2) {
    go = need & (u32)__ballot(mail == tag); need &= ~go;
    if (WO_G < 32 && !wo_done) WAITVM(0);
#pragma unroll
    for (int i = 0; i < 4; ++i) if (go >> i & 1) load_o(i);
    mail = poll32(ws, mb2, lane < 4 && need != 0);
    if (!wo_done) {
#pragma nounroll
      for (int it = 0; it < SPIN_CAP; ++it) {
        u32 m = 0;
#pragma unroll
        for (int k = 0; k < 32; ++k) m = sum4(m, wof[k]);
        if (__all(m == 16u * tag)) break;
        asm volatile("" ::: "memory");
#pragma unroll
        for (int k = 0; k < 32; ++k) load_wo(k);
        WAITVM(0);
      }
#pragma unroll
      for (int k = 1; k < 32; k += 2) { wof[k].w = 0u; PIN(wof[k]); }
      wo_done = true;
    }
    WAITVM(0);
    COUNT(12);
    if (need == 0) break;
  }
  STAMP(8);
  f32x16 acc;
#pragma nounroll
  for (int it = 0; it < SPIN_CAP; ++it) {
    BAR();
    STAMP(9);
    u32x4 of[32];
    u32 m = 0;
#pragma unroll
    for (int k = 0; k < 6; ++k) of[k] = *(LAS u32x4*)(lds + OST + k * 1024 + l16);
    acc = (f32x16){};
    SBAR();
#pragma unroll
    for (int k = 0; k < 32; ++k) {
      acc = mfma32(wof[k], of[k], acc);
      if (k + 6 < 32) of[k + 6] = *(LAS u32x4*)(lds + OST + (k + 6) * 1024 + l16);
      m = sum4(m, of[k]);
      PIN(m);
      SBAR();
    }
    if (__all(((16u * tag - m) & 0x3FF83FFFu) == 0u)) break;
    BAR();
    load_o(0); load_o(1); load_o(2); load_o(3);
    WAITVM(0);
    COUNT(13);
  }
  STAMP(22);
  {
    LAS char* p = lds + OTR + r32 * 272 + w * 64 + half * 32;
    *(LAS u32x4*)p = (u32x4){pkbf(acc[0], acc[1]), pkbf(acc[2], acc[3]), pkbf(acc[4], acc[5]), pkbf(acc[6], acc[7])};
    *(LAS u32x4*)(p + 16) = (u32x4){pkbf(acc[8], acc[9]), pkbf(acc[10], acc[11]), pkbf(acc[12], acc[13]), pkbf(acc[14], acc[15])};
  }
  BAR();
  if (ct < 3 || (tid & 15) < 8) {
#pragma unroll
    for (int i = 0; i < 2; ++i)
      __builtin_amdgcn_raw_buffer_store_b128(*(LAS u32x4*)(lds + OTR + (16 * i + (tid >> 4)) * 272 + (tid & 15) * 16), RSRC(out, 2048 * 1792),
                                             (128 * rb + 32 * rt + 16 * i + (tid >> 4)) * 1792 + 896 + ct * 256 + (tid & 15) * 16, 0, OUTAUX);
  }
  if (b == EPW && tid == 0) __hip_atomic_store((GAS u32*)ws, tag, RLX_AGENT);
  STAMP(10);
  STAMP_FLUSH();
}

DI u32 hmul1(u32 a, u32 b) { const f16x2 x = __builtin_bit_cast(f16x2, a), y = __builtin_bit_cast(f16x2, b); return __builtin_bit_cast(u32, (f16x2){x.x, x.x} * y); }
DI u32 hmul2(u32 a, u32 b, u32 t) {
  const f16x2 x = __builtin_bit_cast(f16x2, a), y = __builtin_bit_cast(f16x2, b);
  return __builtin_bit_cast(u32, __builtin_elementwise_fma((f16x2){x.y, x.y}, (f16x2){-y.y, y.x}, __builtin_bit_cast(f16x2, t)));
}
DI u32 hmul(u32 a, u32 b) { return hmul2(a, b, hmul1(a, b)); }
DI f32x2 csq(f32x2 a) { return (f32x2){__builtin_fmaf(a.x, a.x, -a.y * a.y), (a.x + a.x) * a.y}; }
DI float exp_acc(float x) {
  const float hi = x * 1.4426950408889634f, lo = __builtin_fmaf(x, 1.9259629911266175e-8f, __builtin_fmaf(x, 1.4426950408889634f, -hi));
  const float e = __builtin_amdgcn_exp2f(hi);
  return __builtin_fmaf(e, lo * 0.6931471805599453f, e);
}
DI f32x2 rot(f32x2 b) { return (f32x2){-b.y, b.x}; }
DI f32x2 cm1(f32x2 a, f32x2 b) { return (f32x2){a.x, a.x} * b; }
DI f32x2 cm2(f32x2 a, f32x2 br, f32x2 t) { return __builtin_elementwise_fma((f32x2){a.y, a.y}, br, t); }
DI f32x2 cexp_p(float a2, float f, float p) {
  const float m = __builtin_amdgcn_exp2f(a2 * p), ph = __builtin_amdgcn_fractf(f * p);
  return (f32x2){m, m} * (f32x2){__builtin_amdgcn_cosf(ph), __builtin_amdgcn_sinf(ph)};
}
DI void role_a(LAS char* lds, const char* __restrict__ u, const char* __restrict__ Dsk, const char* __restrict__ wqkv, const char* __restrict__ wo, char* __restrict__ out, char* ws, u32 epoch,
               u32 pA, u32 pI, u32 pR, u32 pC, u32 ldt) {
  const int a = blockIdx.x, cb = a >> 5, jp = a & 31, tid = threadIdx.x, lane = tid & 63;
  const int w = __builtin_amdgcn_readfirstlane(tid >> 6);
  STAMP_DECL;
  STAMP(0);
  __builtin_amdgcn_s_setprio(APRIO);
  constexpr int SROW = 528, STG = 32 * SROW, UC = 0, UCS = 4160, RV = 36864, RVS = 4608, EX = RV + 2 * RVS, UIM = 4 * STG;
  const int PKS = UIM + 8192 + (w ? 1024 + w * 1024 : 0);
  const __amdgpu_buffer_rsrc_t rs = RSRC(ws, WS_END);
  const int ch = w >> 1, hf = w & 1, d = 2 * a + ch;
  const int r32 = lane & 31, half = lane >> 5;
  const int pkh_ = w == 0 ? (a & 15) : 2 * (a & 7) + (w - 1);
  {
    const __amdgpu_buffer_rsrc_t rp = RSRC(w == 0 ? wo : wqkv, w == 0 ? 448 * 896 : w < 3 ? 448 * 2688 : 0);
    const u32 vo = (lane >> 2) * (w == 0 ? 896 : 2688) + (lane & 3) * 16;
    const u32 so = w == 0 ? 28 * 896 * pkh_ + (a >> 4) * 64 : 16 * 2688 * (a >> 3) + ((56 * pkh_) & ~15);
    DMA16(rp, vo, so, lds + PKS, 0);
    DMA16(rp, vo, w == 0 ? so + 16 * 896 : 448 * 2688, lds + (w == 0 ? PKS + 1024 : UIM + 8192 + 5120), 0);
  }
  u32x4 dk;
  auto load_tile = [&]() {
#pragma unroll
    for (int i = 0; i < 2; ++i)
      DMA16(RSRC(u, 2048 * 896), (tid >> 3) * 896 + (((tid & 7) ^ ((tid >> 5) & 7)) << 4), (64 * jp + 32 * i) * 896 + cb * 128, lds + UIM + (32 * i + 8 * w) * 128, NTA ? 2 : 0);
    dk = *(const GAS u32x4*)(Dsk + cb * 128 + (tid & 7) * 16);
  };
  PRIO(ABASE);
  int tix[8];
#pragma unroll
  for (int j = 0; j < 8; ++j) {
    const int p = (int)rbf((float)(2047 - 8 * tid - j));
    const int low = p & 7, blk = p >> 8;
    const int k4 = (blk < 4 ? blk : 4) * 4;
    const int sh = (0x32210 >> k4) & 15;
    const int base = blk + ((0xCBA70 >> k4) & 15);
    tix[j] = ((base + (low >> sh)) * 32 + ((p >> 3) & 31)) * 4;
    PIN(tix[j]);
  }
  const u32 tag = take_tag(epoch);
  auto publish_weights = [&]() {
  PRIO(APRIO);
  BAR();
  if (w == 0) {
    const int q = (lane >> 2) & 3, gam = 4 * ((lane >> 4) & 1) + (lane & 3), ng = 4 * (gam & 1) + (gam >> 1);
    LAS char* p = lds + PKS + (14 * half + q) * 64 + 8 * ng;
    const u32x2 r0 = tr_read(p), r1 = tr_read(p + 256), r2 = tr_read(p + 512), r3 = tr_read(p + 768);
    const u32 off = WS_WO + ((a >> 4) * 16 + pkh_) * 2048 + lane * 16;
    ST_SC1(((u32x4){r0.x, r0.y, r1.x, r1.y}), rs, off);
    ST_SC1(((u32x4){r2.x, r2.y, r3.x, tag - ((r0.x + r0.y + r1.x) + (r1.y + r2.x + r2.y) + r3.x)}), rs, off + 1024);
  } else if (w < 3) {
    LAS char* p = lds + PKS + (8 * half + ((lane >> 2) & 3)) * 64 + ((lane >> 4) & 1) * 32 + (lane & 3) * 8;
    const u32x2 r0 = tr_read(p), r1 = tr_read(p + 256);
    u32x4 v = {r0.x, r0.y, r1.x, r1.y};
    const bool ck = lane == ((pkh_ & 1) ? 1 : 29);
    if (ck) v = (u32x4){0u, 0u, 0u, 0u};
    const u32 c = tag - wave_sum(sum4(0u, v));
    if (ck) v.x = c;
    ST_SC1(v, rs, WS_WQ + (pkh_ * 28 + (a >> 3)) * 1024 + lane * 16);
    if (lane < 16) ST1_SC1(tag, rs, WS_MBQ + (pkh_ * 16 + lane) * 128 + (a >> 3) * 4);
  }
  PRIO(ABASE);
  };
  auto publish_u = [&]() {
  PRIO(APRIO);
  BAR();
  {
    const int n = tid & 15;
    u32x4 v[2];
#pragma unroll
    for (int i = 0; i < 2; ++i) {
      const int jj = 16 * i + (tid >> 4);
      LAS char* p = lds + UIM + 4 * n * 128 + (((jj >> 2) ^ (n & 7)) << 4) + (jj & 3) * 4;
      v[i] = (u32x4){*(LAS u32*)p, *(LAS u32*)(p + 128), *(LAS u32*)(p + 256), *(LAS u32*)(p + 384)};
    }
    const u32 off = WS_UX + (cb * 32 + (tid >> 4)) * 8192 + jp * 256 + n * 16;
    ST_SC1(v[0], rs, off); ST_SC1(v[1], rs, off + 16 * 8192);
    const u32 ck = tag - row_sum2(sum4(0u, v[0]), sum4(0u, v[1]));
    __builtin_amdgcn_raw_buffer_store_b64((u32x2){tag, ck}, rs, n == 15 || n == 0 ? WS_MBU + (cb * 32 + (tid >> 4) + (n ? 0 : 16)) * 256 + jp * 8 : WS_END, 0, 16);
  }
  PRIO(ABASE);
  };
  WAITVM(2);
  PIN(pA); PIN(pI); PIN(pR); PIN(pC); PIN(ldt);
  STAMP(1);
  const float dt = rbf(exp_acc(ch ? bfhi(ldt) : bflo(ldt)));
  const u32 mx = wave_umax(umax3(umax3(pR & 0x7fffu, (pR >> 16) & 0x7fffu, pC & 0x7fffu), (pC >> 16) & 0x7fffu, 0u));
  int ex = mx >> 7; ex = ex < 13 ? 13 : ex > 254 ? 254 : ex;
  const float sc = __uint_as_float((u32)(266 - ex) << 23), isc = __uint_as_float((u32)(ex - 12) << 23);
  LAS char* stg = lds + w * STG;
  u32x4 ud[3];
  const __amdgpu_buffer_rsrc_t ru = RSRC(ws + WS_UX + a * 8192, 8192);
  const int npc = w < 3 ? 9 : 8;
  u64 mail = 0;
  const u32 mbu = WS_MBU + a * 256 + (8 * w + lane) * 8;
  auto load_u = [&]() {
    PRIO(APRIO);
    ud[0] = LD_SC1(ru, w * 2048 + lane * 16); ud[1] = LD_SC1(ru, w * 2048 + 1024 + lane * 16);
    ud[2] = LD_SC1(ru, lane < 16 ? w * 2048 + 2048 + lane * 16 : 8192);
    PRIO(ABASE);
  };
  bool have_u = false;
  {
    u32x2 c0, y1, y2, y4, q1, q2, q4, q8, z8, z16, z32, z64, z128;
#pragma unroll
    for (int m = 0; m < 2; ++m) {
      const float la = m ? bfhi(pA) : bflo(pA), ai = m ? bfhi(pI) : bflo(pI), cr = m ? bfhi(pR) : bflo(pR), ci = m ? bfhi(pC) : bflo(pC);
      const float a2 = -rbf(exp_acc(la)) * dt * 1.4426950408889634f, f = ai * dt * 0.15915494309189535f;
      f32x2 a = cexp_p(a2, -f, 1.f), b = cexp_p(a2, -f, 256.f), c = cexp_p(a2, f, 8.f);
      c0[m] = pkh((f32x2){cr * sc, -ci * sc});
      y1[m] = pkh(a); a = csq(a); y2[m] = pkh(a); a = csq(a); y4[m] = pkh(a);
      q1[m] = pkh(b); b = csq(b); q2[m] = pkh(b); b = csq(b); q4[m] = pkh(b); b = csq(b); q8[m] = pkh(b);
      z8[m] = pkh(c); c = csq(c); z16[m] = pkh(c); c = csq(c); z32[m] = pkh(c); c = csq(c); z64[m] = pkh(c); c = csq(c); z128[m] = pkh(c);
    }
#define OPQ(v) asm("" : "+v"(v))
    OPQ(c0); OPQ(y1); OPQ(y2); OPQ(y4); OPQ(q1); OPQ(q2); OPQ(q4); OPQ(q8); OPQ(z8); OPQ(z16); OPQ(z32); OPQ(z64); OPQ(z128);
    LAS char* sw = stg + lane * 8;
    auto put = [&](int r, u32x2 v) { *(LAS u32x2*)(sw + r * SROW) = v; };
    auto mul = [&](u32x2 x, u32x2 y) { const u32 x0 = x.x, x1 = x.y, y0 = y.x, y1_ = y.y; u32x2 r = {hmul(x0, y0), hmul(x1, y1_)}; OPQ(r); return r; };
    put(0, c0); put(21, (u32x2){0x3C00u, 0x3C00u}); put(22, z8); put(23, z16); put(25, z32); put(26, z64); put(28, z128);
    const u32x2 c2 = mul(c0, y2), c4 = mul(c0, y4), c512 = mul(c0, q2), c1024 = mul(c0, q4), z96 = mul(z32, z64);
    put(2, c2); put(4, c4); put(12, c512); put(16, c1024); put(27, z96);
    const u32x2 c6 = mul(c4, y2), c516 = mul(c4, q2), c1536 = mul(c1024, q2);
    put(6, c6); put(13, c516); put(18, c1536);
    put(1, mul(c0, y1)); put(3, mul(c2, y1)); put(5, mul(c4, y1)); put(7, mul(c6, y1));
    put(8, mul(c0, q1)); put(9, mul(c2, q1)); put(10, mul(c4, q1)); put(11, mul(c6, q1));
    put(14, mul(c512, q1)); put(15, mul(c516, q1)); put(17, mul(c1024, q1)); put(19, mul(c1536, q1)); put(20, mul(c0, q8));
    put(24, mul(z8, z16)); put(29, mul(z32, z128)); put(30, mul(z64, z128)); put(31, mul(z96, z128));
  }
  STAMP(2);
  WAITVM(0);
  if (UEARLY == 3) { PRIO(APRIO); load_tile(); }
  publish_weights();
  STAMP(13);
  {
    const int c16 = lane & 15, quad = lane >> 4;
    LAS char* py0 = stg + c16 * SROW + quad * 16;
    LAS char* py1 = stg + (16 + (c16 < 4 ? c16 : 4)) * SROW + quad * 16;
    LAS char* pt1 = stg + (21 + (c16 & 3)) * SROW + quad * 16;
    LAS char* pt2 = stg + (c16 < 4 ? 21 : 24 + (c16 >> 2)) * SROW + quad * 16;
    LAS char* pt3 = stg + (28 + (c16 >> 2)) * SROW + quad * 16;
    f32x4 ka[4] = {};
    u32x4 ft[2][3], fy[2][2], xx[2][2];
    auto rdt = [&](int s) { ft[s & 1][0] = *(LAS u32x4*)(pt1 + s * 64); ft[s & 1][1] = *(LAS u32x4*)(pt2 + s * 64); ft[s & 1][2] = *(LAS u32x4*)(pt3 + s * 64); };
    auto rdy = [&](int s) { fy[s & 1][0] = *(LAS u32x4*)(py0 + s * 64); fy[s & 1][1] = *(LAS u32x4*)(py1 + s * 64); };
    auto prod = [&](int s, int i) {
      u32 t1 = ft[s & 1][0][i], t2 = ft[s & 1][1][i], t3 = ft[s & 1][2][i];
      asm("" : "+v"(t2), "+v"(t3));
      const u32 p0 = hmul1(t1, t2), p1 = hmul1(t1, t3);
      xx[s & 1][0][i] = hmul2(t1, t2, p0); xx[s & 1][1][i] = hmul2(t1, t3, p1);
    };
    rdt(0); rdt(1); rdy(0);
    prod(0, 0); prod(0, 1); prod(0, 2); prod(0, 3);
#pragma unroll
    for (int s = 0; s < 8; ++s) {
      if (s < 6) rdt(s + 2);
      if (s < 7) rdy(s + 1);
      SBAR();
#pragma unroll
      for (int i = 0; i < 4; ++i) {
        ka[i] = mfma16h(fy[s & 1][i >> 1], xx[s & 1][i & 1], ka[i]);
        if (s < 7) prod(s + 1, i);
        SBAR();
      }
    }
    if (UEARLY == 0) { __builtin_amdgcn_s_sleep(TDELAY); PRIO(APRIO); load_tile(); PRIO(ABASE); }
    STAMP(3);
    const float fac = 2.f * dt * isc;
    LAS float* tb = (LAS float*)stg + 4 * quad * 32 + c16;
#pragma unroll
    for (int g = 0; g < 4; ++g) { tb[g * 32] = ka[0][g] * fac; tb[g * 32 + 16] = ka[1][g] * fac; }
    if (quad < 2) {
#pragma unroll
      for (int g = 0; g < 4; ++g) { tb[(16 + g) * 32] = ka[2][g] * fac; tb[(16 + g) * 32 + 16] = ka[3][g] * fac; }
    }
  }
  STAMP(4);
  if (UEARLY == 3) { WAITVM(0); publish_u(); } else BAR();
  {
    float kk[4][8];
#pragma unroll
    for (int c = 0; c < 4; ++c)
#pragma unroll
      for (int j = 0; j < 8; ++j) kk[c][j] = *(LAS float*)(lds + c * STG + tix[j]);
    SBAR();
#pragma unroll
    for (int c = 0; c < 2; ++c) {
      u32x4 v;
#pragma unroll
      for (int j = 0; j < 4; ++j) v[j] = pkbf(kk[2 * c][2 * j] + kk[2 * c + 1][2 * j], kk[2 * c][2 * j + 1] + kk[2 * c + 1][2 * j + 1]);
      *(LAS u32x4*)(lds + RV + c * RVS + tid * 16) = v;
    }
  }
  STAMP(11);
  if (UEARLY) { PRIO(APRIO); mail = poll64(ws, mbu, lane < npc); PRIO(ABASE); BAR(); } else { WAITVM(0); publish_u(); }
  STAMP(12);
  u32 sk[8];
#pragma unroll
  for (int i = 0; i < 2; ++i) {
    const u32x4 us = *(LAS u32x4*)(lds + UIM + (32 * i + (tid >> 3)) * 128 + (((tid & 7) ^ ((tid >> 5) & 7)) << 4));
#pragma unroll
    for (int k = 0; k < 4; ++k) { sk[4 * i + k] = pkbf(bflo(us[k]) * bflo(dk[k]), bfhi(us[k]) * bfhi(dk[k])); PIN(sk[4 * i + k]); }
  }
  if (!UEARLY) { PRIO(APRIO); mail = poll64(ws, mbu, lane < npc); PRIO(ABASE); }
  STAMP(14);
  if (tid < 64) *(LAS u32x4*)(lds + RV + half * RVS + 4096 + r32 * 16) = (u32x4){0u, 0u, 0u, 0u};
  SBAR();
#pragma nounroll
  for (int it = 0; it < SPIN_CAP; ++it) {
    if (!have_u) {
#pragma nounroll
      for (int i2 = 0; i2 < SPIN_CAP && __popcll(__ballot((u32)mail == tag)) != npc; ++i2) { __builtin_amdgcn_s_sleep(ASLEEP); mail = poll64(ws, mbu, lane < npc); COUNT(14); }
      load_u();
    }
    if (wave_sum(sum4(sum4(sum4((u32)(mail >> 32), ud[0]), ud[1]), ud[2])) == npc * tag) break;
    have_u = false;
    asm volatile("" ::: "memory");
    COUNT(11);
  }
  STAMP(15);
#pragma unroll
  for (int i = 0; i < 2; ++i) {
    const u32 nx = __builtin_amdgcn_update_dpp(__builtin_amdgcn_readlane(ud[i + 1].x, 0), ud[i].x, 0x130, 0xf, 0xf, false);
    const u32 dw[5] = {ud[i].x, ud[i].y, ud[i].z, ud[i].w, nx};
    LAS char* p = lds + UC + (128 * w + 64 * i + lane + 4) * 8;
#pragma unroll
    for (int c = 0; c < 2; ++c) {
      u32 pr[4];
#pragma unroll
      for (int k = 0; k < 4; ++k) pr[k] = __builtin_amdgcn_perm(dw[k + 1], dw[k], c ? 0x07060302u : 0x05040100u);
      *(LAS u32x2*)(p + (4 * c) * UCS) = (u32x2){pr[0], pr[2]};
      *(LAS u32x2*)(p + (4 * c + 1) * UCS) = (u32x2){pr[1], pr[3]};
      *(LAS u32*)(p + (4 * c + 2) * UCS - 4) = pr[0]; *(LAS u32*)(p + (4 * c + 2) * UCS) = pr[2];
      *(LAS u32*)(p + (4 * c + 3) * UCS - 4) = pr[1]; *(LAS u32*)(p + (4 * c + 3) * UCS) = pr[3];
    }
  }
  if (w == 0) {
    const u32 u0 = __builtin_amdgcn_readlane(ud[0].x, 0);
    const int cs = lane >> 3, ix = lane & 7, last = (cs & 2) ? 6 : 7;
    const u32 v = ((cs & 1) && ix == last) ? (cs & 4 ? u0 & 0xffff0000u : u0 << 16) : 0u;
    if (ix <= last) *(LAS u32*)(lds + UC + cs * UCS + ix * 4) = v;
  }
  STAMP(16);
  BAR();
  STAMP(5);
  PRIO(ACONV);
  {
    const int c16 = lane & 15, hq = lane >> 4;
    LAS char* pa = lds + RV + ch * RVS + 2 * (2032 - 256 * 7 + 128 * hf - 16 * c16 + 8 * hq);
    LAS char* pb = lds + UC + (4 * ch + ((c16 + 1) & 3)) * UCS + 2 * (128 * hf + 8 * hq + 4 * ((c16 + 1) >> 2));
    f32x4 acc[8] = {};
    u32x4 fa[2][8], fb[2][8];
    auto ldf = [&](int rho, int m) {
      fa[rho & 1][m] = *(LAS u32x4*)(pa + 64 * rho + 512 * (7 - m));
      const u32x2 lo = *(volatile LAS u32x2*)(pb + 64 * rho + 512 * m), hi = *(volatile LAS u32x2*)(pb + 64 * rho + 512 * m + 8);
      fb[rho & 1][m] = (u32x4){lo.x, lo.y, hi.x, hi.y};
    };
#pragma unroll
    for (int m = 0; m < 8; ++m) ldf(0, m);
    u32 ydst = WS_YX + (cb * 32 + 8 * w + hq) * 8192 + jp * 256 + c16 * 16;
    u32 ymb = c16 == 15 || c16 == 0 ? WS_MBY + (cb * 32 + 8 * w + hq + (c16 ? 0 : 4)) * 256 + jp * 8 : WS_END;
    LAS char* exw = lds + EX + w * 8192 + lane * 16;
    LAS char* exr = lds + EX + w * 2048 + lane * 16;
    PIN(ydst); PIN(ymb); PIN(exw); PIN(exr);
#pragma unroll
    for (int rho = 0; rho < 3; ++rho) {
      SBAR();
#pragma unroll
      for (int g = 0; g < 8; ++g) {
#pragma unroll
        for (int aa = 0; aa <= g; ++aa) {
          if (aa + g < 8) acc[aa + g] = mfma16(fa[rho & 1][g], fb[rho & 1][aa], acc[aa + g]);
          if (aa < g && aa + g < 8) acc[aa + g] = mfma16(fa[rho & 1][aa], fb[rho & 1][g], acc[aa + g]);
        }
        ldf(rho + 1, g); SBAR();
      }
    }
#pragma unroll
    for (int t = 0; t < 8; ++t)
#pragma unroll
      for (int aa = 0; aa <= t; ++aa) {
        acc[t] = mfma16(fa[1][t - aa], fb[1][aa], acc[t]);
        SBAR();
#pragma unroll
        for (int f = 0; f < 7; ++f) if (t * (t + 1) / 2 + aa == f * (f + 3) / 2 + 8) { *(LAS f32x4*)(exw + f * 1024) = acc[f]; SBAR(); }
      }
    *(LAS f32x4*)(exw + 7168) = acc[7];
    STAMP(6);
    PRIO(ATAIL);
    BAR();
    STAMP(18);
    f32x4 e[8];
#pragma unroll
    for (int k = 0; k < 8; ++k) e[k] = *(LAS f32x4*)(exr + (k >> 2) * 1024 + (k & 3) * 8192);
    u32x4 v[2];
#pragma unroll
    for (int i = 0; i < 2; ++i) {
      const f32x4 y0 = e[4 * i] + e[4 * i + 1], y1 = e[4 * i + 2] + e[4 * i + 3];
      v[i] = (u32x4){pkbf(y0.x, y1.x), pkbf(y0.y, y1.y), pkbf(y0.z, y1.z), pkbf(y0.w, y1.w)};
      ST_SC1(v[i], rs, ydst + i * 32768);
    }
    const u32 ck = tag - row_sum2(sum4(0u, v[0]), sum4(0u, v[1]));
    __builtin_amdgcn_raw_buffer_store_b64((u32x2){tag, ck}, rs, ymb, 0, 16);
  }
  STAMP(7);
  {
    u32x4 y[2];
    const u32 mby = WS_MBY + a * 256 + (8 * w + lane) * 8;
    mail = poll64(ws, mby, lane < 8);
    float sf[16];
#pragma unroll
    for (int k = 0; k < 8; ++k) { sf[2 * k] = bflo(sk[k]); sf[2 * k + 1] = bfhi(sk[k]); PIN(sf[2 * k]); PIN(sf[2 * k + 1]); }
    u32 ysrc = WS_YX + a * 8192 + w * 2048 + lane * 16, odst = (64 * jp + (tid >> 3)) * 1792 + cb * 128 + (tid & 7) * 16;
    LAS char* tw = lds + (lane & 15) * 144 + (8 * w + (lane >> 4)) * 4;
    LAS char* tr = lds + (tid >> 3) * 144 + (tid & 7) * 16;
    PIN(ysrc); PIN(odst); PIN(tw); PIN(tr);
#pragma nounroll
    for (int it = 0; it < SPIN_CAP && __ballot((u32)mail == tag) != 0xffull; ++it) { mail = poll64(ws, mby, lane < 8); COUNT(13); }
    STAMP(19);
#pragma nounroll
    for (int it = 0; it < SPIN_CAP; ++it) {
      asm volatile("" ::: "memory");
      y[0] = LD_SC1(rs, ysrc); y[1] = LD_SC1(rs, ysrc + 1024);
#pragma unroll
      for (int i = 0; i < 2; ++i)
#pragma unroll
        for (int g = 0; g < 4; ++g) *(LAS u32*)(tw + g * 2304 + i * 16) = y[i][g];
      if (wave_sum(sum4(sum4((u32)(mail >> 32), y[0]), y[1])) == 8u * tag) break;
      COUNT(12);
    }
    STAMP(8);
    BAR();
#pragma unroll
    for (int i = 0; i < 2; ++i) {
      const u32x4 v = *(LAS u32x4*)(tr + i * 4608);
      u32x4 o;
#pragma unroll
      for (int k = 0; k < 4; ++k) o[k] = pkbf(bflo(v[k]) + sf[8 * i + 2 * k], bfhi(v[k]) + sf[8 * i + 2 * k + 1]);
      __builtin_amdgcn_raw_buffer_store_b128(o, RSRC(out, 2048 * 1792), odst + i * (32 * 1792), 0, OUTAUX);
    }
  }
  STAMP(9);
  STAMP_FLUSH();
}

__global__ void NOPK __launch_bounds__(256, 2)
fused_kernel(const void*, const void*, const void*, const void*, const void*, const void*, const void*, const void*, const void*,
             const void*, void*, void*) {
  __shared__ __attribute__((aligned(16))) char smem[81920];
  LAS char* lds = (LAS char*)smem;
  if ((blockIdx.x >> 5) == 7) {
    u64 pc;
    asm volatile("s_getpc_b64 %0" : "=s"(pc));
    if (threadIdx.x < CODE_LINES / 4)
      __builtin_amdgcn_global_load_lds((const GAS void*)((pc & ~127ull) + (threadIdx.x * 4 + ((blockIdx.x >> 3) & 3)) * 128), (LAS void*)lds, 4, 0, 0);
  }
  const __attribute__((address_space(4))) u64* ka = (const __attribute__((address_space(4))) u64*)__builtin_amdgcn_kernarg_segment_ptr();
  asm volatile("" : "+s"(ka));
  const char* u = (const char*)ka[0]; const GAS u32* logA = (const GAS u32*)ka[1]; const GAS u32* Aim = (const GAS u32*)ka[2];
  const GAS u32* Cre = (const GAS u32*)ka[3]; const GAS u32* Cim = (const GAS u32*)ka[4]; const GAS u32* logdt = (const GAS u32*)ka[5];
  const char* Dsk = (const char*)ka[6]; const char* x = (const char*)ka[7]; const char* wqkv = (const char*)ka[8];
  const char* wo = (const char*)ka[9]; char* out = (char*)ka[10]; char* ws = (char*)ka[11];
  asm volatile("" :: "s"(u), "s"(logA), "s"(Aim), "s"(Cre), "s"(Cim), "s"(logdt), "s"(Dsk), "s"(x), "s"(wqkv), "s"(wo), "s"(out), "s"(ws));
  const u32 epoch = request_epoch(ws);
  int bid = blockIdx.x;
  if (bid >= 256) role_b<true>(lds, x, wqkv, out, ws, epoch);
  asm volatile("" : "+s"(bid) :: "memory");
  if (bid >= 224) {
    if (bid >= 256) role_b<false>(lds, x, wqkv, out, ws, epoch);
  else if (bid == 224) {
      const int t = threadIdx.x;
      const u64 t00 = __builtin_amdgcn_s_memrealtime();
      while (__builtin_amdgcn_s_memrealtime() < t00 + 3000) __builtin_amdgcn_s_sleep(32);
      u64 mn = ~0ull, mxv = 0, sum = 0;
      for (int i = 0; i < 8; ++i) {
        const int wv = t + 256 * i, blk = wv >> 2;
        if (blk >= 224 && blk < 256) continue;
        const GAS u64* p = (const GAS u64*)(ws + WS_DBG) + wv * 2;
        const u64 s0 = __hip_atomic_load(p, RLX_AGENT), sk = RK ? __hip_atomic_load(p + 1, RLX_AGENT) : s0;
        if (((READOUT >> 5) & 1) == (blk >= 256)) { mxv = sk > mxv ? sk : mxv; sum += sk - t00 + 1000; }
      }
      LAS u64* l = (LAS u64*)lds;
      l[256 + t] = mxv; l[512 + t] = sum;
      BAR();
      sum = 0;
      for (int i = 0; i < 256; ++i) { mxv = l[256 + i] > mxv ? l[256 + i] : mxv; sum += l[512 + i]; }
      const u64 avg = sum / (((READOUT >> 5) & 1) ? 1024 : 896);
      const u64 until = t00 + 5000 + ((READOUT & 128) ? 1000 : (READOUT & 64) ? avg : mxv - t00 + 1000);
      for (int it = 0; it < 1000000 && __builtin_amdgcn_s_memrealtime() < until; ++it) __builtin_amdgcn_s_sleep(1);
  } }
  else if (bid < 224) {
    const int pi = bid * 256 + threadIdx.x;
    const u32 pA = __builtin_nontemporal_load(logA + pi), pI = __builtin_nontemporal_load(Aim + pi);
    const u32 pR = __builtin_nontemporal_load(Cre + pi), pC = __builtin_nontemporal_load(Cim + pi), ldt = logdt[bid];
    SBAR();
    role_a(lds, u, Dsk, wqkv, wo, out, ws, epoch, pA, pI, pR, pC, ldt);
  }
}

extern "C" void kernel_launch(void* const* d_in, const int* in_sizes, int n_in, void* d_out, int out_size, void* d_ws,
                              size_t ws_size, hipStream_t stream) {
  fused_kernel<<<512, 256, 0, stream>>>(d_in[0], d_in[1], d_in[2], d_in[3], d_in[4], d_in[5], d_in[6], d_in[7], d_in[8], d_in[9], d_out, d_ws);
}
```

```cpp
#define UEARLY 3
#define HEADC 0
#include <hip/hip_runtime.h>
#include <stdint.h>

typedef unsigned short u16;
typedef unsigned u32;
typedef unsigned long long u64;
typedef short s16x4 __attribute__((ext_vector_type(4)));
typedef __bf16 bf16x8 __attribute__((ext_vector_type(8)));
typedef __bf16 bf16x2 __attribute__((ext_vector_type(2)));
typedef _Float16 f16x8 __attribute__((ext_vector_type(8)));
typedef _Float16 f16x2 __attribute__((ext_vector_type(2)));
typedef float f32x16 __attribute__((ext_vector_type(16)));
typedef float f32x4 __attribute__((ext_vector_type(4)));
typedef float f32x2 __attribute__((ext_vector_type(2)));
typedef u32 u32x4 __attribute__((ext_vector_type(4)));
typedef u32 u32x2 __attribute__((ext_vector_type(2)));
#define LAS __attribute__((address_space(3)))
#define GAS __attribute__((address_space(1)))
#define DI __device__ __forceinline__
#if defined(__HIP_DEVICE_COMPILE__)
#define NOPK __attribute__((target("no-packed-fp32-ops")))
#else
#define NOPK
#endif
#define RLX_AGENT __ATOMIC_RELAXED, __HIP_MEMORY_SCOPE_AGENT

#define BMAP 1
#define PREFETCH 1
#define CODE_BEFORE 0
#define CODE_LINES 208
#define QEARLY 3
#define GATH_AT 18
#define WO_EARLY 0
#define WO_MID 12
#define WO_G 32
#define WO_E 4
#define KVSTEP 1
#define PUB_FIRST 0
#define UPOLL_AT 8
#define TMAP 1
#define BPRIO1 2
#define ABASE 0
#define ACONV 0
#define L2PF 0
#define NTA 1
#define NTX 0
#define W0FIRST 1
#define ORD 4
#define PF2AT 1
#define UMID 0
#define WFIRST 0
#define ASLEEP 0
#define TDELAY 0
#define EPW 255
#define OUTAUX 16
#define TELL2_LATE 1
#define ROT 1
#define APRIO 3
#define ATAIL 1
#define BPRIO 2

constexpr u32 WS_MB1 = 73728;
constexpr u32 WS_MB2 = WS_MB1 + 32768;
constexpr u32 WS_MBU = WS_MB2 + 32768;
constexpr u32 WS_MBY = WS_MBU + 57344;
constexpr u32 WS_KVP = 1u << 18;
constexpr u32 WS_OB = WS_KVP + (1u << 20);
constexpr u32 WS_UX = WS_OB + (2u << 20);
constexpr u32 WS_YX = WS_UX + (2u << 20);
constexpr u32 WS_WQ = WS_YX + (2u << 20);
constexpr u32 WS_WO = WS_WQ + (1u << 19);
constexpr u32 WS_MBQ = WS_WO + 458752;
constexpr u32 WS_END = WS_WO + (1u << 19);
constexpr u32 WS_DBG = WS_END;
constexpr int SPIN_CAP = 1 << 15;

#define STAMP(i) do {} while (0)
#define STAMP_DECL
#define COUNT(i) do {} while (0)
#define STAMP_FLUSH() do {} while (0)

DI float bflo(u32 v) { return __uint_as_float(v << 16); }
DI float bfhi(u32 v) { return __uint_as_float(v & 0xffff0000u); }
DI u32 pkbf(float a, float b) { f32x2 p = {a, b}; return __builtin_bit_cast(u32, __builtin_convertvector(p, bf16x2)); }
DI u32 pkh(f32x2 p) { return __builtin_bit_cast(u32, __builtin_convertvector(p, f16x2)); }
DI float rbf(float a) { return bflo(pkbf(a, a)); }
DI u32 umax3(u32 a, u32 b, u32 c) { u32 m = a > b ? a : b; return m > c ? m : c; }
DI u32 sum4(u32 m, u32x4 v) { return (m + v.x + v.y) + v.z + v.w; }
DI u32 row_sum(u32 v) {
  v += __builtin_amdgcn_update_dpp(0u, v, 0x111, 0xf, 0xf, true); v += __builtin_amdgcn_update_dpp(0u, v, 0x112, 0xf, 0xf, true);
  v += __builtin_amdgcn_update_dpp(0u, v, 0x114, 0xf, 0xf, true); v += __builtin_amdgcn_update_dpp(0u, v, 0x118, 0xf, 0xf, true);
  return v;
}
DI u32 row_sum2(u32 a, u32 b) {
  a += __builtin_amdgcn_update_dpp(0u, a, 0x111, 0xf, 0xf, true); b += __builtin_amdgcn_update_dpp(0u, b, 0x101, 0xf, 0xf, true);
  a += __builtin_amdgcn_update_dpp(0u, a, 0x112, 0xf, 0xf, true); b += __builtin_amdgcn_update_dpp(0u, b, 0x102, 0xf, 0xf, true);
  a += __builtin_amdgcn_update_dpp(0u, a, 0x114, 0xf, 0xf, true); b += __builtin_amdgcn_update_dpp(0u, b, 0x104, 0xf, 0xf, true);
  a += __builtin_amdgcn_update_dpp(0u, a, 0x118, 0xf, 0xf, true); b += __builtin_amdgcn_update_dpp(0u, b, 0x108, 0xf, 0xf, true);
  return (threadIdx.x & 8) ? a : b;
}
DI u32 wave_sum(u32 v) {
  v = row_sum(v);
  return (__builtin_amdgcn_readlane(v, 15) + __builtin_amdgcn_readlane(v, 31)) + (__builtin_amdgcn_readlane(v, 47) + __builtin_amdgcn_readlane(v, 63));
}
DI u32 wave_umax(u32 v) {
  u32 t;
  t = __builtin_amdgcn_update_dpp(0u, v, 0x111, 0xf, 0xf, false); v = v > t ? v : t;
  t = __builtin_amdgcn_update_dpp(0u, v, 0x112, 0xf, 0xf, false); v = v > t ? v : t;
  t = __builtin_amdgcn_update_dpp(0u, v, 0x114, 0xf, 0xf, false); v = v > t ? v : t;
  t = __builtin_amdgcn_update_dpp(0u, v, 0x118, 0xf, 0xf, false); v = v > t ? v : t;
  const u32 a = __builtin_amdgcn_readlane(v, 15), b = __builtin_amdgcn_readlane(v, 31), c = __builtin_amdgcn_readlane(v, 47), d = __builtin_amdgcn_readlane(v, 63);
  const u32 ab = a > b ? a : b, cd = c > d ? c : d;
  return ab > cd ? ab : cd;
}

DI u32 request_epoch(const char* ws) { u32 e; asm volatile("s_load_dword %0, %1, 0x0" : "=s"(e) : "s"(ws) : "memory"); return e; }
DI u32 take_tag(u32 e) { u32 r; asm volatile("s_waitcnt lgkmcnt(0)\n\ts_and_b32 %0, %1, -2" : "=s"(r) : "s"(e) : "memory", "scc"); return r * 1664525u + 2027808447u; }
#define PSCOPE __HIP_MEMORY_SCOPE_AGENT
DI u32 poll32(char* ws, u32 off, bool on) { u32 z = 0, r = 0; asm volatile("" : "+v"(z)); if (on) r = __hip_atomic_fetch_add((GAS u32*)(ws + off), z, __ATOMIC_RELAXED, PSCOPE); return r; }
DI u64 poll64(char* ws, u32 off, bool on) { u64 z = 0, r = 0; asm volatile("" : "+v"(z)); if (on) r = __hip_atomic_fetch_add((GAS u64*)(ws + off), z, __ATOMIC_RELAXED, PSCOPE); return r; }
#define SBAR() __builtin_amdgcn_sched_barrier(0)
#define PRIO(n) do { SBAR(); __builtin_amdgcn_s_setprio(n); SBAR(); } while (0)
#define PIN(x) asm volatile("" : "+v"(x))
DI f32x16 mfma32(u32x4 a, u32x4 b, f32x16 c) { return __builtin_amdgcn_mfma_f32_32x32x16_bf16(__builtin_bit_cast(bf16x8, a), __builtin_bit_cast(bf16x8, b), c, 0, 0, 0); }
DI f32x16 mfma32h(u32x4 a, u32x4 b, f32x16 c) { return __builtin_amdgcn_mfma_f32_32x32x16_f16(__builtin_bit_cast(f16x8, a), __builtin_bit_cast(f16x8, b), c, 0, 0, 0); }
DI f32x4 mfma16h(u32x4 a, u32x4 b, f32x4 c) { return __builtin_amdgcn_mfma_f32_16x16x32_f16(__builtin_bit_cast(f16x8, a), __builtin_bit_cast(f16x8, b), c, 0, 0, 0); }
DI f32x4 mfma16(u32x4 a, u32x4 b, f32x4 c) { return __builtin_amdgcn_mfma_f32_16x16x32_bf16(__builtin_bit_cast(bf16x8, a), __builtin_bit_cast(bf16x8, b), c, 0, 0, 0); }
DI u32x2 tr_read(LAS char* p) { return __builtin_bit_cast(u32x2, __builtin_amdgcn_ds_read_tr16_b64_v4i16((LAS s16x4*)p)); }
#define RSRC(p, n) __builtin_amdgcn_make_buffer_rsrc((void*)(p), 0, (n), 0x00020000)
#define DMA16(rs, voff, soff, dst, aux) __builtin_amdgcn_raw_ptr_buffer_load_lds((rs), (LAS void*)(dst), 16, (voff), (soff), 0, (aux))
#define WAITVM(n) do { asm volatile("" ::: "memory"); __builtin_amdgcn_s_waitcnt(0x0F70 | ((n) & 15) | (((n) >> 4) << 14)); asm volatile("" ::: "memory"); } while (0)
#define VMCNT(n) asm volatile("s_waitcnt vmcnt(" #n ")" ::: "memory")
#define LGKM0() asm volatile("s_waitcnt lgkmcnt(0)" ::: "memory")
#define BAR() do { asm volatile("s_waitcnt lgkmcnt(0)" ::: "memory"); __builtin_amdgcn_s_barrier(); asm volatile("" ::: "memory"); } while (0)
#define ST_SC1(v, rs, off) __builtin_amdgcn_raw_buffer_store_b128((v), (rs), (off), 0, 16)
#define LD_SC1(rs, off) __builtin_amdgcn_raw_buffer_load_b128((rs), (off), 0, 16)
#define LDS_SC1(rs, off, soff) __builtin_amdgcn_raw_buffer_load_b128((rs), (off), (soff), 16)
#define ST1_SC1(v, rs, off) __builtin_amdgcn_raw_buffer_store_b32((v), (rs), (off), 0, 16)
#define LD1_SC1(rs, off) __builtin_amdgcn_raw_buffer_load_b32((rs), (off), 0, 16)

typedef unsigned short u16x2 __attribute__((ext_vector_type(2)));
DI u32 phi1(float a, float b) {
  const u32 t = pkbf(a, b);
  const f32x2 x = {bflo(t), bfhi(t)};
  const f32x2 y = x * 1.4426950408889634f;
  const float e0 = __builtin_amdgcn_fmed3f(__builtin_amdgcn_exp2f(y.x), 0.f, 1.f);
  const float e1 = __builtin_amdgcn_fmed3f(__builtin_amdgcn_exp2f(y.y), 0.f, 1.f);
  const f32x2 x1 = x + 1.0f;
  return pkbf(__builtin_fmaxf(x1.x, e0), __builtin_fmaxf(x1.y, e1));
}
DI u32 pkmin(u32 a, u32 b) { return __builtin_bit_cast(u32, __builtin_elementwise_min(__builtin_bit_cast(u16x2, a), __builtin_bit_cast(u16x2, b))); }
DI void phi_eps(u32x4 (&p)[2], u32 mn) {
  if (__any((mn & 0xffffu) < 0x3A80u || mn < 0x3A800000u)) {
#pragma unroll
    for (int j = 0; j < 8; ++j) { const u32 r = p[j >> 2][j & 3]; p[j >> 2][j & 3] = pkbf(bflo(r) + 1e-6f, bfhi(r) + 1e-6f); }
  }
}

template <bool HEAD>
DI void role_b(LAS char* lds, const char* __restrict__ x, const char* __restrict__ wqkv, char* __restrict__ out, char* ws, u32 epoch) {
  const int b = blockIdx.x - 256, tid = threadIdx.x, lane = tid & 63;
  const int w = __builtin_amdgcn_readfirstlane(tid >> 6);
  STAMP_DECL;
  STAMP(0);
  const int rb = 4 * ((b & 7) >> 1) + (b >> 6), h = (b & 1) * 8 + ((b >> 3) & 7);
  const int start = (int)((0x1065432106543210ull >> (4 * ((h & 7) + 2 * (rb & 3)))) & 7);
  const __amdgpu_buffer_rsrc_t rs = RSRC(ws, WS_END), rx = RSRC(x, 2048 * 896), rw = RSRC(wqkv, 448 * 2688);
  constexpr int WR = 49152, KVI = WR + 4096, OST = 16384, OTR = WR;
#define XS(i) ((i) * 16384)
#define QS(cc) ((cc) < 2 ? WR + 24576 + (cc) * 4096 : (cc) < 6 ? XS(1) + ((cc) - 2) * 1024 : WR)
#define QD(cc) ((cc) >= 2 && (cc) < 6 ? 4096 : 1024)
  const int wsb = (56 * h) & ~15;
  const int coff = (h & 1) * 4;
  const int wone = (h & 1) ? 0 : 28;
  const u32 xv0 = (128 * rb + 32 * w + (lane >> 3)) * 896 + (((lane & 7) ^ (lane >> 4)) << 4);
  const u32 xv1 = (xv0 ^ 64) + 8 * 896;
  const u32 wv = (16 * w + (lane >> 2)) * 2688 + wsb + (lane & 3) * 16;
  const u32 l16 = lane * 16;
  int ch_[7];
  auto chunk = [&](int cc) { return ch_[cc]; };
  const int gi = h + rb;
  const u32 ordc = ORD == 1 ? 0x6543210u : ORD == 2 ? ((gi & 1) ? 0x5461032u : 0x6543210u) : ORD == 3 ? ((gi & 1) ? 0x6452301u : 0x6543210u)
                 : ((gi & 2) ? ((gi & 1) ? 0x4652103u : 0x5461032u) : ((gi & 1) ? 0x4650321u : 0x6543210u));
  auto next_chunk = [&](int cc) { int c = (ordc >> (4 * cc)) & 7; asm volatile("" : "+s"(c)); ch_[cc] = c; };
  auto issue_x = [&](int cc, int i) { DMA16(rx, (i & 1) ? xv1 : xv0, chunk(cc) * 128 + (i >> 1) * (16 * 896), lds + XS(cc % 3) + w * 4096 + i * 1024, NTX); };
  auto issue_w = [&](int cc, int i) { DMA16(rw, wv, chunk(cc) * (64 * 2688) + 896 * (i + 1), lds + WR + (cc & 3) * 8192 + i * 4096 + w * 1024, 0); };
  bool qok = true;
  u32 qso[7];
  auto issue_q = [&](int cc) { DMA16(rs, l16, qok ? qso[cc] : WS_END, lds + QS(cc) + w * QD(cc), 16); };
#pragma unroll
  for (int cc = 0; cc < 2; ++cc) {
    next_chunk(cc);
    if (HEAD == (cc < HEADC)) { issue_w(cc, 0); issue_w(cc, 1); issue_x(cc, 0); issue_x(cc, 1); issue_x(cc, 2); issue_x(cc, 3); }
    SBAR();
  }
  if (HEAD) return;
  next_chunk(2); next_chunk(3); next_chunk(4); next_chunk(5); next_chunk(6);
  __builtin_amdgcn_s_setprio(BPRIO1);
  const int r32 = lane & 31, half = lane >> 5;
  const int xrd = w * 4096 + r32 * 128 + ((half ^ ((r32 >> 1) & 7)) << 4);
  LAS char* whi = lds + WR + (8 * half + ((lane >> 2) & 3)) * 64 + ((lane >> 4) & 1) * 32 + (lane & 3) * 8;
  PIN(whi);
  const int rt = h >> 2, ct = (h + ((h & 8) ? 2 : 1)) & 3;
  u32 wo_src = WS_WO + (4 * ct + w < 14 ? 4 * ct + w : 13) * 32768, kvp_src = WS_KVP + h * 65536 + w * 16384;
  u32 ob_dst = WS_OB + ((rb * 4 + w) * 16 + h) * 2048 + l16, ob_src = WS_OB + ((rb * 4 + rt) * 16 + 4 * w) * 2048;
  asm volatile("" : "+s"(wo_src), "+s"(kvp_src), "+s"(ob_src));
  PIN(ob_dst);
  u32 tell1 = lane < 4 ? WS_MB1 + (h * 16 + 4 * w + lane) * 128 + rb * 4 : WS_END;
  u32 tell2 = lane < 4 ? WS_MB2 + (rb * 16 + 4 * w + lane) * 128 + h * 4 : WS_END;
  u32 mb1 = WS_MB1 + (h * 16 + rb) * 128 + ((lane + rb) & 15) * 4, mb2 = WS_MB2 + (rb * 16 + h) * 128 + ((4 * w + lane) & 15) * 4;
  u32 kvp_dst = kvp_src + rb * 1024 + l16, kvs[16];
#pragma unroll
  for (int p = 1; p < 16; ++p) { kvs[p] = kvp_src + ((p + rb) & 15) * 1024; asm volatile("" : "+s"(kvs[p])); }
  const int dw = 8 * w + 4 * half - coff;
  u32 rowmask = dw < 0 || dw >= 28 ? 0u : ~0u;
  const bool padlo = (h & 1) && !half, padhi = !(h & 1) && half;
  const int oi = (r32 & 3) + 4 * (r32 >> 3);
  LAS char* kvrd = lds + KVI + ((oi < 14 ? 14 * ((r32 >> 2) & 1) + oi + coff : wone) + 32 * half) * 32;
  LAS char* qlo = lds + l16; LAS char* qhi = lds + WR + l16;
  PIN(tell1); PIN(tell2); PIN(mb1); PIN(mb2); PIN(kvp_dst); PIN(rowmask); PIN(kvrd); PIN(qhi);
  const bool ck = lane == wone + 1;
  u32 mbq = WS_MBQ + (h * 16 + rb) * 128 + (4 * (lane & 7) + w) * 4, mail = 0, tag = 0;
  PIN(mbq);
  u32x4 xf[28], wf[2][8];
  f32x16 ak = {}, av = {};
  auto rd = [&](int cc, int i) {
    if ((i & 1) == 0) xf[cc * 4 + (i >> 1)] = *(LAS u32x4*)(lds + XS(cc % 3) + (xrd ^ ((i >> 1) << 5)));
    LAS char* p = whi + (cc & 3) * 8192 + (i >> 1) * 1024 + (i & 1) * 4096;
    const u32x2 lo = tr_read(p), hi = tr_read(p + 256);
    wf[cc & 1][i] = (u32x4){lo.x, lo.y, hi.x, hi.y};
  };
#pragma unroll
  for (int cc = 0; cc < 6; ++cc) {
    if (cc == 0) VMCNT(6); else if (cc < 5) VMCNT(8); else VMCNT(7);
    BAR();
    if (cc == 5) __builtin_amdgcn_s_setprio(BPRIO);
    if (cc == 0) { STAMP(1); tag = take_tag(epoch); }
    if (cc == 2) { STAMP(23); } if (cc == 4) { STAMP(24); } if (cc == 5) { STAMP(25); }
    if (cc == 5) {
#pragma unroll
      for (int i = 0; i < 8; ++i) { rd(cc, i); if (i < 7) { qso[i] = WS_WQ + (h * 28 + 4 * chunk(i) + w) * 1024; asm volatile("" : "+s"(qso[i])); } }
      SBAR();
    }
#pragma unroll
    for (int i = 0; i < 8; ++i) {
      if (cc > 0) {
        if (i & 1) av = mfma32(xf[(cc - 1) * 4 + (i >> 1)], wf[(cc - 1) & 1][i], av);
        else ak = mfma32(xf[(cc - 1) * 4 + (i >> 1)], wf[(cc - 1) & 1][i], ak);
      }
      if (cc < 5) rd(cc, i);
      if (cc == 0) { if (i < 2) issue_w(2, i); else if (i < 6) issue_x(2, i - 2); else issue_w(3, i - 6); }
      else if (cc <= 4 && i < 4) issue_x(cc + 2, i);
      else if (cc <= 3 && i < 6) issue_w(cc + 3, i - 4);
      else if (cc == 4 && i == 4) mail = poll32(ws, mbq, lane < 7);
      SBAR();
    }
#pragma unroll
    for (int s = 3; s >= 0; --s) { PIN(wf[cc & 1][2 * s + 1]); PIN(wf[cc & 1][2 * s]); PIN(xf[cc * 4 + s]); }
    if (cc > 0) { PIN(ak); PIN(av); }
  }
  VMCNT(1);
  BAR();
  STAMP(26);
  rd(6, 0); rd(6, 2); rd(6, 4); rd(6, 6);
  SBAR();
#pragma unroll
  for (int s = 0; s < 4; ++s) ak = mfma32(xf[20 + s], wf[1][2 * s], ak);
  PIN(ak);
#pragma unroll
  for (int s = 3; s >= 0; --s) { PIN(wf[0][2 * s]); PIN(xf[24 + s]); }
  rd(6, 1); rd(6, 3); rd(6, 5); rd(6, 7);
  SBAR();
#pragma unroll
  for (int s = 0; s < 4; ++s) ak = mfma32(xf[24 + s], wf[0][2 * s], ak);
  PIN(ak);
  SBAR();
  WAITVM(0);
  PIN(mail);
  qok = __ballot(mail == tag) == 0x7full;
  STAMP(2);
  u32x4 wof[32], d[16], pq[2];
  auto load_wo = [&](int k) { wof[k] = __builtin_amdgcn_raw_buffer_load_b128(rs, l16 + (k & 3) * 1024, wo_src + (k >> 2) * 4096, 16); };
  {
    u32x4 pk[2], pv[2];
    u32 mn = ~0u;
#pragma unroll
    for (int j = 0; j < 8; j += 2) {
      av = mfma32(xf[20 + j], wf[j < 4][2 * (j & 3) + 1], av);
      issue_q(j); if (j < 6) issue_q(j + 1);
      SBAR();
      pk[j >> 2][j & 3] = phi1(ak[2 * j], ak[2 * j + 1]);
      av = mfma32(xf[21 + j], wf[j < 4][2 * (j & 3) + 3], av);
      pk[j >> 2][(j & 3) + 1] = phi1(ak[2 * j + 2], ak[2 * j + 3]);
      mn = pkmin(pkmin(mn, pk[j >> 2][j & 3]), pk[j >> 2][(j & 3) + 1]);
      PIN(mn); PIN(av);
      SBAR();
    }
    phi_eps(pk, mn);
    STAMP(11);
#pragma unroll
    for (int j = 0; j < 8; ++j) { const u32 v = pkbf(av[2 * j], av[2 * j + 1]); pv[j >> 2][j & 3] = r32 == wone ? 0x3F803F80u : v; }
    f32x16 kv = {};
    kv = mfma32(pk[0], pv[0], kv); kv = mfma32(pk[1], pv[1], kv);
    ST1_SC1(tag, rs, tell1);
#pragma unroll
    for (int g = 0; g < 4; ++g) *(LAS f32x4*)(lds + XS(2) + w * 4096 + g * 1024 + l16) = (f32x4){kv[4 * g], kv[4 * g + 1], kv[4 * g + 2], kv[4 * g + 3]};
    STAMP(12);
    BAR();
    f32x4 s4 = *(LAS f32x4*)(lds + XS(2) + tid * 16);
#pragma unroll
    for (int ww = 1; ww < 4; ++ww) s4 += *(LAS f32x4*)(lds + XS(2) + ww * 4096 + tid * 16);
    u32x4 v = __builtin_bit_cast(u32x4, s4);
    const u32 part = v.y + v.z + v.w;
    const u32 c = tag - wave_sum(ck ? part : part + v.x);
    if (ck) v.x = c;
    ST_SC1(v, rs, kvp_dst);
    d[0] = v;
  }
  if (!qok) {
#pragma nounroll
    for (int it = 0; it < SPIN_CAP && __ballot(poll32(ws, mbq, lane < 7) == tag) != 0x7full; ++it) {}
    qok = true;
#pragma unroll
    for (int cc = 0; cc < 7; ++cc) issue_q(cc);
    WAITVM(0);
    COUNT(13);
  }
  u32 go, need = 0xfffeu;
  mail = poll32(ws, mb1, lane < 16);
#pragma unroll
  for (int k = 0; k < WO_E; ++k) load_wo(k);
  WAITVM(WO_E + 3);
  BAR();
  STAMP(3);
#define QP(cc) (QS(cc) >= WR ? qhi + (QS(cc) - WR) : qlo + QS(cc))
  auto load_kv = [&](int p, bool on = true) { d[p] = __builtin_amdgcn_raw_buffer_load_b128(rs, l16, on ? kvs[p] : WS_END, 16); };
  {
    f32x16 aq = {};
    u32x4 qf[2][4];
    u32 qsum = 0;
    auto read_q = [&](int cc) {
#pragma unroll
      for (int s = 0; s < 4; ++s) qf[cc & 1][s] = *(LAS u32x4*)(QP(cc) + s * QD(cc));
      SBAR();
    };
    read_q(0);
#pragma unroll
    for (int cc = 0; cc < 7; ++cc) {
      if (cc < 6) read_q(cc + 1);
#pragma unroll
      for (int s = 0; s < 4; ++s) {
        aq = mfma32(qf[cc & 1][s], xf[cc * 4 + s], aq);
        qsum = sum4(qsum, qf[cc & 1][s]);
        if (s & 1) load_wo(WO_E + 2 * cc + (s >> 1));
        PIN(aq); PIN(qsum);
        SBAR();
      }
    }
    if (wave_sum(qsum) != 28u * tag) {
      aq = (f32x16){};
#pragma nounroll
      for (int k2 = 0; k2 < 28; ++k2) {
        const int ks = 4 * ((ordc >> (k2 & 28)) & 7) + (k2 & 3);
        u32x4 f;
#pragma nounroll
        for (int it = 0; it < SPIN_CAP; ++it) {
          asm volatile("" ::: "memory");
          f = LDS_SC1(rs, l16, WS_WQ + (h * 28 + ks) * 1024);
          if (wave_sum(sum4(0u, f)) == tag) break;
        }
        aq = mfma32(f, __builtin_amdgcn_raw_buffer_load_b128(rx, (128 * rb + 32 * w + r32) * 896 + half * 16, ks * 32, 0), aq);
      }
      COUNT(15);
    }
    STAMP(14);
    go = need & (u32)__ballot(mail == tag); need &= ~go;
    mail = poll32(ws, mb1, lane < 16 && need != 0);
    STAMP(15);
    u32 mn = ~0u;
#pragma unroll
    for (int j = 0; j < 8; ++j) {
      if (j) load_kv(2 * j, go >> (2 * j) & 1);
      load_kv(2 * j + 1, go >> (2 * j + 1) & 1);
      u32 r = phi1(aq[2 * j], aq[2 * j + 1]);
      if (j < 2) r = padlo ? 0x3F803F80u : r; else if (j >= 6) r = padhi ? 0x3F803F80u : r;
      pq[j >> 2][j & 3] = r;
      mn = pkmin(mn, r);
      PIN(mn);
      SBAR();
    }
    phi_eps(pq, mn);
    PIN(pq[0]); PIN(pq[1]);
  }
  STAMP(5);
  PIN(mail);
  STAMP(16);
#pragma nounroll
  for (int i2 = 0; i2 < SPIN_CAP && need != 0; ++i2) {
    go = need & (u32)__ballot(mail == tag); need &= ~go;
#pragma unroll
    for (int p = 1; p < 16; ++p) if (go >> p & 1) load_kv(p);
    if (need != 0) { mail = poll32(ws, mb1, lane < 16); WAITVM(0); }
    COUNT(14);
  }
#pragma unroll
  for (int k = WO_E + 14; k < WO_G; ++k) load_wo(k);
  WAITVM(WO_G - WO_E - 14);
  STAMP(18);
#pragma nounroll
  for (int it = 0; it < SPIN_CAP; ++it) {
    u32 m = 0;
#pragma unroll
    for (int p = 0; p < 16; ++p) m = sum4(m, d[p]);
    if (wave_sum(m) == 16u * tag) break;
    asm volatile("" ::: "memory");
#pragma unroll
    for (int p = 1; p < 16; ++p) load_kv(p);
    WAITVM(0);
    COUNT(11);
  }
  mail = poll32(ws, mb2, lane < 4);
  SBAR();
  {
    f32x4 s4 = __builtin_bit_cast(f32x4, d[0]);
#pragma unroll
    for (int p = 1; p < 16; ++p) s4 += __builtin_bit_cast(f32x4, d[p]);
    *(LAS u32x2*)(lds + KVI + lane * 32 + w * 8) = (u32x2){pkbf(s4.x, s4.y) & rowmask, pkbf(s4.z, s4.w) & rowmask};
  }
  STAMP(6);
  BAR();
  {
    const u32x4 a0 = *(LAS u32x4*)kvrd, a1 = *(LAS u32x4*)(kvrd + 16);
    if (!TELL2_LATE) ST1_SC1(tag, rs, tell2);
    f32x16 nm = {};
    nm = mfma32(a0, pq[0], nm); nm = mfma32(a1, pq[1], nm);
    const float den = rbf(rbf(nm[14]) + 1e-6f);
    const float rc = __builtin_amdgcn_rcpf(den);
    u32 o[8];
#pragma unroll
    for (int j = 0; j < 7; ++j) { const u32 t = pkbf(nm[2 * j], nm[2 * j + 1]); o[j] = pkbf(bflo(t) * rc, bfhi(t) * rc); }
    o[7] = (tag - ((o[0] + o[1] + o[2]) + (o[3] + o[4] + o[5]) + o[6])) & ~0x40004000u;
    __builtin_amdgcn_raw_buffer_store_b128((u32x4){o[0], o[1], o[2], o[3]}, rs, ob_dst, 0, 16);
    __builtin_amdgcn_raw_buffer_store_b128((u32x4){o[4], o[5], o[6], o[7]}, rs, ob_dst + 1024, 0, 16);
    if (TELL2_LATE) ST1_SC1(tag, rs, tell2);
    SBAR();
  }
  STAMP(7);
  auto load_o = [&](int i) {
    DMA16(rs, l16, ob_src + i * 2048, lds + OST + (8 * w + 2 * i) * 1024, 16);
    DMA16(rs, l16, ob_src + i * 2048 + 1024, lds + OST + (8 * w + 2 * i + 1) * 1024, 16);
  };
#pragma unroll
  for (int k = WO_G; k < 32; ++k) load_wo(k);
  WAITVM(35 - WO_G);
  PIN(mail);
  need = 15u;
  bool wo_done = false;
#pragma nounroll
  for (int i2 = 0; i2 < SPIN_CAP; ++i2) {
    go = need & (u32)__ballot(mail == tag); need &= ~go;
    if (WO_G < 32 && !wo_done) WAITVM(0);
#pragma unroll
    for (int i = 0; i < 4; ++i) if (go >> i & 1) load_o(i);
    mail = poll32(ws, mb2, lane < 4 && need != 0);
    if (!wo_done) {
#pragma nounroll
      for (int it = 0; it < SPIN_CAP; ++it) {
        u32 m = 0;
#pragma unroll
        for (int k = 0; k < 32; ++k) m = sum4(m, wof[k]);
        if (__all(m == 16u * tag)) break;
        asm volatile("" ::: "memory");
#pragma unroll
        for (int k = 0; k < 32; ++k) load_wo(k);
        WAITVM(0);
      }
#pragma unroll
      for (int k = 1; k < 32; k += 2) { wof[k].w = 0u; PIN(wof[k]); }
      wo_done = true;
    }
    WAITVM(0);
    COUNT(12);
    if (need == 0) break;
  }
  STAMP(8);
  f32x16 acc;
#pragma nounroll
  for (int it = 0; it < SPIN_CAP; ++it) {
    BAR();
    STAMP(9);
    u32x4 of[32];
    u32 m = 0;
#pragma unroll
    for (int k = 0; k < 6; ++k) of[k] = *(LAS u32x4*)(lds + OST + k * 1024 + l16);
    acc = (f32x16){};
    SBAR();
#pragma unroll
    for (int k = 0; k < 32; ++k) {
      acc = mfma32(wof[k], of[k], acc);
      if (k + 6 < 32) of[k + 6] = *(LAS u32x4*)(lds + OST + (k + 6) * 1024 + l16);
      m = sum4(m, of[k]);
      PIN(m);
      SBAR();
    }
    if (__all(((16u * tag - m) & 0x3FF83FFFu) == 0u)) break;
    BAR();
    load_o(0); load_o(1); load_o(2); load_o(3);
    WAITVM(0);
    COUNT(13);
  }
  STAMP(22);
  {
    LAS char* p = lds + OTR + r32 * 272 + w * 64 + half * 32;
    *(LAS u32x4*)p = (u32x4){pkbf(acc[0], acc[1]), pkbf(acc[2], acc[3]), pkbf(acc[4], acc[5]), pkbf(acc[6], acc[7])};
    *(LAS u32x4*)(p + 16) = (u32x4){pkbf(acc[8], acc[9]), pkbf(acc[10], acc[11]), pkbf(acc[12], acc[13]), pkbf(acc[14], acc[15])};
  }
  BAR();
  if (ct < 3 || (tid & 15) < 8) {
#pragma unroll
    for (int i = 0; i < 2; ++i)
      __builtin_amdgcn_raw_buffer_store_b128(*(LAS u32x4*)(lds + OTR + (16 * i + (tid >> 4)) * 272 + (tid & 15) * 16), RSRC(out, 2048 * 1792),
                                             (128 * rb + 32 * rt + 16 * i + (tid >> 4)) * 1792 + 896 + ct * 256 + (tid & 15) * 16, 0, OUTAUX);
  }
  if (b == EPW && tid == 0) __hip_atomic_store((GAS u32*)ws, tag, RLX_AGENT);
  STAMP(10);
  STAMP_FLUSH();
}

DI u32 hmul1(u32 a, u32 b) { const f16x2 x = __builtin_bit_cast(f16x2, a), y = __builtin_bit_cast(f16x2, b); return __builtin_bit_cast(u32, (f16x2){x.x, x.x} * y); }
DI u32 hmul2(u32 a, u32 b, u32 t) {
  const f16x2 x = __builtin_bit_cast(f16x2, a), y = __builtin_bit_cast(f16x2, b);
  return __builtin_bit_cast(u32, __builtin_elementwise_fma((f16x2){x.y, x.y}, (f16x2){-y.y, y.x}, __builtin_bit_cast(f16x2, t)));
}
DI u32 hmul(u32 a, u32 b) { return hmul2(a, b, hmul1(a, b)); }
DI f32x2 csq(f32x2 a) { return (f32x2){__builtin_fmaf(a.x, a.x, -a.y * a.y), (a.x + a.x) * a.y}; }
DI float exp_acc(float x) {
  const float hi = x * 1.4426950408889634f, lo = __builtin_fmaf(x, 1.9259629911266175e-8f, __builtin_fmaf(x, 1.4426950408889634f, -hi));
  const float e = __builtin_amdgcn_exp2f(hi);
  return __builtin_fmaf(e, lo * 0.6931471805599453f, e);
}
DI f32x2 rot(f32x2 b) { return (f32x2){-b.y, b.x}; }
DI f32x2 cm1(f32x2 a, f32x2 b) { return (f32x2){a.x, a.x} * b; }
DI f32x2 cm2(f32x2 a, f32x2 br, f32x2 t) { return __builtin_elementwise_fma((f32x2){a.y, a.y}, br, t); }
DI f32x2 cexp_p(float a2, float f, float p) {
  const float m = __builtin_amdgcn_exp2f(a2 * p), ph = __builtin_amdgcn_fractf(f * p);
  return (f32x2){m, m} * (f32x2){__builtin_amdgcn_cosf(ph), __builtin_amdgcn_sinf(ph)};
}
DI void role_a(LAS char* lds, const char* __restrict__ u, const char* __restrict__ Dsk, const char* __restrict__ wqkv, const char* __restrict__ wo, char* __restrict__ out, char* ws, u32 epoch,
               u32 pA, u32 pI, u32 pR, u32 pC, u32 ldt) {
  const int a = blockIdx.x, cb = a >> 5, jp = a & 31, tid = threadIdx.x, lane = tid & 63;
  const int w = __builtin_amdgcn_readfirstlane(tid >> 6);
  STAMP_DECL;
  STAMP(0);
  __builtin_amdgcn_s_setprio(APRIO);
  constexpr int SROW = 528, STG = 32 * SROW, UC = 0, UCS = 4160, RV = 36864, RVS = 4608, EX = RV + 2 * RVS, UIM = 4 * STG;
  const int PKS = UIM + 8192 + (w ? 1024 + w * 1024 : 0);
  const __amdgpu_buffer_rsrc_t rs = RSRC(ws, WS_END);
  const int ch = w >> 1, hf = w & 1, d = 2 * a + ch;
  const int r32 = lane & 31, half = lane >> 5;
  const int pkh_ = w == 0 ? (a & 15) : 2 * (a & 7) + (w - 1);
  {
    const __amdgpu_buffer_rsrc_t rp = RSRC(w == 0 ? wo : wqkv, w == 0 ? 448 * 896 : w < 3 ? 448 * 2688 : 0);
    const u32 vo = (lane >> 2) * (w == 0 ? 896 : 2688) + (lane & 3) * 16;
    const u32 so = w == 0 ? 28 * 896 * pkh_ + (a >> 4) * 64 : 16 * 2688 * (a >> 3) + ((56 * pkh_) & ~15);
    DMA16(rp, vo, so, lds + PKS, 0);
    DMA16(rp, vo, w == 0 ? so + 16 * 896 : 448 * 2688, lds + (w == 0 ? PKS + 1024 : UIM + 8192 + 5120), 0);
  }
  u32x4 dk;
  auto load_tile = [&]() {
#pragma unroll
    for (int i = 0; i < 2; ++i)
      DMA16(RSRC(u, 2048 * 896), (tid >> 3) * 896 + (((tid & 7) ^ ((tid >> 5) & 7)) << 4), (64 * jp + 32 * i) * 896 + cb * 128, lds + UIM + (32 * i + 8 * w) * 128, NTA ? 2 : 0);
    dk = *(const GAS u32x4*)(Dsk + cb * 128 + (tid & 7) * 16);
  };
  if (UEARLY == 1) load_tile();
  PRIO(ABASE);
  int tix[8];
#pragma unroll
  for (int j = 0; j < 8; ++j) {
    const int p = (int)rbf((float)(2047 - 8 * tid - j));
    const int low = p & 7, blk = p >> 8;
    const int k4 = (blk < 4 ? blk : 4) * 4;
    const int sh = (0x32210 >> k4) & 15;
    const int base = blk + ((0xCBA70 >> k4) & 15);
    tix[j] = ((base + (low >> sh)) * 32 + ((p >> 3) & 31)) * 4;
    PIN(tix[j]);
  }
  const u32 tag = take_tag(epoch);
  auto publish_weights = [&]() {
  PRIO(APRIO);
  BAR();
  if (w == 0) {
    const int q = (lane >> 2) & 3, gam = 4 * ((lane >> 4) & 1) + (lane & 3), ng = 4 * (gam & 1) + (gam >> 1);
    LAS char* p = lds + PKS + (14 * half + q) * 64 + 8 * ng;
    const u32x2 r0 = tr_read(p), r1 = tr_read(p + 256), r2 = tr_read(p + 512), r3 = tr_read(p + 768);
    const u32 off = WS_WO + ((a >> 4) * 16 + pkh_) * 2048 + lane * 16;
    ST_SC1(((u32x4){r0.x, r0.y, r1.x, r1.y}), rs, off);
    ST_SC1(((u32x4){r2.x, r2.y, r3.x, tag - ((r0.x + r0.y + r1.x) + (r1.y + r2.x + r2.y) + r3.x)}), rs, off + 1024);
  } else if (w < 3) {
    LAS char* p = lds + PKS + (8 * half + ((lane >> 2) & 3)) * 64 + ((lane >> 4) & 1) * 32 + (lane & 3) * 8;
    const u32x2 r0 = tr_read(p), r1 = tr_read(p + 256);
    u32x4 v = {r0.x, r0.y, r1.x, r1.y};
    const bool ck = lane == ((pkh_ & 1) ? 1 : 29);
    if (ck) v = (u32x4){0u, 0u, 0u, 0u};
    const u32 c = tag - wave_sum(sum4(0u, v));
    if (ck) v.x = c;
    ST_SC1(v, rs, WS_WQ + (pkh_ * 28 + (a >> 3)) * 1024 + lane * 16);
    if (lane < 16) ST1_SC1(tag, rs, WS_MBQ + (pkh_ * 16 + lane) * 128 + (a >> 3) * 4);
  }
  PRIO(ABASE);
  };
  auto publish_u = [&]() {
  PRIO(APRIO);
  BAR();
  {
    const int n = tid & 15;
    u32x4 v[2];
#pragma unroll
    for (int i = 0; i < 2; ++i) {
      const int jj = 16 * i + (tid >> 4);
      LAS char* p = lds + UIM + 4 * n * 128 + (((jj >> 2) ^ (n & 7)) << 4) + (jj & 3) * 4;
      v[i] = (u32x4){*(LAS u32*)p, *(LAS u32*)(p + 128), *(LAS u32*)(p + 256), *(LAS u32*)(p + 384)};
    }
    const u32 off = WS_UX + (cb * 32 + (tid >> 4)) * 8192 + jp * 256 + n * 16;
    ST_SC1(v[0], rs, off); ST_SC1(v[1], rs, off + 16 * 8192);
    const u32 ck = tag - row_sum2(sum4(0u, v[0]), sum4(0u, v[1]));
    __builtin_amdgcn_raw_buffer_store_b64((u32x2){tag, ck}, rs, n == 15 || n == 0 ? WS_MBU + (cb * 32 + (tid >> 4) + (n ? 0 : 16)) * 256 + jp * 8 : WS_END, 0, 16);
  }
  PRIO(ABASE);
  };
  WAITVM(UEARLY == 1 ? 5 : 2);
  PIN(pA); PIN(pI); PIN(pR); PIN(pC); PIN(ldt);
  if (UEARLY == 2) { PRIO(APRIO); load_tile(); PRIO(ABASE); }
  STAMP(1);
  const float dt = rbf(exp_acc(ch ? bfhi(ldt) : bflo(ldt)));
  const u32 mx = wave_umax(umax3(umax3(pR & 0x7fffu, (pR >> 16) & 0x7fffu, pC & 0x7fffu), (pC >> 16) & 0x7fffu, 0u));
  int ex = mx >> 7; ex = ex < 13 ? 13 : ex > 254 ? 254 : ex;
  const float sc = __uint_as_float((u32)(266 - ex) << 23), isc = __uint_as_float((u32)(ex - 12) << 23);
  LAS char* stg = lds + w * STG;
  u32x4 ud[3];
  const __amdgpu_buffer_rsrc_t ru = RSRC(ws + WS_UX + a * 8192, 8192);
  const int npc = w < 3 ? 9 : 8;
  u64 mail = 0;
  const u32 mbu = WS_MBU + a * 256 + (8 * w + lane) * 8;
  auto load_u = [&](bool on) {
    const u32 so = on ? 0u : 8192u;
    ud[0] = LDS_SC1(ru, w * 2048 + lane * 16, so); ud[1] = LDS_SC1(ru, w * 2048 + 1024 + lane * 16, so);
    ud[2] = LDS_SC1(ru, lane < 16 ? w * 2048 + 2048 + lane * 16 : 8192, so);
  };
  bool have_u = false;
  u64 mail2 = 0;
  {
    u32x2 c0, y1, y2, y4, q1, q2, q4, q8, z8, z16, z32, z64, z128;
#pragma unroll
    for (int m = 0; m < 2; ++m) {
      const float la = m ? bfhi(pA) : bflo(pA), ai = m ? bfhi(pI) : bflo(pI), cr = m ? bfhi(pR) : bflo(pR), ci = m ? bfhi(pC) : bflo(pC);
      const float a2 = -rbf(exp_acc(la)) * dt * 1.4426950408889634f, f = ai * dt * 0.15915494309189535f;
      f32x2 a = cexp_p(a2, -f, 1.f), b = cexp_p(a2, -f, 256.f), c = cexp_p(a2, f, 8.f);
      c0[m] = pkh((f32x2){cr * sc, -ci * sc});
      y1[m] = pkh(a); a = csq(a); y2[m] = pkh(a); a = csq(a); y4[m] = pkh(a);
      q1[m] = pkh(b); b = csq(b); q2[m] = pkh(b); b = csq(b); q4[m] = pkh(b); b = csq(b); q8[m] = pkh(b);
      z8[m] = pkh(c); c = csq(c); z16[m] = pkh(c); c = csq(c); z32[m] = pkh(c); c = csq(c); z64[m] = pkh(c); c = csq(c); z128[m] = pkh(c);
    }
#define OPQ(v) asm("" : "+v"(v))
    OPQ(c0); OPQ(y1); OPQ(y2); OPQ(y4); OPQ(q1); OPQ(q2); OPQ(q4); OPQ(q8); OPQ(z8); OPQ(z16); OPQ(z32); OPQ(z64); OPQ(z128);
    LAS char* sw = stg + lane * 8;
    auto put = [&](int r, u32x2 v) { *(LAS u32x2*)(sw + r * SROW) = v; };
    auto mul = [&](u32x2 x, u32x2 y) { const u32 x0 = x.x, x1 = x.y, y0 = y.x, y1_ = y.y; u32x2 r = {hmul(x0, y0), hmul(x1, y1_)}; OPQ(r); return r; };
    put(0, c0); put(21, (u32x2){0x3C00u, 0x3C00u}); put(22, z8); put(23, z16); put(25, z32); put(26, z64); put(28, z128);
    const u32x2 c2 = mul(c0, y2), c4 = mul(c0, y4), c512 = mul(c0, q2), c1024 = mul(c0, q4), z96 = mul(z32, z64);
    put(2, c2); put(4, c4); put(12, c512); put(16, c1024); put(27, z96);
    const u32x2 c6 = mul(c4, y2), c516 = mul(c4, q2), c1536 = mul(c1024, q2);
    put(6, c6); put(13, c516); put(18, c1536);
    put(1, mul(c0, y1)); put(3, mul(c2, y1)); put(5, mul(c4, y1)); put(7, mul(c6, y1));
    put(8, mul(c0, q1)); put(9, mul(c2, q1)); put(10, mul(c4, q1)); put(11, mul(c6, q1));
    put(14, mul(c512, q1)); put(15, mul(c516, q1)); put(17, mul(c1024, q1)); put(19, mul(c1536, q1)); put(20, mul(c0, q8));
    put(24, mul(z8, z16)); put(29, mul(z32, z128)); put(30, mul(z64, z128)); put(31, mul(z96, z128));
  }
  STAMP(2);
  WAITVM(0);
  if (UEARLY == 3) { PRIO(APRIO); load_tile(); }
  if (UEARLY == 1 || UEARLY == 2) publish_u();
  publish_weights();
  STAMP(13);
  {
    const int c16 = lane & 15, quad = lane >> 4;
    LAS char* py0 = stg + c16 * SROW + quad * 16;
    LAS char* py1 = stg + (16 + (c16 < 4 ? c16 : 4)) * SROW + quad * 16;
    LAS char* pt1 = stg + (21 + (c16 & 3)) * SROW + quad * 16;
    LAS char* pt2 = stg + (c16 < 4 ? 21 : 24 + (c16 >> 2)) * SROW + quad * 16;
    LAS char* pt3 = stg + (28 + (c16 >> 2)) * SROW + quad * 16;
    f32x4 ka[4] = {};
    u32x4 ft[2][3], fy[2][2], xx[2][2];
    auto rdt = [&](int s) { ft[s & 1][0] = *(LAS u32x4*)(pt1 + s * 64); ft[s & 1][1] = *(LAS u32x4*)(pt2 + s * 64); ft[s & 1][2] = *(LAS u32x4*)(pt3 + s * 64); };
    auto rdy = [&](int s) { fy[s & 1][0] = *(LAS u32x4*)(py0 + s * 64); fy[s & 1][1] = *(LAS u32x4*)(py1 + s * 64); };
    auto prod = [&](int s, int i) {
      u32 t1 = ft[s & 1][0][i], t2 = ft[s & 1][1][i], t3 = ft[s & 1][2][i];
      asm("" : "+v"(t2), "+v"(t3));
      const u32 p0 = hmul1(t1, t2), p1 = hmul1(t1, t3);
      xx[s & 1][0][i] = hmul2(t1, t2, p0); xx[s & 1][1][i] = hmul2(t1, t3, p1);
    };
    rdt(0); rdt(1); rdy(0);
    prod(0, 0); prod(0, 1); prod(0, 2); prod(0, 3);
#pragma unroll
    for (int s = 0; s < 8; ++s) {
      if (s < 6) rdt(s + 2);
      if (s < 7) rdy(s + 1);
      SBAR();
#pragma unroll
      for (int i = 0; i < 4; ++i) {
        ka[i] = mfma16h(fy[s & 1][i >> 1], xx[s & 1][i & 1], ka[i]);
        if (s < 7) prod(s + 1, i);
        SBAR();
      }
    }
    if (UEARLY == 1 || UEARLY == 2) { PRIO(APRIO); mail = poll64(ws, mbu, lane < npc); PRIO(ABASE); }
    if (UEARLY == 0) { __builtin_amdgcn_s_sleep(TDELAY); PRIO(APRIO); load_tile(); PRIO(ABASE); }
    STAMP(3);
    const float fac = 2.f * dt * isc;
    LAS float* tb = (LAS float*)stg + 4 * quad * 32 + c16;
#pragma unroll
    for (int g = 0; g < 4; ++g) { tb[g * 32] = ka[0][g] * fac; tb[g * 32 + 16] = ka[1][g] * fac; }
    if (quad < 2) {
#pragma unroll
      for (int g = 0; g < 4; ++g) { tb[(16 + g) * 32] = ka[2][g] * fac; tb[(16 + g) * 32 + 16] = ka[3][g] * fac; }
    }
  }
  STAMP(4);
  if (UEARLY == 3) { WAITVM(0); publish_u(); } else BAR();
  {
    float kk[4][8];
#pragma unroll
    for (int c = 0; c < 4; ++c)
#pragma unroll
      for (int j = 0; j < 8; ++j) kk[c][j] = *(LAS float*)(lds + c * STG + tix[j]);
    SBAR();
#pragma unroll
    for (int c = 0; c < 2; ++c) {
      u32x4 v;
#pragma unroll
      for (int j = 0; j < 4; ++j) v[j] = pkbf(kk[2 * c][2 * j] + kk[2 * c + 1][2 * j], kk[2 * c][2 * j + 1] + kk[2 * c + 1][2 * j + 1]);
      *(LAS u32x4*)(lds + RV + c * RVS + tid * 16) = v;
    }
  }
  STAMP(11);
  if (UEARLY == 1 || UEARLY == 2) {
    have_u = __popcll(__ballot((u32)mail == tag)) == npc;
    PRIO(APRIO); load_u(have_u); mail2 = poll64(ws, mbu, lane < npc && !have_u); PRIO(ABASE);
    BAR();
  } else if (UEARLY) { PRIO(APRIO); mail = poll64(ws, mbu, lane < npc); PRIO(ABASE); BAR(); } else { WAITVM(0); publish_u(); }
  STAMP(12);
  u32 sk[8];
#pragma unroll
  for (int i = 0; i < 2; ++i) {
    const u32x4 us = *(LAS u32x4*)(lds + UIM + (32 * i + (tid >> 3)) * 128 + (((tid & 7) ^ ((tid >> 5) & 7)) << 4));
#pragma unroll
    for (int k = 0; k < 4; ++k) { sk[4 * i + k] = pkbf(bflo(us[k]) * bflo(dk[k]), bfhi(us[k]) * bfhi(dk[k])); PIN(sk[4 * i + k]); }
  }
  if (!UEARLY) { PRIO(APRIO); mail = poll64(ws, mbu, lane < npc); PRIO(ABASE); }
  STAMP(14);
  if (tid < 64) *(LAS u32x4*)(lds + RV + half * RVS + 4096 + r32 * 16) = (u32x4){0u, 0u, 0u, 0u};
  SBAR();
  if ((UEARLY == 1 || UEARLY == 2) && !have_u) mail = mail2;
#pragma nounroll
  for (int it = 0; it < SPIN_CAP; ++it) {
    if (!have_u) {
#pragma nounroll
      for (int i2 = 0; i2 < SPIN_CAP && __popcll(__ballot((u32)mail == tag)) != npc; ++i2) { __builtin_amdgcn_s_sleep(ASLEEP); mail = poll64(ws, mbu, lane < npc); COUNT(14); }
      PRIO(APRIO); load_u(true); PRIO(ABASE);
    }
    if (wave_sum(sum4(sum4(sum4((u32)(mail >> 32), ud[0]), ud[1]), ud[2])) == npc * tag) break;
    have_u = false;
    asm volatile("" ::: "memory");
    COUNT(11);
  }
  STAMP(15);
#pragma unroll
  for (int i = 0; i < 2; ++i) {
    const u32 nx = __builtin_amdgcn_update_dpp(__builtin_amdgcn_readlane(ud[i + 1].x, 0), ud[i].x, 0x130, 0xf, 0xf, false);
    const u32 dw[5] = {ud[i].x, ud[i].y, ud[i].z, ud[i].w, nx};
    LAS char* p = lds + UC + (128 * w + 64 * i + lane + 4) * 8;
#pragma unroll
    for (int c = 0; c < 2; ++c) {
      u32 pr[4];
#pragma unroll
      for (int k = 0; k < 4; ++k) pr[k] = __builtin_amdgcn_perm(dw[k + 1], dw[k], c ? 0x07060302u : 0x05040100u);
      *(LAS u32x2*)(p + (4 * c) * UCS) = (u32x2){pr[0], pr[2]};
      *(LAS u32x2*)(p + (4 * c + 1) * UCS) = (u32x2){pr[1], pr[3]};
      *(LAS u32*)(p + (4 * c + 2) * UCS - 4) = pr[0]; *(LAS u32*)(p + (4 * c + 2) * UCS) = pr[2];
      *(LAS u32*)(p + (4 * c + 3) * UCS - 4) = pr[1]; *(LAS u32*)(p + (4 * c + 3) * UCS) = pr[3];
    }
  }
  if (w == 0) {
    const u32 u0 = __builtin_amdgcn_readlane(ud[0].x, 0);
    const int cs = lane >> 3, ix = lane & 7, last = (cs & 2) ? 6 : 7;
    const u32 v = ((cs & 1) && ix == last) ? (cs & 4 ? u0 & 0xffff0000u : u0 << 16) : 0u;
    if (ix <= last) *(LAS u32*)(lds + UC + cs * UCS + ix * 4) = v;
  }
  STAMP(16);
  BAR();
  STAMP(5);
  PRIO(ACONV);
  {
    const int c16 = lane & 15, hq = lane >> 4;
    LAS char* pa = lds + RV + ch * RVS + 2 * (2032 - 256 * 7 + 128 * hf - 16 * c16 + 8 * hq);
    LAS char* pb = lds + UC + (4 * ch + ((c16 + 1) & 3)) * UCS + 2 * (128 * hf + 8 * hq + 4 * ((c16 + 1) >> 2));
    f32x4 acc[8] = {};
    u32x4 fa[2][8], fb[2][8];
    auto ldf = [&](int rho, int m) {
      fa[rho & 1][m] = *(LAS u32x4*)(pa + 64 * rho + 512 * (7 - m));
      const u32x2 lo = *(volatile LAS u32x2*)(pb + 64 * rho + 512 * m), hi = *(volatile LAS u32x2*)(pb + 64 * rho + 512 * m + 8);
      fb[rho & 1][m] = (u32x4){lo.x, lo.y, hi.x, hi.y};
    };
#pragma unroll
    for (int m = 0; m < 8; ++m) ldf(0, m);
    u32 ydst = WS_YX + (cb * 32 + 8 * w + hq) * 8192 + jp * 256 + c16 * 16;
    u32 ymb = c16 == 15 || c16 == 0 ? WS_MBY + (cb * 32 + 8 * w + hq + (c16 ? 0 : 4)) * 256 + jp * 8 : WS_END;
    LAS char* exw = lds + EX + w * 8192 + lane * 16;
    LAS char* exr = lds + EX + w * 2048 + lane * 16;
    PIN(ydst); PIN(ymb); PIN(exw); PIN(exr);
#pragma unroll
    for (int rho = 0; rho < 3; ++rho) {
      SBAR();
#pragma unroll
      for (int g = 0; g < 8; ++g) {
#pragma unroll
        for (int aa = 0; aa <= g; ++aa) {
          if (aa + g < 8) acc[aa + g] = mfma16(fa[rho & 1][g], fb[rho & 1][aa], acc[aa + g]);
          if (aa < g && aa + g < 8) acc[aa + g] = mfma16(fa[rho & 1][aa], fb[rho & 1][g], acc[aa + g]);
        }
        ldf(rho + 1, g); SBAR();
      }
    }
#pragma unroll
    for (int t = 0; t < 8; ++t)
#pragma unroll
      for (int aa = 0; aa <= t; ++aa) {
        acc[t] = mfma16(fa[1][t - aa], fb[1][aa], acc[t]);
        SBAR();
#pragma unroll
        for (int f = 0; f < 7; ++f) if (t * (t + 1) / 2 + aa == f * (f + 3) / 2 + 8) { *(LAS f32x4*)(exw + f * 1024) = acc[f]; SBAR(); }
      }
    *(LAS f32x4*)(exw + 7168) = acc[7];
    STAMP(6);
    PRIO(ATAIL);
    BAR();
    STAMP(18);
    f32x4 e[8];
#pragma unroll
    for (int k = 0; k < 8; ++k) e[k] = *(LAS f32x4*)(exr + (k >> 2) * 1024 + (k & 3) * 8192);
    u32x4 v[2];
#pragma unroll
    for (int i = 0; i < 2; ++i) {
      const f32x4 y0 = e[4 * i] + e[4 * i + 1], y1 = e[4 * i + 2] + e[4 * i + 3];
      v[i] = (u32x4){pkbf(y0.x, y1.x), pkbf(y0.y, y1.y), pkbf(y0.z, y1.z), pkbf(y0.w, y1.w)};
      ST_SC1(v[i], rs, ydst + i * 32768);
    }
    const u32 ck = tag - row_sum2(sum4(0u, v[0]), sum4(0u, v[1]));
    __builtin_amdgcn_raw_buffer_store_b64((u32x2){tag, ck}, rs, ymb, 0, 16);
  }
  STAMP(7);
  {
    u32x4 y[2];
    const u32 mby = WS_MBY + a * 256 + (8 * w + lane) * 8;
    mail = poll64(ws, mby, lane < 8);
    float sf[16];
#pragma unroll
    for (int k = 0; k < 8; ++k) { sf[2 * k] = bflo(sk[k]); sf[2 * k + 1] = bfhi(sk[k]); PIN(sf[2 * k]); PIN(sf[2 * k + 1]); }
    u32 ysrc = WS_YX + a * 8192 + w * 2048 + lane * 16, odst = (64 * jp + (tid >> 3)) * 1792 + cb * 128 + (tid & 7) * 16;
    LAS char* tw = lds + (lane & 15) * 144 + (8 * w + (lane >> 4)) * 4;
    LAS char* tr = lds + (tid >> 3) * 144 + (tid & 7) * 16;
    PIN(ysrc); PIN(odst); PIN(tw); PIN(tr);
#pragma nounroll
    for (int it = 0; it < SPIN_CAP && __ballot((u32)mail == tag) != 0xffull; ++it) { mail = poll64(ws, mby, lane < 8); COUNT(13); }
    STAMP(19);
#pragma nounroll
    for (int it = 0; it < SPIN_CAP; ++it) {
      asm volatile("" ::: "memory");
      y[0] = LD_SC1(rs, ysrc); y[1] = LD_SC1(rs, ysrc + 1024);
#pragma unroll
      for (int i = 0; i < 2; ++i)
#pragma unroll
        for (int g = 0; g < 4; ++g) *(LAS u32*)(tw + g * 2304 + i * 16) = y[i][g];
      if (wave_sum(sum4(sum4((u32)(mail >> 32), y[0]), y[1])) == 8u * tag) break;
      COUNT(12);
    }
    STAMP(8);
    BAR();
#pragma unroll
    for (int i = 0; i < 2; ++i) {
      const u32x4 v = *(LAS u32x4*)(tr + i * 4608);
      u32x4 o;
#pragma unroll
      for (int k = 0; k < 4; ++k) o[k] = pkbf(bflo(v[k]) + sf[8 * i + 2 * k], bfhi(v[k]) + sf[8 * i + 2 * k + 1]);
      __builtin_amdgcn_raw_buffer_store_b128(o, RSRC(out, 2048 * 1792), odst + i * (32 * 1792), 0, OUTAUX);
    }
  }
  STAMP(9);
  STAMP_FLUSH();
}

__global__ void NOPK __launch_bounds__(256, 2)
fused_kernel(const void*, const void*, const void*, const void*, const void*, const void*, const void*, const void*, const void*,
             const void*, void*, void*) {
  __shared__ __attribute__((aligned(16))) char smem[81920];
  LAS char* lds = (LAS char*)smem;
  if ((blockIdx.x >> 5) == 7) {
    u64 pc;
    asm volatile("s_getpc_b64 %0" : "=s"(pc));
    if (threadIdx.x < CODE_LINES / 4)
      __builtin_amdgcn_global_load_lds((const GAS void*)((pc & ~127ull) + (threadIdx.x * 4 + ((blockIdx.x >> 3) & 3)) * 128), (LAS void*)lds, 4, 0, 0);
  }
  const __attribute__((address_space(4))) u64* ka = (const __attribute__((address_space(4))) u64*)__builtin_amdgcn_kernarg_segment_ptr();
  asm volatile("" : "+s"(ka));
  const char* u = (const char*)ka[0]; const GAS u32* logA = (const GAS u32*)ka[1]; const GAS u32* Aim = (const GAS u32*)ka[2];
  const GAS u32* Cre = (const GAS u32*)ka[3]; const GAS u32* Cim = (const GAS u32*)ka[4]; const GAS u32* logdt = (const GAS u32*)ka[5];
  const char* Dsk = (const char*)ka[6]; const char* x = (const char*)ka[7]; const char* wqkv = (const char*)ka[8];
  const char* wo = (const char*)ka[9]; char* out = (char*)ka[10]; char* ws = (char*)ka[11];
  asm volatile("" :: "s"(u), "s"(logA), "s"(Aim), "s"(Cre), "s"(Cim), "s"(logdt), "s"(Dsk), "s"(x), "s"(wqkv), "s"(wo), "s"(out), "s"(ws));
  const u32 epoch = request_epoch(ws);
  int bid = blockIdx.x;
  if (bid >= 256) role_b<true>(lds, x, wqkv, out, ws, epoch);
  asm volatile("" : "+s"(bid) :: "memory");
  if (bid >= 256) role_b<false>(lds, x, wqkv, out, ws, epoch);
  else if (bid < 224) {
    const int pi = bid * 256 + threadIdx.x;
    const u32 pA = __builtin_nontemporal_load(logA + pi), pI = __builtin_nontemporal_load(Aim + pi);
    const u32 pR = __builtin_nontemporal_load(Cre + pi), pC = __builtin_nontemporal_load(Cim + pi), ldt = logdt[bid];
    SBAR();
    role_a(lds, u, Dsk, wqkv, wo, out, ws, epoch, pA, pI, pR, pC, ldt);
  }
}

extern "C" void kernel_launch(void* const* d_in, const int* in_sizes, int n_in, void* d_out, int out_size, void* d_ws,
                              size_t ws_size, hipStream_t stream) {
  fused_kernel<<<512, 256, 0, stream>>>(d_in[0], d_in[1], d_in[2], d_in[3], d_in[4], d_in[5], d_in[6], d_in[7], d_in[8], d_in[9], d_out, d_ws);
}
```

```cpp
#define WPUB 0
#include <hip/hip_runtime.h>
#include <stdint.h>

typedef unsigned u32;
typedef unsigned long long u64;
typedef short s16x4 __attribute__((ext_vector_type(4)));
typedef __bf16 bf16x8 __attribute__((ext_vector_type(8)));
typedef __bf16 bf16x2 __attribute__((ext_vector_type(2)));
typedef _Float16 f16x8 __attribute__((ext_vector_type(8)));
typedef _Float16 f16x2 __attribute__((ext_vector_type(2)));
typedef float f32x16 __attribute__((ext_vector_type(16)));
typedef float f32x4 __attribute__((ext_vector_type(4)));
typedef float f32x2 __attribute__((ext_vector_type(2)));
typedef u32 u32x4 __attribute__((ext_vector_type(4)));
typedef u32 u32x2 __attribute__((ext_vector_type(2)));
#define LAS __attribute__((address_space(3)))
#define GAS __attribute__((address_space(1)))
#define DI __device__ __forceinline__
#if defined(__HIP_DEVICE_COMPILE__)
#define NOPK __attribute__((target("no-packed-fp32-ops")))
#else
#define NOPK
#endif
#define RLX_AGENT __ATOMIC_RELAXED, __HIP_MEMORY_SCOPE_AGENT

#define CODE_LINES 208
#define WO_E 4
#define ORD 4
#define BMAP 1
#define UEARLY 3
#define QT 7
#define HEADC 1
#define ATAIL 1

constexpr u32 WS_MB1 = 4096;
constexpr u32 WS_MB2 = WS_MB1 + 32768;
constexpr u32 WS_MBU = WS_MB2 + 32768;
constexpr u32 WS_MBY = WS_MBU + 57344;
constexpr u32 WS_KVP = 1u << 18;
constexpr u32 WS_OB = WS_KVP + (1u << 20);
constexpr u32 WS_UX = WS_OB + (2u << 20);
constexpr u32 WS_YX = WS_UX + (2u << 20);
constexpr u32 WS_WQ = WS_YX + (2u << 20);
constexpr u32 WS_WO = WS_WQ + (1u << 19);
constexpr u32 WS_MBQ = WS_WO + 458752;
constexpr u32 WS_END = WS_WO + (1u << 19);
constexpr u32 WS_DBG = WS_END;
constexpr int SPIN_CAP = 1 << 20;

#define STAMP(i) do {} while (0)
#define STAMP_DECL
#define COUNT(i) do {} while (0)
#define STAMP_FLUSH() do {} while (0)

DI float bflo(u32 v) { return __uint_as_float(v << 16); }
DI float bfhi(u32 v) { return __uint_as_float(v & 0xffff0000u); }
DI u32 pkbf(float a, float b) { f32x2 p = {a, b}; return __builtin_bit_cast(u32, __builtin_convertvector(p, bf16x2)); }
DI u32 pkh(f32x2 p) { return __builtin_bit_cast(u32, __builtin_convertvector(p, f16x2)); }
DI float rbf(float a) { return bflo(pkbf(a, a)); }
DI u32 umax3(u32 a, u32 b, u32 c) { u32 m = a > b ? a : b; return m > c ? m : c; }
DI u32 sum4(u32 m, u32x4 v) { return (m + v.x + v.y) + v.z + v.w; }
DI u32 row_sum(u32 v) {
  v += __builtin_amdgcn_update_dpp(0u, v, 0x111, 0xf, 0xf, true); v += __builtin_amdgcn_update_dpp(0u, v, 0x112, 0xf, 0xf, true);
  v += __builtin_amdgcn_update_dpp(0u, v, 0x114, 0xf, 0xf, true); v += __builtin_amdgcn_update_dpp(0u, v, 0x118, 0xf, 0xf, true);
  return v;
}
DI u32 row_sum2(u32 a, u32 b) {
  a += __builtin_amdgcn_update_dpp(0u, a, 0x111, 0xf, 0xf, true); b += __builtin_amdgcn_update_dpp(0u, b, 0x101, 0xf, 0xf, true);
  a += __builtin_amdgcn_update_dpp(0u, a, 0x112, 0xf, 0xf, true); b += __builtin_amdgcn_update_dpp(0u, b, 0x102, 0xf, 0xf, true);
  a += __builtin_amdgcn_update_dpp(0u, a, 0x114, 0xf, 0xf, true); b += __builtin_amdgcn_update_dpp(0u, b, 0x104, 0xf, 0xf, true);
  a += __builtin_amdgcn_update_dpp(0u, a, 0x118, 0xf, 0xf, true); b += __builtin_amdgcn_update_dpp(0u, b, 0x108, 0xf, 0xf, true);
  return (threadIdx.x & 8) ? a : b;
}
DI u32 wave_sum(u32 v) {
  v = row_sum(v);
  return (__builtin_amdgcn_readlane(v, 15) + __builtin_amdgcn_readlane(v, 31)) + (__builtin_amdgcn_readlane(v, 47) + __builtin_amdgcn_readlane(v, 63));
}
DI u32 wave_umax(u32 v) {
  u32 t;
  t = __builtin_amdgcn_update_dpp(0u, v, 0x111, 0xf, 0xf, false); v = v > t ? v : t;
  t = __builtin_amdgcn_update_dpp(0u, v, 0x112, 0xf, 0xf, false); v = v > t ? v : t;
  t = __builtin_amdgcn_update_dpp(0u, v, 0x114, 0xf, 0xf, false); v = v > t ? v : t;
  t = __builtin_amdgcn_update_dpp(0u, v, 0x118, 0xf, 0xf, false); v = v > t ? v : t;
  const u32 a = __builtin_amdgcn_readlane(v, 15), b = __builtin_amdgcn_readlane(v, 31), c = __builtin_amdgcn_readlane(v, 47), d = __builtin_amdgcn_readlane(v, 63);
  const u32 ab = a > b ? a : b, cd = c > d ? c : d;
  return ab > cd ? ab : cd;
}

DI u32 request_epoch(const char* ws) { u32 e; asm volatile("s_load_dword %0, %1, 0x0" : "=s"(e) : "s"(ws) : "memory"); return e; }
DI u32 take_tag(u32 e) { u32 r; asm volatile("s_waitcnt lgkmcnt(0)\n\ts_and_b32 %0, %1, -2" : "=s"(r) : "s"(e) : "memory", "scc"); return r * 1664525u + 2027808447u; }
DI u32 poll32(char* ws, u32 off, bool on) { u32 z = 0, r = 0; asm volatile("" : "+v"(z)); if (on) r = __hip_atomic_fetch_add((GAS u32*)(ws + off), z, RLX_AGENT); return r; }
DI u64 poll64(char* ws, u32 off, bool on) { u64 z = 0, r = 0; asm volatile("" : "+v"(z)); if (on) r = __hip_atomic_fetch_add((GAS u64*)(ws + off), z, RLX_AGENT); return r; }
#define SBAR() __builtin_amdgcn_sched_barrier(0)
#define PRIO(n) do { SBAR(); __builtin_amdgcn_s_setprio(n); SBAR(); } while (0)
#define PIN(x) asm volatile("" : "+v"(x))
DI f32x16 mfma32(u32x4 a, u32x4 b, f32x16 c) { return __builtin_amdgcn_mfma_f32_32x32x16_bf16(__builtin_bit_cast(bf16x8, a), __builtin_bit_cast(bf16x8, b), c, 0, 0, 0); }
DI f32x4 mfma16h(u32x4 a, u32x4 b, f32x4 c) { return __builtin_amdgcn_mfma_f32_16x16x32_f16(__builtin_bit_cast(f16x8, a), __builtin_bit_cast(f16x8, b), c, 0, 0, 0); }
DI f32x4 mfma16(u32x4 a, u32x4 b, f32x4 c) { return __builtin_amdgcn_mfma_f32_16x16x32_bf16(__builtin_bit_cast(bf16x8, a), __builtin_bit_cast(bf16x8, b), c, 0, 0, 0); }
DI u32x2 tr_read(LAS char* p) { return __builtin_bit_cast(u32x2, __builtin_amdgcn_ds_read_tr16_b64_v4i16((LAS s16x4*)p)); }
#define RSRC(p, n) __builtin_amdgcn_make_buffer_rsrc((void*)(p), 0, (n), 0x00020000)
#define DMA16(rs, voff, soff, dst, aux) __builtin_amdgcn_raw_ptr_buffer_load_lds((rs), (LAS void*)(dst), 16, (voff), (soff), 0, (aux))
#define WAITVM(n) do { asm volatile("" ::: "memory"); __builtin_amdgcn_s_waitcnt(0x0F70 | ((n) & 15) | (((n) >> 4) << 14)); asm volatile("" ::: "memory"); } while (0)
#define VMCNT(n) asm volatile("s_waitcnt vmcnt(" #n ")" ::: "memory")
#define BAR() do { asm volatile("s_waitcnt lgkmcnt(0)" ::: "memory"); __builtin_amdgcn_s_barrier(); asm volatile("" ::: "memory"); } while (0)
#define ST_SC1(v, rs, off) __builtin_amdgcn_raw_buffer_store_b128((v), (rs), (off), 0, 16)
#define LD_SC1(rs, off) __builtin_amdgcn_raw_buffer_load_b128((rs), (off), 0, 16)
#define LDS_SC1(rs, off, soff) __builtin_amdgcn_raw_buffer_load_b128((rs), (off), (soff), 16)
#define ST1_SC1(v, rs, off) __builtin_amdgcn_raw_buffer_store_b32((v), (rs), (off), 0, 16)

typedef unsigned short u16x2 __attribute__((ext_vector_type(2)));
DI u32 phi1(float a, float b) {
  const u32 t = pkbf(a, b);
  const f32x2 x = {bflo(t), bfhi(t)};
  const f32x2 y = x * 1.4426950408889634f;
  const float e0 = __builtin_amdgcn_fmed3f(__builtin_amdgcn_exp2f(y.x), 0.f, 1.f);
  const float e1 = __builtin_amdgcn_fmed3f(__builtin_amdgcn_exp2f(y.y), 0.f, 1.f);
  const f32x2 x1 = x + 1.0f;
  return pkbf(__builtin_fmaxf(x1.x, e0), __builtin_fmaxf(x1.y, e1));
}
DI u32 pkmin(u32 a, u32 b) { return __builtin_bit_cast(u32, __builtin_elementwise_min(__builtin_bit_cast(u16x2, a), __builtin_bit_cast(u16x2, b))); }
DI void phi_eps(u32x4 (&p)[2], u32 mn) {
  if (__any((mn & 0xffffu) < 0x3A80u || mn < 0x3A800000u)) {
#pragma unroll
    for (int j = 0; j < 8; ++j) { const u32 r = p[j >> 2][j & 3]; p[j >> 2][j & 3] = pkbf(bflo(r) + 1e-6f, bfhi(r) + 1e-6f); }
  }
}

template <bool HEAD>
DI void role_b(LAS char* lds, const char* __restrict__ x, const char* __restrict__ wqkv, char* __restrict__ out, char* ws, u32 epoch) {
  const int b = blockIdx.x - 256, tid = threadIdx.x, lane = tid & 63;
  const int w = __builtin_amdgcn_readfirstlane(tid >> 6);
  STAMP_DECL;
  STAMP(0);
  const int rb = 4 * ((b & 7) >> 1) + (b >> 6), h = (b & 1) * 8 + ((b >> 3) & 7);
  const __amdgpu_buffer_rsrc_t rs = RSRC(ws, WS_END), rx = RSRC(x, 2048 * 896), rw = RSRC(wqkv, 448 * 2688);
  constexpr int WR = 49152, KVI = WR + 4096, OST = 16384, OTR = WR;
#define XS(i) ((i) * 16384)
#define QS(cc) ((cc) < 2 ? WR + 24576 + (cc) * 4096 : (cc) < 6 ? XS(1) + ((cc) - 2) * 1024 : WR)
#define QD(cc) ((cc) >= 2 && (cc) < 6 ? 4096 : 1024)
  const int wsb = (56 * h) & ~15;
  const int coff = (h & 1) * 4;
  const int wone = (h & 1) ? 0 : 28;
  const u32 xv0 = (128 * rb + 32 * w + (lane >> 3)) * 896 + (((lane & 7) ^ (lane >> 4)) << 4);
  const u32 xv1 = (xv0 ^ 64) + 8 * 896;
  const u32 wv = (16 * w + (lane >> 2)) * 2688 + wsb + (lane & 3) * 16;
  const u32 l16 = lane * 16;
  int ch_[7];
  auto chunk = [&](int cc) { return ch_[cc]; };
  const int gi = h + rb;
  const u32 ordc = ORD == 4 ? ((gi & 2) ? ((gi & 1) ? 0x4652103u : 0x5461032u) : ((gi & 1) ? 0x4650321u : 0x6543210u))
                 : ORD == 5 ? ((gi & 2) ? ((gi & 1) ? 0x2103654u : 0x1065432u) : ((gi & 1) ? 0x3210546u : 0x6543210u))
                 : ORD == 6 ? ((gi % 3) == 0 ? 0x6543210u : (gi % 3) == 1 ? 0x6354021u : 0x6435102u)
                 : (u32)((0x65432106543210ull >> (4 * (gi % 7))) & 0xfffffff);
  auto next_chunk = [&](int cc) { int c = (ordc >> (4 * cc)) & 7; asm volatile("" : "+s"(c)); ch_[cc] = c; };
  auto issue_x = [&](int cc, int i) { DMA16(rx, (i & 1) ? xv1 : xv0, chunk(cc) * 128 + (i >> 1) * (16 * 896), lds + XS(cc % 3) + w * 4096 + i * 1024, 0); };
  auto issue_w = [&](int cc, int i) { DMA16(rw, wv, chunk(cc) * (64 * 2688) + 896 * (i + 1), lds + WR + (cc & 3) * 8192 + i * 4096 + w * 1024, 0); };
  bool qok = true;
  int spin = SPIN_CAP;
  u32 qso[7];
  auto issue_q = [&](int cc) { DMA16(rs, l16, qok ? qso[cc] : WS_END, lds + QS(cc) + w * QD(cc), 16); };
#pragma unroll
  for (int cc = 0; cc < 2; ++cc) {
    next_chunk(cc);
    if (HEAD == (cc < HEADC)) { issue_w(cc, 0); issue_w(cc, 1); issue_x(cc, 0); issue_x(cc, 1); issue_x(cc, 2); issue_x(cc, 3); }
    SBAR();
  }
  if (HEAD) return;
  next_chunk(2); next_chunk(3); next_chunk(4); next_chunk(5); next_chunk(6);
  __builtin_amdgcn_s_setprio(2);
  const int r32 = lane & 31, half = lane >> 5;
  const int xrd = w * 4096 + r32 * 128 + ((half ^ ((r32 >> 1) & 7)) << 4);
  LAS char* whi = lds + WR + (8 * half + ((lane >> 2) & 3)) * 64 + ((lane >> 4) & 1) * 32 + (lane & 3) * 8;
  PIN(whi);
  const int rt = h >> 2, ct = (h + ((h & 8) ? 2 : 1)) & 3;
  u32 wo_src = WS_WO + (4 * ct + w < 14 ? 4 * ct + w : 13) * 32768, kvp_src = WS_KVP + h * 65536 + w * 16384;
  u32 ob_dst = WS_OB + ((rb * 4 + w) * 16 + h) * 2048 + l16, ob_src = WS_OB + ((rb * 4 + rt) * 16 + 4 * w) * 2048;
  asm volatile("" : "+s"(wo_src), "+s"(kvp_src), "+s"(ob_src));
  PIN(ob_dst);
  u32 tell1 = lane < 4 ? WS_MB1 + (h * 16 + 4 * w + lane) * 128 + rb * 4 : WS_END;
  u32 tell2 = lane < 4 ? WS_MB2 + (rb * 16 + 4 * w + lane) * 128 + h * 4 : WS_END;
  u32 mb1 = WS_MB1 + (h * 16 + rb) * 128 + ((lane + rb) & 15) * 4, mb2 = WS_MB2 + (rb * 16 + h) * 128 + ((4 * w + lane) & 15) * 4;
  u32 kvp_dst = kvp_src + rb * 1024 + l16, kvs[16];
#pragma unroll
  for (int p = 1; p < 16; ++p) { kvs[p] = kvp_src + ((p + rb) & 15) * 1024; asm volatile("" : "+s"(kvs[p])); }
  const int dw = 8 * w + 4 * half - coff;
  u32 rowmask = dw < 0 || dw >= 28 ? 0u : ~0u;
  const bool padlo = (h & 1) && !half, padhi = !(h & 1) && half;
  const int oi = (r32 & 3) + 4 * (r32 >> 3);
  LAS char* kvrd = lds + KVI + ((oi < 14 ? 14 * ((r32 >> 2) & 1) + oi + coff : wone) + 32 * half) * 32;
  LAS char* qlo = lds + l16; LAS char* qhi = lds + WR + l16;
  PIN(tell1); PIN(tell2); PIN(mb1); PIN(mb2); PIN(kvp_dst); PIN(rowmask); PIN(kvrd); PIN(qhi);
  const bool ck = lane == wone + 1;
  u32 mbq = WS_MBQ + (h * 16 + rb) * 128 + (4 * (lane & 7) + w) * 4, mail = 0, tag = 0;
  PIN(mbq);
  u32x4 xf[28], wf[2][8];
  f32x16 ak = {}, av = {};
  auto rd = [&](int cc, int i) {
    if ((i & 1) == 0) xf[cc * 4 + (i >> 1)] = *(LAS u32x4*)(lds + XS(cc % 3) + (xrd ^ ((i >> 1) << 5)));
    LAS char* p = whi + (cc & 3) * 8192 + (i >> 1) * 1024 + (i & 1) * 4096;
    const u32x2 lo = tr_read(p), hi = tr_read(p + 256);
    wf[cc & 1][i] = (u32x4){lo.x, lo.y, hi.x, hi.y};
  };
#pragma unroll
  for (int cc = 0; cc < 6; ++cc) {
    if (cc == 0) VMCNT(6); else if (cc < 5) VMCNT(8); else VMCNT(7);
    BAR();
    if (cc == 0) { STAMP(1); tag = take_tag(epoch); }
    if (cc == 2) { STAMP(23); } if (cc == 4) { STAMP(24); } if (cc == 5) { STAMP(25); }
    if (cc == 5) {
#pragma unroll
      for (int i = 0; i < 8; ++i) { rd(cc, i); if (i < 7) { qso[i] = WS_WQ + (h * 28 + 4 * chunk(i) + w) * 1024; asm volatile("" : "+s"(qso[i])); } }
      SBAR();
    }
#pragma unroll
    for (int i = 0; i < 8; ++i) {
      if (cc > 0) {
        if (i & 1) av = mfma32(xf[(cc - 1) * 4 + (i >> 1)], wf[(cc - 1) & 1][i], av);
        else ak = mfma32(xf[(cc - 1) * 4 + (i >> 1)], wf[(cc - 1) & 1][i], ak);
      }
      if (cc < 5) rd(cc, i);
      if (cc == 0) { if (i < 2) issue_w(2, i); else if (i < 6) issue_x(2, i - 2); else issue_w(3, i - 6); }
      else if (cc <= 4 && i < 4) issue_x(cc + 2, i);
      else if (cc <= 3 && i < 6) issue_w(cc + 3, i - 4);
      else if (cc == 4 && i == 4) mail = poll32(ws, mbq, lane < 7);
      SBAR();
    }
#pragma unroll
    for (int s = 3; s >= 0; --s) { PIN(wf[cc & 1][2 * s + 1]); PIN(wf[cc & 1][2 * s]); PIN(xf[cc * 4 + s]); }
    if (cc > 0) { PIN(ak); PIN(av); }
  }
  VMCNT(1);
  BAR();
  STAMP(26);
  rd(6, 0); rd(6, 2); rd(6, 4); rd(6, 6);
  SBAR();
#pragma unroll
  for (int s = 0; s < 4; ++s) ak = mfma32(xf[20 + s], wf[1][2 * s], ak);
  PIN(ak);
#pragma unroll
  for (int s = 3; s >= 0; --s) { PIN(wf[0][2 * s]); PIN(xf[24 + s]); }
  rd(6, 1); rd(6, 3); rd(6, 5); rd(6, 7);
  SBAR();
#pragma unroll
  for (int s = 0; s < 4; ++s) ak = mfma32(xf[24 + s], wf[0][2 * s], ak);
  PIN(ak);
  SBAR();
  WAITVM(0);
  PIN(mail);
  qok = __ballot(mail == tag) == 0x7full;
  STAMP(2);
  u32x4 wof[32], d[16], pq[2];
  auto load_wo = [&](int k) { wof[k] = __builtin_amdgcn_raw_buffer_load_b128(rs, l16 + (k & 3) * 1024, wo_src + (k >> 2) * 4096, 16); };
  {
    u32x4 pk[2], pv[2];
    u32 mn = ~0u;
#pragma unroll
    for (int j = 0; j < 8; j += 2) {
      av = mfma32(xf[20 + j], wf[j < 4][2 * (j & 3) + 1], av);
      issue_q(j); if (j < 6) issue_q(j + 1);
      SBAR();
      pk[j >> 2][j & 3] = phi1(ak[2 * j], ak[2 * j + 1]);
      av = mfma32(xf[21 + j], wf[j < 4][2 * (j & 3) + 3], av);
      pk[j >> 2][(j & 3) + 1] = phi1(ak[2 * j + 2], ak[2 * j + 3]);
      mn = pkmin(pkmin(mn, pk[j >> 2][j & 3]), pk[j >> 2][(j & 3) + 1]);
      PIN(mn); PIN(av);
      SBAR();
    }
    phi_eps(pk, mn);
    STAMP(11);
#pragma unroll
    for (int j = 0; j < 8; ++j) { const u32 v = pkbf(av[2 * j], av[2 * j + 1]); pv[j >> 2][j & 3] = r32 == wone ? 0x3F803F80u : v; }
    f32x16 kv = {};
    kv = mfma32(pk[0], pv[0], kv); kv = mfma32(pk[1], pv[1], kv);
    ST1_SC1(tag, rs, tell1);
#pragma unroll
    for (int g = 0; g < 4; ++g) *(LAS f32x4*)(lds + XS(2) + w * 4096 + g * 1024 + l16) = (f32x4){kv[4 * g], kv[4 * g + 1], kv[4 * g + 2], kv[4 * g + 3]};
    STAMP(12);
    BAR();
    f32x4 s4 = *(LAS f32x4*)(lds + XS(2) + tid * 16);
#pragma unroll
    for (int ww = 1; ww < 4; ++ww) s4 += *(LAS f32x4*)(lds + XS(2) + ww * 4096 + tid * 16);
    u32x4 v = __builtin_bit_cast(u32x4, s4);
    const u32 part = v.y + v.z + v.w;
    const u32 c = tag - wave_sum(ck ? part : part + v.x);
    if (ck) v.x = c;
    ST_SC1(v, rs, kvp_dst);
    d[0] = v;
  }
  if (!qok) {
#pragma nounroll
    while (__ballot(poll32(ws, mbq, lane < 7) == tag) != 0x7full && --spin > 0) {}
    qok = true;
#pragma unroll
    for (int cc = 0; cc < 7; ++cc) issue_q(cc);
    WAITVM(0);
    COUNT(13);
  }
  u32 go, need = 0xfffeu;
  mail = poll32(ws, mb1, lane < 16);
#pragma unroll
  for (int k = 0; k < WO_E; ++k) load_wo(k);
  WAITVM(WO_E + 3);
  BAR();
  STAMP(3);
#define QP(cc) (QS(cc) >= WR ? qhi + (QS(cc) - WR) : qlo + QS(cc))
  auto load_kv = [&](int p, bool on = true) { d[p] = __builtin_amdgcn_raw_buffer_load_b128(rs, l16, on ? kvs[p] : WS_END, 16); };
  {
    f32x16 aq = {};
    u32x4 qf[2][4];
    u32 qsum = 0;
    auto read_q = [&](int cc) {
#pragma unroll
      for (int s = 0; s < 4; ++s) qf[cc & 1][s] = *(LAS u32x4*)(QP(cc) + s * QD(cc));
      SBAR();
    };
    read_q(0);
#pragma unroll
    for (int cc = 0; cc < 7; ++cc) {
      if (cc < 6) read_q(cc + 1);
#pragma unroll
      for (int s = 0; s < 4; ++s) {
        aq = mfma32(qf[cc & 1][s], xf[cc * 4 + s], aq);
        qsum = sum4(qsum, qf[cc & 1][s]);
        if (s & 1) load_wo(WO_E + 2 * cc + (s >> 1));
        PIN(aq); PIN(qsum);
        SBAR();
      }
    }
    if (wave_sum(qsum) != 28u * tag) {
      aq = (f32x16){};
#pragma nounroll
      for (int k2 = 0; k2 < 28; ++k2) {
        const int ks = 4 * ((ordc >> (k2 & 28)) & 7) + (k2 & 3);
        u32x4 f;
#pragma nounroll
        for (;;) {
          asm volatile("" ::: "memory");
          f = LDS_SC1(rs, l16, WS_WQ + (h * 28 + ks) * 1024);
          if (wave_sum(sum4(0u, f)) == tag || --spin <= 0) break;
        }
        aq = mfma32(f, __builtin_amdgcn_raw_buffer_load_b128(rx, (128 * rb + 32 * w + r32) * 896 + half * 16, ks * 32, 0), aq);
      }
      COUNT(15);
    }
    STAMP(14);
    go = need & (u32)__ballot(mail == tag); need &= ~go;
    mail = poll32(ws, mb1, lane < 16 && need != 0);
    STAMP(15);
    u32 mn = ~0u;
#pragma unroll
    for (int j = 0; j < 8; ++j) {
      if (j) load_kv(2 * j, go >> (2 * j) & 1);
      load_kv(2 * j + 1, go >> (2 * j + 1) & 1);
      u32 r = phi1(aq[2 * j], aq[2 * j + 1]);
      if (j < 2) r = padlo ? 0x3F803F80u : r; else if (j >= 6) r = padhi ? 0x3F803F80u : r;
      pq[j >> 2][j & 3] = r;
      mn = pkmin(mn, r);
      PIN(mn);
      SBAR();
    }
    phi_eps(pq, mn);
    PIN(pq[0]); PIN(pq[1]);
  }
  STAMP(5);
  PIN(mail);
  STAMP(16);
#pragma nounroll
  while (need != 0 && --spin > 0) {
    go = need & (u32)__ballot(mail == tag); need &= ~go;
#pragma unroll
    for (int p = 1; p < 16; ++p) if (go >> p & 1) load_kv(p);
    if (need != 0) { mail = poll32(ws, mb1, lane < 16); WAITVM(0); }
    COUNT(14);
  }
#pragma unroll
  for (int k = WO_E + 14; k < 32; ++k) load_wo(k);
  WAITVM(18 - WO_E);
  STAMP(18);
#pragma nounroll
  for (;;) {
    u32 m = 0;
#pragma unroll
    for (int p = 0; p < 16; ++p) m = sum4(m, d[p]);
    if (wave_sum(m) == 16u * tag || --spin <= 0) break;
    asm volatile("" ::: "memory");
#pragma unroll
    for (int p = 1; p < 16; ++p) load_kv(p);
    WAITVM(0);
    COUNT(11);
  }
  mail = poll32(ws, mb2, lane < 4);
  SBAR();
  {
    f32x4 s4 = __builtin_bit_cast(f32x4, d[0]);
#pragma unroll
    for (int p = 1; p < 16; ++p) s4 += __builtin_bit_cast(f32x4, d[p]);
    *(LAS u32x2*)(lds + KVI + lane * 32 + w * 8) = (u32x2){pkbf(s4.x, s4.y) & rowmask, pkbf(s4.z, s4.w) & rowmask};
  }
  STAMP(6);
  BAR();
  {
    const u32x4 a0 = *(LAS u32x4*)kvrd, a1 = *(LAS u32x4*)(kvrd + 16);
    f32x16 nm = {};
    nm = mfma32(a0, pq[0], nm); nm = mfma32(a1, pq[1], nm);
    const float den = rbf(rbf(nm[14]) + 1e-6f);
    const float rc = __builtin_amdgcn_rcpf(den);
    u32 o[8];
#pragma unroll
    for (int j = 0; j < 7; ++j) { const u32 t = pkbf(nm[2 * j], nm[2 * j + 1]); o[j] = pkbf(bflo(t) * rc, bfhi(t) * rc); }
    o[7] = (tag - ((o[0] + o[1] + o[2]) + (o[3] + o[4] + o[5]) + o[6])) & ~0x40004000u;
    __builtin_amdgcn_raw_buffer_store_b128((u32x4){o[0], o[1], o[2], o[3]}, rs, ob_dst, 0, 16);
    __builtin_amdgcn_raw_buffer_store_b128((u32x4){o[4], o[5], o[6], o[7]}, rs, ob_dst + 1024, 0, 16);
    ST1_SC1(tag, rs, tell2);
    SBAR();
  }
  STAMP(7);
  auto load_o = [&](int i) {
    DMA16(rs, l16, ob_src + i * 2048, lds + OST + (8 * w + 2 * i) * 1024, 16);
    DMA16(rs, l16, ob_src + i * 2048 + 1024, lds + OST + (8 * w + 2 * i + 1) * 1024, 16);
  };
  WAITVM(3);
  PIN(mail);
  need = 15u;
  bool wo_done = false;
#pragma nounroll
  for (;;) {
    go = need & (u32)__ballot(mail == tag); need &= ~go;
#pragma unroll
    for (int i = 0; i < 4; ++i) if (go >> i & 1) load_o(i);
    mail = poll32(ws, mb2, lane < 4 && need != 0);
    if (!wo_done) {
#pragma nounroll
      for (;;) {
        u32 m = 0;
#pragma unroll
        for (int k = 0; k < 32; ++k) m = sum4(m, wof[k]);
        if (__all(m == 16u * tag) || --spin <= 0) break;
        asm volatile("" ::: "memory");
#pragma unroll
        for (int k = 0; k < 32; ++k) load_wo(k);
        WAITVM(0);
      }
#pragma unroll
      for (int k = 1; k < 32; k += 2) { wof[k].w = 0u; PIN(wof[k]); }
      wo_done = true;
    }
    WAITVM(0);
    COUNT(12);
    if (need == 0 || --spin <= 0) break;
  }
  STAMP(8);
  f32x16 acc;
#pragma nounroll
  for (int it = 0; it < 4096; ++it) {
    BAR();
    STAMP(9);
    u32x4 of[32];
    u32 m = 0;
#pragma unroll
    for (int k = 0; k < 6; ++k) of[k] = *(LAS u32x4*)(lds + OST + k * 1024 + l16);
    acc = (f32x16){};
    SBAR();
#pragma unroll
    for (int k = 0; k < 32; ++k) {
      acc = mfma32(wof[k], of[k], acc);
      if (k + 6 < 32) of[k + 6] = *(LAS u32x4*)(lds + OST + (k + 6) * 1024 + l16);
      m = sum4(m, of[k]);
      PIN(m);
      SBAR();
    }
    if (__all(((16u * tag - m) & 0x3FF83FFFu) == 0u)) break;
    BAR();
    load_o(0); load_o(1); load_o(2); load_o(3);
    WAITVM(0);
    COUNT(13);
  }
  STAMP(22);
  {
    LAS char* p = lds + OTR + r32 * 272 + w * 64 + half * 32;
    *(LAS u32x4*)p = (u32x4){pkbf(acc[0], acc[1]), pkbf(acc[2], acc[3]), pkbf(acc[4], acc[5]), pkbf(acc[6], acc[7])};
    *(LAS u32x4*)(p + 16) = (u32x4){pkbf(acc[8], acc[9]), pkbf(acc[10], acc[11]), pkbf(acc[12], acc[13]), pkbf(acc[14], acc[15])};
  }
  BAR();
  if (ct < 3 || (tid & 15) < 8) {
#pragma unroll
    for (int i = 0; i < 2; ++i)
      __builtin_amdgcn_raw_buffer_store_b128(*(LAS u32x4*)(lds + OTR + (16 * i + (tid >> 4)) * 272 + (tid & 15) * 16), RSRC(out, 2048 * 1792),
                                             (128 * rb + 32 * rt + 16 * i + (tid >> 4)) * 1792 + 896 + ct * 256 + (tid & 15) * 16, 0, 16);
  }
  if (b == 255 && tid == 0) __hip_atomic_store((GAS u32*)ws, tag, RLX_AGENT);
  STAMP(10);
  STAMP_FLUSH();
}

DI u32 hmul1(u32 a, u32 b) { const f16x2 x = __builtin_bit_cast(f16x2, a), y = __builtin_bit_cast(f16x2, b); return __builtin_bit_cast(u32, (f16x2){x.x, x.x} * y); }
DI u32 hmul2(u32 a, u32 b, u32 t) {
  const f16x2 x = __builtin_bit_cast(f16x2, a), y = __builtin_bit_cast(f16x2, b);
  return __builtin_bit_cast(u32, __builtin_elementwise_fma((f16x2){x.y, x.y}, (f16x2){-y.y, y.x}, __builtin_bit_cast(f16x2, t)));
}
DI u32 hmul(u32 a, u32 b) { return hmul2(a, b, hmul1(a, b)); }
DI f32x2 csq(f32x2 a) { return (f32x2){__builtin_fmaf(a.x, a.x, -a.y * a.y), (a.x + a.x) * a.y}; }
DI float exp_acc(float x) {
  const float hi = x * 1.4426950408889634f, lo = __builtin_fmaf(x, 1.9259629911266175e-8f, __builtin_fmaf(x, 1.4426950408889634f, -hi));
  const float e = __builtin_amdgcn_exp2f(hi);
  return __builtin_fmaf(e, lo * 0.6931471805599453f, e);
}
DI f32x2 cexp_p(float a2, float f, float p) {
  const float m = __builtin_amdgcn_exp2f(a2 * p), ph = __builtin_amdgcn_fractf(f * p);
  return (f32x2){m, m} * (f32x2){__builtin_amdgcn_cosf(ph), __builtin_amdgcn_sinf(ph)};
}
DI void role_a(LAS char* lds, const char* __restrict__ u, const char* __restrict__ Dsk, const char* __restrict__ wqkv, const char* __restrict__ wo, char* __restrict__ out, char* ws, u32 epoch,
               u32 pA, u32 pI, u32 pR, u32 pC, u32 ldt) {
  const int a = blockIdx.x, cb = a >> 5, jp = a & 31, tid = threadIdx.x, lane = tid & 63;
  const int w = __builtin_amdgcn_readfirstlane(tid >> 6);
  STAMP_DECL;
  STAMP(0);
  __builtin_amdgcn_s_setprio(3);
  constexpr int SROW = 528, STG = 32 * SROW, UC = 0, UCS = 4160, RV = 36864, RVS = 4608, EX = RV + 2 * RVS, UIM = 4 * STG;
  const int PKS = UIM + 8192 + (w ? 1024 + w * 1024 : 0);
  const __amdgpu_buffer_rsrc_t rs = RSRC(ws, WS_END);
  const int ch = w >> 1, hf = w & 1;
  int spin = SPIN_CAP;
  const int r32 = lane & 31, half = lane >> 5;
  const int pkh_ = w == 0 ? (a & 15) : 2 * (a & 7) + (w - 1);
  {
    const __amdgpu_buffer_rsrc_t rp = RSRC(w == 0 ? wo : wqkv, w == 0 ? 448 * 896 : w < 3 ? 448 * 2688 : 0);
    const u32 vo = (lane >> 2) * (w == 0 ? 896 : 2688) + (lane & 3) * 16;
    const u32 so = w == 0 ? 28 * 896 * pkh_ + (a >> 4) * 64 : 16 * 2688 * (a >> 3) + ((56 * pkh_) & ~15);
    DMA16(rp, vo, so, lds + PKS, 0);
    DMA16(rp, vo, w == 0 ? so + 16 * 896 : 448 * 2688, lds + (w == 0 ? PKS + 1024 : UIM + 8192 + 5120), 0);
  }
  u32x4 dk;
  auto load_tile = [&]() {
#pragma unroll
    for (int i = 0; i < 2; ++i)
      DMA16(RSRC(u, 2048 * 896), (tid >> 3) * 896 + (((tid & 7) ^ ((tid >> 5) & 7)) << 4), (64 * jp + 32 * i) * 896 + cb * 128, lds + UIM + (32 * i + 8 * w) * 128, 2);
    dk = *(const GAS u32x4*)(Dsk + cb * 128 + (tid & 7) * 16);
  };
  PRIO(0);
  int tix[8];
#pragma unroll
  for (int j = 0; j < 8; ++j) {
    const int p = (int)rbf((float)(2047 - 8 * tid - j));
    const int low = p & 7, blk = p >> 8;
    const int k4 = (blk < 4 ? blk : 4) * 4;
    const int sh = (0x32210 >> k4) & 15;
    const int base = blk + ((0xCBA70 >> k4) & 15);
    tix[j] = ((base + (low >> sh)) * 32 + ((p >> 3) & 31)) * 4;
    PIN(tix[j]);
  }
  const u32 tag = take_tag(epoch);
  auto publish_weights = [&]() {
  PRIO(3);
  BAR();
  if (w == 0) {
    const int q = (lane >> 2) & 3, gam = 4 * ((lane >> 4) & 1) + (lane & 3), ng = 4 * (gam & 1) + (gam >> 1);
    LAS char* p = lds + PKS + (14 * half + q) * 64 + 8 * ng;
    const u32x2 r0 = tr_read(p), r1 = tr_read(p + 256), r2 = tr_read(p + 512), r3 = tr_read(p + 768);
    const u32 off = WS_WO + ((a >> 4) * 16 + pkh_) * 2048 + lane * 16;
    ST_SC1(((u32x4){r0.x, r0.y, r1.x, r1.y}), rs, off);
    ST_SC1(((u32x4){r2.x, r2.y, r3.x, tag - ((r0.x + r0.y + r1.x) + (r1.y + r2.x + r2.y) + r3.x)}), rs, off + 1024);
  } else if (w < 3) {
    LAS char* p = lds + PKS + (8 * half + ((lane >> 2) & 3)) * 64 + ((lane >> 4) & 1) * 32 + (lane & 3) * 8;
    const u32x2 r0 = tr_read(p), r1 = tr_read(p + 256);
    u32x4 v = {r0.x, r0.y, r1.x, r1.y};
    const bool ck = lane == ((pkh_ & 1) ? 1 : 29);
    if (ck) v = (u32x4){0u, 0u, 0u, 0u};
    const u32 c = tag - wave_sum(sum4(0u, v));
    if (ck) v.x = c;
    ST_SC1(v, rs, WS_WQ + (pkh_ * 28 + (a >> 3)) * 1024 + lane * 16);
  }
  PRIO(0);
  };
  auto publish_u = [&]() {
  PRIO(3);
  BAR();
  {
    const int n = tid & 15;
    u32x4 v[2];
#pragma unroll
    for (int i = 0; i < 2; ++i) {
      const int jj = 16 * i + (tid >> 4);
      LAS char* p = lds + UIM + 4 * n * 128 + (((jj >> 2) ^ (n & 7)) << 4) + (jj & 3) * 4;
      v[i] = (u32x4){*(LAS u32*)p, *(LAS u32*)(p + 128), *(LAS u32*)(p + 256), *(LAS u32*)(p + 384)};
    }
    const u32 off = WS_UX + (cb * 32 + (tid >> 4)) * 8192 + jp * 256 + n * 16;
    ST_SC1(v[0], rs, off); ST_SC1(v[1], rs, off + 16 * 8192);
    const u32 ck = tag - row_sum2(sum4(0u, v[0]), sum4(0u, v[1]));
    __builtin_amdgcn_raw_buffer_store_b64((u32x2){tag, ck}, rs, n == 15 || n == 0 ? WS_MBU + (cb * 32 + (tid >> 4) + (n ? 0 : 16)) * 256 + jp * 8 : WS_END, 0, 16);
  }
  PRIO(0);
  };
  if (WPUB == 0) { WAITVM(0); publish_weights(); } else WAITVM(2);
  PIN(pA); PIN(pI); PIN(pR); PIN(pC); PIN(ldt);
  STAMP(1);
  const float dt = rbf(exp_acc(ch ? bfhi(ldt) : bflo(ldt)));
  const u32 mx = wave_umax(umax3(umax3(pR & 0x7fffu, (pR >> 16) & 0x7fffu, pC & 0x7fffu), (pC >> 16) & 0x7fffu, 0u));
  int ex = mx >> 7; ex = ex < 13 ? 13 : ex > 254 ? 254 : ex;
  const float sc = __uint_as_float((u32)(266 - ex) << 23), isc = __uint_as_float((u32)(ex - 12) << 23);
  LAS char* stg = lds + w * STG;
  u32x4 ud[3];
  const __amdgpu_buffer_rsrc_t ru = RSRC(ws + WS_UX + a * 8192, 8192);
  const int npc = w < 3 ? 9 : 8;
  u64 mail = 0;
  const u32 mbu = WS_MBU + a * 256 + (8 * w + lane) * 8;
  {
    u32x2 c0, y1, y2, y4, q1, q2, q4, q8, z8, z16, z32, z64, z128;
#pragma unroll
    for (int m = 0; m < 2; ++m) {
      const float la = m ? bfhi(pA) : bflo(pA), ai = m ? bfhi(pI) : bflo(pI), cr = m ? bfhi(pR) : bflo(pR), ci = m ? bfhi(pC) : bflo(pC);
      const float a2 = -rbf(exp_acc(la)) * dt * 1.4426950408889634f, f = ai * dt * 0.15915494309189535f;
      f32x2 a = cexp_p(a2, -f, 1.f), b = cexp_p(a2, -f, 256.f), c = cexp_p(a2, f, 8.f);
      c0[m] = pkh((f32x2){cr * sc, -ci * sc});
      y1[m] = pkh(a); a = csq(a); y2[m] = pkh(a); a = csq(a); y4[m] = pkh(a);
      q1[m] = pkh(b); b = csq(b); q2[m] = pkh(b); b = csq(b); q4[m] = pkh(b); b = csq(b); q8[m] = pkh(b);
      z8[m] = pkh(c); c = csq(c); z16[m] = pkh(c); c = csq(c); z32[m] = pkh(c); c = csq(c); z64[m] = pkh(c); c = csq(c); z128[m] = pkh(c);
    }
#define OPQ(v) asm("" : "+v"(v))
    if (WPUB == 1) { PIN(c0); PIN(y4); PIN(q8); PIN(z128); WAITVM(0); publish_weights(); }
    OPQ(c0); OPQ(y1); OPQ(y2); OPQ(y4); OPQ(q1); OPQ(q2); OPQ(q4); OPQ(q8); OPQ(z8); OPQ(z16); OPQ(z32); OPQ(z64); OPQ(z128);
    LAS char* sw = stg + lane * 8;
    auto put = [&](int r, u32x2 v) { *(LAS u32x2*)(sw + r * SROW) = v; };
    auto mul = [&](u32x2 x, u32x2 y) { const u32 x0 = x.x, x1 = x.y, y0 = y.x, y1_ = y.y; u32x2 r = {hmul(x0, y0), hmul(x1, y1_)}; OPQ(r); return r; };
    put(0, c0); put(21, (u32x2){0x3C00u, 0x3C00u}); put(22, z8); put(23, z16); put(25, z32); put(26, z64); put(28, z128);
    const u32x2 c2 = mul(c0, y2), c4 = mul(c0, y4), c512 = mul(c0, q2), c1024 = mul(c0, q4), z96 = mul(z32, z64);
    put(2, c2); put(4, c4); put(12, c512); put(16, c1024); put(27, z96);
    const u32x2 c6 = mul(c4, y2), c516 = mul(c4, q2), c1536 = mul(c1024, q2);
    put(6, c6); put(13, c516); put(18, c1536);
    put(1, mul(c0, y1)); put(3, mul(c2, y1)); put(5, mul(c4, y1)); put(7, mul(c6, y1));
    put(8, mul(c0, q1)); put(9, mul(c2, q1)); put(10, mul(c4, q1)); put(11, mul(c6, q1));
    put(14, mul(c512, q1)); put(15, mul(c516, q1)); put(17, mul(c1024, q1)); put(19, mul(c1536, q1)); put(20, mul(c0, q8));
    put(24, mul(z8, z16)); put(29, mul(z32, z128)); put(30, mul(z64, z128)); put(31, mul(z96, z128));
  }
  STAMP(2);
  if (WPUB < 2) {
    PRIO(3);
    if (UEARLY == 3) { load_tile(); WAITVM(3); } else WAITVM(0);
    ST1_SC1(tag, rs, lane < 16 && (w == 1 || w == 2) ? WS_MBQ + (pkh_ * 16 + lane) * 128 + (a >> 3) * 4 : WS_END);
    PRIO(0);
  } else {
    WAITVM(0);
    if (UEARLY == 3) { PRIO(3); load_tile(); }
    publish_weights();
  }
  STAMP(13);
  {
    const int c16 = lane & 15, quad = lane >> 4;
    LAS char* py0 = stg + c16 * SROW + quad * 16;
    LAS char* py1 = stg + (16 + (c16 < 4 ? c16 : 4)) * SROW + quad * 16;
    LAS char* pt1 = stg + (21 + (c16 & 3)) * SROW + quad * 16;
    LAS char* pt2 = stg + (c16 < 4 ? 21 : 24 + (c16 >> 2)) * SROW + quad * 16;
    LAS char* pt3 = stg + (28 + (c16 >> 2)) * SROW + quad * 16;
    f32x4 ka[4] = {};
    u32x4 ft[2][3], fy[2][2], xx[2][2];
    auto rdt = [&](int s) { ft[s & 1][0] = *(LAS u32x4*)(pt1 + s * 64); ft[s & 1][1] = *(LAS u32x4*)(pt2 + s * 64); ft[s & 1][2] = *(LAS u32x4*)(pt3 + s * 64); };
    auto rdy = [&](int s) { fy[s & 1][0] = *(LAS u32x4*)(py0 + s * 64); fy[s & 1][1] = *(LAS u32x4*)(py1 + s * 64); };
    auto prod = [&](int s, int i) {
      u32 t1 = ft[s & 1][0][i], t2 = ft[s & 1][1][i], t3 = ft[s & 1][2][i];
      asm("" : "+v"(t2), "+v"(t3));
      const u32 p0 = hmul1(t1, t2), p1 = hmul1(t1, t3);
      xx[s & 1][0][i] = hmul2(t1, t2, p0); xx[s & 1][1][i] = hmul2(t1, t3, p1);
    };
    rdt(0); rdt(1); rdy(0);
    prod(0, 0); prod(0, 1); prod(0, 2); prod(0, 3);
#pragma unroll
    for (int s = 0; s < 8; ++s) {
      if (WPUB == 2 && s == QT) {
        PRIO(3); WAITVM(0);
        ST1_SC1(tag, rs, lane < 16 && (w == 1 || w == 2) ? WS_MBQ + (pkh_ * 16 + lane) * 128 + (a >> 3) * 4 : WS_END);
        PRIO(0);
      }
      if (s < 6) rdt(s + 2);
      if (s < 7) rdy(s + 1);
      SBAR();
#pragma unroll
      for (int i = 0; i < 4; ++i) {
        ka[i] = mfma16h(fy[s & 1][i >> 1], xx[s & 1][i & 1], ka[i]);
        if (s < 7) prod(s + 1, i);
        SBAR();
      }
    }
    if (UEARLY == 0) { PRIO(3); load_tile(); PRIO(0); }
    STAMP(3);
    const float fac = 2.f * dt * isc;
    LAS float* tb = (LAS float*)stg + 4 * quad * 32 + c16;
#pragma unroll
    for (int g = 0; g < 4; ++g) { tb[g * 32] = ka[0][g] * fac; tb[g * 32 + 16] = ka[1][g] * fac; }
    if (quad < 2) {
#pragma unroll
      for (int g = 0; g < 4; ++g) { tb[(16 + g) * 32] = ka[2][g] * fac; tb[(16 + g) * 32 + 16] = ka[3][g] * fac; }
    }
  }
  STAMP(4);
  if (UEARLY == 3) { if (WPUB < 2) WAITVM(1); publish_u(); } else BAR();
  {
    float kk[4][8];
#pragma unroll
    for (int c = 0; c < 4; ++c)
#pragma unroll
      for (int j = 0; j < 8; ++j) kk[c][j] = *(LAS float*)(lds + c * STG + tix[j]);
    SBAR();
#pragma unroll
    for (int c = 0; c < 2; ++c) {
      u32x4 v;
#pragma unroll
      for (int j = 0; j < 4; ++j) v[j] = pkbf(kk[2 * c][2 * j] + kk[2 * c + 1][2 * j], kk[2 * c][2 * j + 1] + kk[2 * c + 1][2 * j + 1]);
      *(LAS u32x4*)(lds + RV + c * RVS + tid * 16) = v;
    }
  }
  STAMP(11);
  if (UEARLY) { PRIO(3); mail = poll64(ws, mbu, lane < npc); PRIO(0); BAR(); } else { WAITVM(0); publish_u(); }
  STAMP(12);
  u32 sk[8];
#pragma unroll
  for (int i = 0; i < 2; ++i) {
    const u32x4 us = *(LAS u32x4*)(lds + UIM + (32 * i + (tid >> 3)) * 128 + (((tid & 7) ^ ((tid >> 5) & 7)) << 4));
#pragma unroll
    for (int k = 0; k < 4; ++k) { sk[4 * i + k] = pkbf(bflo(us[k]) * bflo(dk[k]), bfhi(us[k]) * bfhi(dk[k])); PIN(sk[4 * i + k]); }
  }
  if (!UEARLY) { PRIO(3); mail = poll64(ws, mbu, lane < npc); PRIO(0); }
  STAMP(14);
  if (tid < 64) *(LAS u32x4*)(lds + RV + half * RVS + 4096 + r32 * 16) = (u32x4){0u, 0u, 0u, 0u};
  SBAR();
  PRIO(3);
#pragma nounroll
  for (;;) {
#pragma nounroll
    while (__popcll(__ballot((u32)mail == tag)) != npc && --spin > 0) { mail = poll64(ws, mbu, lane < npc); COUNT(14); }
    ud[0] = LD_SC1(ru, w * 2048 + lane * 16); ud[1] = LD_SC1(ru, w * 2048 + 1024 + lane * 16);
    ud[2] = LD_SC1(ru, lane < 16 ? w * 2048 + 2048 + lane * 16 : 8192);
    if (wave_sum(sum4(sum4(sum4((u32)(mail >> 32), ud[0]), ud[1]), ud[2])) == npc * tag || --spin <= 0) break;
    mail = 0;
    COUNT(11);
  }
  PRIO(0);
  STAMP(15);
#pragma unroll
  for (int i = 0; i < 2; ++i) {
    const u32 nx = __builtin_amdgcn_update_dpp(__builtin_amdgcn_readlane(ud[i + 1].x, 0), ud[i].x, 0x130, 0xf, 0xf, false);
    const u32 dw[5] = {ud[i].x, ud[i].y, ud[i].z, ud[i].w, nx};
    LAS char* p = lds + UC + (128 * w + 64 * i + lane + 4) * 8;
#pragma unroll
    for (int c = 0; c < 2; ++c) {
      u32 pr[4];
#pragma unroll
      for (int k = 0; k < 4; ++k) pr[k] = __builtin_amdgcn_perm(dw[k + 1], dw[k], c ? 0x07060302u : 0x05040100u);
      *(LAS u32x2*)(p + (4 * c) * UCS) = (u32x2){pr[0], pr[2]};
      *(LAS u32x2*)(p + (4 * c + 1) * UCS) = (u32x2){pr[1], pr[3]};
      *(LAS u32*)(p + (4 * c + 2) * UCS - 4) = pr[0]; *(LAS u32*)(p + (4 * c + 2) * UCS) = pr[2];
      *(LAS u32*)(p + (4 * c + 3) * UCS - 4) = pr[1]; *(LAS u32*)(p + (4 * c + 3) * UCS) = pr[3];
    }
  }
  if (w == 0) {
    const u32 u0 = __builtin_amdgcn_readlane(ud[0].x, 0);
    const int cs = lane >> 3, ix = lane & 7, last = (cs & 2) ? 6 : 7;
    const u32 v = ((cs & 1) && ix == last) ? (cs & 4 ? u0 & 0xffff0000u : u0 << 16) : 0u;
    if (ix <= last) *(LAS u32*)(lds + UC + cs * UCS + ix * 4) = v;
  }
  STAMP(16);
  BAR();
  STAMP(5);
  {
    const int c16 = lane & 15, hq = lane >> 4;
    LAS char* pa = lds + RV + ch * RVS + 2 * (2032 - 256 * 7 + 128 * hf - 16 * c16 + 8 * hq);
    LAS char* pb = lds + UC + (4 * ch + ((c16 + 1) & 3)) * UCS + 2 * (128 * hf + 8 * hq + 4 * ((c16 + 1) >> 2));
    f32x4 acc[8] = {};
    u32x4 fa[2][8], fb[2][8];
    auto ldf = [&](int rho, int m) {
      fa[rho & 1][m] = *(LAS u32x4*)(pa + 64 * rho + 512 * (7 - m));
      const u32x2 lo = *(volatile LAS u32x2*)(pb + 64 * rho + 512 * m), hi = *(volatile LAS u32x2*)(pb + 64 * rho + 512 * m + 8);
      fb[rho & 1][m] = (u32x4){lo.x, lo.y, hi.x, hi.y};
    };
#pragma unroll
    for (int m = 0; m < 8; ++m) ldf(0, m);
    u32 ydst = WS_YX + (cb * 32 + 8 * w + hq) * 8192 + jp * 256 + c16 * 16;
    u32 ymb = c16 == 15 || c16 == 0 ? WS_MBY + (cb * 32 + 8 * w + hq + (c16 ? 0 : 4)) * 256 + jp * 8 : WS_END;
    LAS char* exw = lds + EX + w * 8192 + lane * 16;
    LAS char* exr = lds + EX + w * 2048 + lane * 16;
    PIN(ydst); PIN(ymb); PIN(exw); PIN(exr);
#pragma unroll
    for (int rho = 0; rho < 3; ++rho) {
      SBAR();
#pragma unroll
      for (int g = 0; g < 8; ++g) {
#pragma unroll
        for (int aa = 0; aa <= g; ++aa) {
          if (aa + g < 8) acc[aa + g] = mfma16(fa[rho & 1][g], fb[rho & 1][aa], acc[aa + g]);
          if (aa < g && aa + g < 8) acc[aa + g] = mfma16(fa[rho & 1][aa], fb[rho & 1][g], acc[aa + g]);
        }
        ldf(rho + 1, g); SBAR();
      }
    }
#pragma unroll
    for (int t = 0; t < 8; ++t)
#pragma unroll
      for (int aa = 0; aa <= t; ++aa) {
        acc[t] = mfma16(fa[1][t - aa], fb[1][aa], acc[t]);
        SBAR();
#pragma unroll
        for (int f = 0; f < 7; ++f) if (t * (t + 1) / 2 + aa == f * (f + 3) / 2 + 8) { *(LAS f32x4*)(exw + f * 1024) = acc[f]; SBAR(); }
      }
    *(LAS f32x4*)(exw + 7168) = acc[7];
    STAMP(6);
    PRIO(ATAIL);
    BAR();
    STAMP(18);
    f32x4 e[8];
#pragma unroll
    for (int k = 0; k < 8; ++k) e[k] = *(LAS f32x4*)(exr + (k >> 2) * 1024 + (k & 3) * 8192);
    u32x4 v[2];
#pragma unroll
    for (int i = 0; i < 2; ++i) {
      const f32x4 y0 = e[4 * i] + e[4 * i + 1], y1 = e[4 * i + 2] + e[4 * i + 3];
      v[i] = (u32x4){pkbf(y0.x, y1.x), pkbf(y0.y, y1.y), pkbf(y0.z, y1.z), pkbf(y0.w, y1.w)};
      ST_SC1(v[i], rs, ydst + i * 32768);
    }
    const u32 ck = tag - row_sum2(sum4(0u, v[0]), sum4(0u, v[1]));
    __builtin_amdgcn_raw_buffer_store_b64((u32x2){tag, ck}, rs, ymb, 0, 16);
  }
  STAMP(7);
  {
    u32x4 y[2];
    const u32 mby = WS_MBY + a * 256 + (8 * w + lane) * 8;
    mail = poll64(ws, mby, lane < 8);
    float sf[16];
#pragma unroll
    for (int k = 0; k < 8; ++k) { sf[2 * k] = bflo(sk[k]); sf[2 * k + 1] = bfhi(sk[k]); PIN(sf[2 * k]); PIN(sf[2 * k + 1]); }
    u32 ysrc = WS_YX + a * 8192 + w * 2048 + lane * 16, odst = (64 * jp + (tid >> 3)) * 1792 + cb * 128 + (tid & 7) * 16;
    LAS char* tw = lds + (lane & 15) * 144 + (8 * w + (lane >> 4)) * 4;
    LAS char* tr = lds + (tid >> 3) * 144 + (tid & 7) * 16;
    PIN(ysrc); PIN(odst); PIN(tw); PIN(tr);
#pragma nounroll
    for (;;) {
#pragma nounroll
      while (__ballot((u32)mail == tag) != 0xffull && --spin > 0) { mail = poll64(ws, mby, lane < 8); COUNT(13); }
      STAMP(19);
      y[0] = LD_SC1(rs, ysrc); y[1] = LD_SC1(rs, ysrc + 1024);
#pragma unroll
      for (int i = 0; i < 2; ++i)
#pragma unroll
        for (int g = 0; g < 4; ++g) *(LAS u32*)(tw + g * 2304 + i * 16) = y[i][g];
      if (wave_sum(sum4(sum4((u32)(mail >> 32), y[0]), y[1])) == 8u * tag || --spin <= 0) break;
      mail = 0;
      COUNT(12);
    }
    STAMP(8);
    BAR();
#pragma unroll
    for (int i = 0; i < 2; ++i) {
      const u32x4 v = *(LAS u32x4*)(tr + i * 4608);
      u32x4 o;
#pragma unroll
      for (int k = 0; k < 4; ++k) o[k] = pkbf(bflo(v[k]) + sf[8 * i + 2 * k], bfhi(v[k]) + sf[8 * i + 2 * k + 1]);
      __builtin_amdgcn_raw_buffer_store_b128(o, RSRC(out, 2048 * 1792), odst + i * (32 * 1792), 0, 16);
    }
  }
  STAMP(9);
  STAMP_FLUSH();
}

__global__ void NOPK __launch_bounds__(256, 2)
fused_kernel(const void*, const void*, const void*, const void*, const void*, const void*, const void*, const void*, const void*,
             const void*, void*, void*) {
  __shared__ __attribute__((aligned(16))) char smem[81920];
  LAS char* lds = (LAS char*)smem;
  if ((blockIdx.x >> 5) == 7) {
    u64 pc;
    asm volatile("s_getpc_b64 %0" : "=s"(pc));
    if (threadIdx.x < CODE_LINES / 4)
      __builtin_amdgcn_global_load_lds((const GAS void*)((pc & ~127ull) + (threadIdx.x * 4 + ((blockIdx.x >> 3) & 3)) * 128), (LAS void*)lds, 4, 0, 0);
  }
  const __attribute__((address_space(4))) u64* ka = (const __attribute__((address_space(4))) u64*)__builtin_amdgcn_kernarg_segment_ptr();
  asm volatile("" : "+s"(ka));
  const char* u = (const char*)ka[0]; const GAS u32* logA = (const GAS u32*)ka[1]; const GAS u32* Aim = (const GAS u32*)ka[2];
  const GAS u32* Cre = (const GAS u32*)ka[3]; const GAS u32* Cim = (const GAS u32*)ka[4]; const GAS u32* logdt = (const GAS u32*)ka[5];
  const char* Dsk = (const char*)ka[6]; const char* x = (const char*)ka[7]; const char* wqkv = (const char*)ka[8];
  const char* wo = (const char*)ka[9]; char* out = (char*)ka[10]; char* ws = (char*)ka[11];
  asm volatile("" :: "s"(u), "s"(logA), "s"(Aim), "s"(Cre), "s"(Cim), "s"(logdt), "s"(Dsk), "s"(x), "s"(wqkv), "s"(wo), "s"(out), "s"(ws));
  const u32 epoch = request_epoch(ws);
  int bid = blockIdx.x;
  if (bid >= 256) role_b<true>(lds, x, wqkv, out, ws, epoch);
  asm volatile("" : "+s"(bid) :: "memory");
  if (bid >= 256) role_b<false>(lds, x, wqkv, out, ws, epoch);
  else if (bid < 224) {
    const int pi = bid * 256 + threadIdx.x;
    const u32 pA = __builtin_nontemporal_load(logA + pi), pI = __builtin_nontemporal_load(Aim + pi);
    const u32 pR = __builtin_nontemporal_load(Cre + pi), pC = __builtin_nontemporal_load(Cim + pi), ldt = logdt[bid];
    SBAR();
    role_a(lds, u, Dsk, wqkv, wo, out, ws, epoch, pA, pI, pR, pC, ldt);
  }
}

extern "C" void kernel_launch(void* const* d_in, const int* in_sizes, int n_in, void* d_out, int out_size, void* d_ws,
                              size_t ws_size, hipStream_t stream) {
  fused_kernel<<<512, 256, 0, stream>>>(d_in[0], d_in[1], d_in[2], d_in[3], d_in[4], d_in[5], d_in[6], d_in[7], d_in[8], d_in[9], d_out, d_ws);
}
```

```cpp
#define TLBW 524288
#include <hip/hip_runtime.h>
#include <stdint.h>

typedef unsigned u32;
typedef unsigned long long u64;
typedef short s16x4 __attribute__((ext_vector_type(4)));
typedef __bf16 bf16x8 __attribute__((ext_vector_type(8)));
typedef __bf16 bf16x2 __attribute__((ext_vector_type(2)));
typedef _Float16 f16x8 __attribute__((ext_vector_type(8)));
typedef _Float16 f16x2 __attribute__((ext_vector_type(2)));
typedef float f32x16 __attribute__((ext_vector_type(16)));
typedef float f32x4 __attribute__((ext_vector_type(4)));
typedef float f32x2 __attribute__((ext_vector_type(2)));
typedef u32 u32x4 __attribute__((ext_vector_type(4)));
typedef u32 u32x2 __attribute__((ext_vector_type(2)));
#define LAS __attribute__((address_space(3)))
#define GAS __attribute__((address_space(1)))
#define DI __device__ __forceinline__
#if defined(__HIP_DEVICE_COMPILE__)
#define NOPK __attribute__((target("no-packed-fp32-ops")))
#else
#define NOPK
#endif
#define RLX_AGENT __ATOMIC_RELAXED, __HIP_MEMORY_SCOPE_AGENT

#define CODE_LINES 208
#define WO_E 4
#define ORD 4
#define BMAP 1
#define UEARLY 3
#define WPUB 0
#define QT 7
#define HEADC 1
#define ATAIL 1

constexpr u32 WS_MB1 = 4096;
constexpr u32 WS_MB2 = WS_MB1 + 32768;
constexpr u32 WS_MBU = WS_MB2 + 32768;
constexpr u32 WS_MBY = WS_MBU + 57344;
constexpr u32 WS_KVP = 1u << 18;
constexpr u32 WS_OB = WS_KVP + (1u << 20);
constexpr u32 WS_UX = WS_OB + (2u << 20);
constexpr u32 WS_YX = WS_UX + (2u << 20);
constexpr u32 WS_WQ = WS_YX + (2u << 20);
constexpr u32 WS_WO = WS_WQ + (1u << 19);
constexpr u32 WS_MBQ = WS_WO + 458752;
constexpr u32 WS_END = WS_WO + (1u << 19);
constexpr u32 WS_DBG = WS_END;
constexpr int SPIN_CAP = 1 << 20;

#define STAMP(i) do {} while (0)
#define STAMP_DECL
#define COUNT(i) do {} while (0)
#define STAMP_FLUSH() do {} while (0)

DI float bflo(u32 v) { return __uint_as_float(v << 16); }
DI float bfhi(u32 v) { return __uint_as_float(v & 0xffff0000u); }
DI u32 pkbf(float a, float b) { f32x2 p = {a, b}; return __builtin_bit_cast(u32, __builtin_convertvector(p, bf16x2)); }
DI u32 pkh(f32x2 p) { return __builtin_bit_cast(u32, __builtin_convertvector(p, f16x2)); }
DI float rbf(float a) { return bflo(pkbf(a, a)); }
DI u32 umax3(u32 a, u32 b, u32 c) { u32 m = a > b ? a : b; return m > c ? m : c; }
DI u32 sum4(u32 m, u32x4 v) { return (m + v.x + v.y) + v.z + v.w; }
DI u32 row_sum(u32 v) {
  v += __builtin_amdgcn_update_dpp(0u, v, 0x111, 0xf, 0xf, true); v += __builtin_amdgcn_update_dpp(0u, v, 0x112, 0xf, 0xf, true);
  v += __builtin_amdgcn_update_dpp(0u, v, 0x114, 0xf, 0xf, true); v += __builtin_amdgcn_update_dpp(0u, v, 0x118, 0xf, 0xf, true);
  return v;
}
DI u32 row_sum2(u32 a, u32 b) {
  a += __builtin_amdgcn_update_dpp(0u, a, 0x111, 0xf, 0xf, true); b += __builtin_amdgcn_update_dpp(0u, b, 0x101, 0xf, 0xf, true);
  a += __builtin_amdgcn_update_dpp(0u, a, 0x112, 0xf, 0xf, true); b += __builtin_amdgcn_update_dpp(0u, b, 0x102, 0xf, 0xf, true);
  a += __builtin_amdgcn_update_dpp(0u, a, 0x114, 0xf, 0xf, true); b += __builtin_amdgcn_update_dpp(0u, b, 0x104, 0xf, 0xf, true);
  a += __builtin_amdgcn_update_dpp(0u, a, 0x118, 0xf, 0xf, true); b += __builtin_amdgcn_update_dpp(0u, b, 0x108, 0xf, 0xf, true);
  return (threadIdx.x & 8) ? a : b;
}
DI u32 wave_sum(u32 v) {
  v = row_sum(v);
  return (__builtin_amdgcn_readlane(v, 15) + __builtin_amdgcn_readlane(v, 31)) + (__builtin_amdgcn_readlane(v, 47) + __builtin_amdgcn_readlane(v, 63));
}
DI u32 wave_umax(u32 v) {
  u32 t;
  t = __builtin_amdgcn_update_dpp(0u, v, 0x111, 0xf, 0xf, false); v = v > t ? v : t;
  t = __builtin_amdgcn_update_dpp(0u, v, 0x112, 0xf, 0xf, false); v = v > t ? v : t;
  t = __builtin_amdgcn_update_dpp(0u, v, 0x114, 0xf, 0xf, false); v = v > t ? v : t;
  t = __builtin_amdgcn_update_dpp(0u, v, 0x118, 0xf, 0xf, false); v = v > t ? v : t;
  const u32 a = __builtin_amdgcn_readlane(v, 15), b = __builtin_amdgcn_readlane(v, 31), c = __builtin_amdgcn_readlane(v, 47), d = __builtin_amdgcn_readlane(v, 63);
  const u32 ab = a > b ? a : b, cd = c > d ? c : d;
  return ab > cd ? ab : cd;
}

DI u32 request_epoch(const char* ws) { u32 e; asm volatile("s_load_dword %0, %1, 0x0" : "=s"(e) : "s"(ws) : "memory"); return e; }
DI u32 take_tag(u32 e) { u32 r; asm volatile("s_waitcnt lgkmcnt(0)\n\ts_and_b32 %0, %1, -2" : "=s"(r) : "s"(e) : "memory", "scc"); return r * 1664525u + 2027808447u; }
DI u32 poll32(char* ws, u32 off, bool on) { u32 z = 0, r = 0; asm volatile("" : "+v"(z)); if (on) r = __hip_atomic_fetch_add((GAS u32*)(ws + off), z, RLX_AGENT); return r; }
DI u64 poll64(char* ws, u32 off, bool on) { u64 z = 0, r = 0; asm volatile("" : "+v"(z)); if (on) r = __hip_atomic_fetch_add((GAS u64*)(ws + off), z, RLX_AGENT); return r; }
#define SBAR() __builtin_amdgcn_sched_barrier(0)
#define PRIO(n) do { SBAR(); __builtin_amdgcn_s_setprio(n); SBAR(); } while (0)
#define PIN(x) asm volatile("" : "+v"(x))
DI f32x16 mfma32(u32x4 a, u32x4 b, f32x16 c) { return __builtin_amdgcn_mfma_f32_32x32x16_bf16(__builtin_bit_cast(bf16x8, a), __builtin_bit_cast(bf16x8, b), c, 0, 0, 0); }
DI f32x4 mfma16h(u32x4 a, u32x4 b, f32x4 c) { return __builtin_amdgcn_mfma_f32_16x16x32_f16(__builtin_bit_cast(f16x8, a), __builtin_bit_cast(f16x8, b), c, 0, 0, 0); }
DI f32x4 mfma16(u32x4 a, u32x4 b, f32x4 c) { return __builtin_amdgcn_mfma_f32_16x16x32_bf16(__builtin_bit_cast(bf16x8, a), __builtin_bit_cast(bf16x8, b), c, 0, 0, 0); }
DI u32x2 tr_read(LAS char* p) { return __builtin_bit_cast(u32x2, __builtin_amdgcn_ds_read_tr16_b64_v4i16((LAS s16x4*)p)); }
#define RSRC(p, n) __builtin_amdgcn_make_buffer_rsrc((void*)(p), 0, (n), 0x00020000)
#define DMA16(rs, voff, soff, dst, aux) __builtin_amdgcn_raw_ptr_buffer_load_lds((rs), (LAS void*)(dst), 16, (voff), (soff), 0, (aux))
#define WAITVM(n) do { asm volatile("" ::: "memory"); __builtin_amdgcn_s_waitcnt(0x0F70 | ((n) & 15) | (((n) >> 4) << 14)); asm volatile("" ::: "memory"); } while (0)
#define VMCNT(n) asm volatile("s_waitcnt vmcnt(" #n ")" ::: "memory")
#define BAR() do { asm volatile("s_waitcnt lgkmcnt(0)" ::: "memory"); __builtin_amdgcn_s_barrier(); asm volatile("" ::: "memory"); } while (0)
#define ST_SC1(v, rs, off) __builtin_amdgcn_raw_buffer_store_b128((v), (rs), (off), 0, 16)
#define LD_SC1(rs, off) __builtin_amdgcn_raw_buffer_load_b128((rs), (off), 0, 16)
#define LDS_SC1(rs, off, soff) __builtin_amdgcn_raw_buffer_load_b128((rs), (off), (soff), 16)
#define ST1_SC1(v, rs, off) __builtin_amdgcn_raw_buffer_store_b32((v), (rs), (off), 0, 16)

typedef unsigned short u16x2 __attribute__((ext_vector_type(2)));
DI u32 phi1(float a, float b) {
  const u32 t = pkbf(a, b);
  const f32x2 x = {bflo(t), bfhi(t)};
  const f32x2 y = x * 1.4426950408889634f;
  const float e0 = __builtin_amdgcn_fmed3f(__builtin_amdgcn_exp2f(y.x), 0.f, 1.f);
  const float e1 = __builtin_amdgcn_fmed3f(__builtin_amdgcn_exp2f(y.y), 0.f, 1.f);
  const f32x2 x1 = x + 1.0f;
  return pkbf(__builtin_fmaxf(x1.x, e0), __builtin_fmaxf(x1.y, e1));
}
DI u32 pkmin(u32 a, u32 b) { return __builtin_bit_cast(u32, __builtin_elementwise_min(__builtin_bit_cast(u16x2, a), __builtin_bit_cast(u16x2, b))); }
DI void phi_eps(u32x4 (&p)[2], u32 mn) {
  if (__any((mn & 0xffffu) < 0x3A80u || mn < 0x3A800000u)) {
#pragma unroll
    for (int j = 0; j < 8; ++j) { const u32 r = p[j >> 2][j & 3]; p[j >> 2][j & 3] = pkbf(bflo(r) + 1e-6f, bfhi(r) + 1e-6f); }
  }
}

template <bool HEAD>
DI void role_b(LAS char* lds, const char* __restrict__ x, const char* __restrict__ wqkv, char* __restrict__ out, char* ws, u32 epoch) {
  const int b = blockIdx.x - 256, tid = threadIdx.x, lane = tid & 63;
  const int w = __builtin_amdgcn_readfirstlane(tid >> 6);
  STAMP_DECL;
  STAMP(0);
  const int rb = 4 * ((b & 7) >> 1) + (b >> 6), h = (b & 1) * 8 + ((b >> 3) & 7);
  const __amdgpu_buffer_rsrc_t rs = RSRC(ws, WS_END), rx = RSRC(x, 2048 * 896), rw = RSRC(wqkv, 448 * 2688);
  constexpr int WR = 49152, KVI = WR + 4096, OST = 16384, OTR = WR;
#define XS(i) ((i) * 16384)
#define QS(cc) ((cc) < 2 ? WR + 24576 + (cc) * 4096 : (cc) < 6 ? XS(1) + ((cc) - 2) * 1024 : WR)
#define QD(cc) ((cc) >= 2 && (cc) < 6 ? 4096 : 1024)
  const int wsb = (56 * h) & ~15;
  const int coff = (h & 1) * 4;
  const int wone = (h & 1) ? 0 : 28;
  const u32 xv0 = (128 * rb + 32 * w + (lane >> 3)) * 896 + (((lane & 7) ^ (lane >> 4)) << 4);
  const u32 xv1 = (xv0 ^ 64) + 8 * 896;
  const u32 wv = (16 * w + (lane >> 2)) * 2688 + wsb + (lane & 3) * 16;
  const u32 l16 = lane * 16;
  int ch_[7];
  auto chunk = [&](int cc) { return ch_[cc]; };
  const int gi = h + rb;
  const u32 ordc = ORD == 4 ? ((gi & 2) ? ((gi & 1) ? 0x4652103u : 0x5461032u) : ((gi & 1) ? 0x4650321u : 0x6543210u))
                 : ORD == 5 ? ((gi & 2) ? ((gi & 1) ? 0x2103654u : 0x1065432u) : ((gi & 1) ? 0x3210546u : 0x6543210u))
                 : ORD == 6 ? ((gi % 3) == 0 ? 0x6543210u : (gi % 3) == 1 ? 0x6354021u : 0x6435102u)
                 : (u32)((0x65432106543210ull >> (4 * (gi % 7))) & 0xfffffff);
  auto next_chunk = [&](int cc) { int c = (ordc >> (4 * cc)) & 7; asm volatile("" : "+s"(c)); ch_[cc] = c; };
  auto issue_x = [&](int cc, int i) { DMA16(rx, (i & 1) ? xv1 : xv0, chunk(cc) * 128 + (i >> 1) * (16 * 896), lds + XS(cc % 3) + w * 4096 + i * 1024, 0); };
  auto issue_w = [&](int cc, int i) { DMA16(rw, wv, chunk(cc) * (64 * 2688) + 896 * (i + 1), lds + WR + (cc & 3) * 8192 + i * 4096 + w * 1024, 0); };
  bool qok = true;
  int spin = SPIN_CAP;
  u32 qso[7];
  auto issue_q = [&](int cc) { DMA16(rs, l16, qok ? qso[cc] : WS_END, lds + QS(cc) + w * QD(cc), 16); };
#pragma unroll
  for (int cc = 0; cc < 2; ++cc) {
    next_chunk(cc);
    if (HEAD == (cc < HEADC)) { issue_w(cc, 0); issue_w(cc, 1); issue_x(cc, 0); issue_x(cc, 1); issue_x(cc, 2); issue_x(cc, 3); }
    SBAR();
  }
  if (HEAD) return;
  next_chunk(2); next_chunk(3); next_chunk(4); next_chunk(5); next_chunk(6);
  __builtin_amdgcn_s_setprio(2);
  const int r32 = lane & 31, half = lane >> 5;
  const int xrd = w * 4096 + r32 * 128 + ((half ^ ((r32 >> 1) & 7)) << 4);
  LAS char* whi = lds + WR + (8 * half + ((lane >> 2) & 3)) * 64 + ((lane >> 4) & 1) * 32 + (lane & 3) * 8;
  PIN(whi);
  const int rt = h >> 2, ct = (h + ((h & 8) ? 2 : 1)) & 3;
  u32 wo_src = WS_WO + (4 * ct + w < 14 ? 4 * ct + w : 13) * 32768, kvp_src = WS_KVP + h * 65536 + w * 16384;
  u32 ob_dst = WS_OB + ((rb * 4 + w) * 16 + h) * 2048 + l16, ob_src = WS_OB + ((rb * 4 + rt) * 16 + 4 * w) * 2048;
  asm volatile("" : "+s"(wo_src), "+s"(kvp_src), "+s"(ob_src));
  PIN(ob_dst);
  u32 tell1 = lane < 4 ? WS_MB1 + (h * 16 + 4 * w + lane) * 128 + rb * 4 : WS_END;
  u32 tell2 = lane < 4 ? WS_MB2 + (rb * 16 + 4 * w + lane) * 128 + h * 4 : WS_END;
  u32 mb1 = WS_MB1 + (h * 16 + rb) * 128 + ((lane + rb) & 15) * 4, mb2 = WS_MB2 + (rb * 16 + h) * 128 + ((4 * w + lane) & 15) * 4;
  u32 kvp_dst = kvp_src + rb * 1024 + l16, kvs[16];
#pragma unroll
  for (int p = 1; p < 16; ++p) { kvs[p] = kvp_src + ((p + rb) & 15) * 1024; asm volatile("" : "+s"(kvs[p])); }
  const int dw = 8 * w + 4 * half - coff;
  u32 rowmask = dw < 0 || dw >= 28 ? 0u : ~0u;
  const bool padlo = (h & 1) && !half, padhi = !(h & 1) && half;
  const int oi = (r32 & 3) + 4 * (r32 >> 3);
  LAS char* kvrd = lds + KVI + ((oi < 14 ? 14 * ((r32 >> 2) & 1) + oi + coff : wone) + 32 * half) * 32;
  LAS char* qlo = lds + l16; LAS char* qhi = lds + WR + l16;
  PIN(tell1); PIN(tell2); PIN(mb1); PIN(mb2); PIN(kvp_dst); PIN(rowmask); PIN(kvrd); PIN(qhi);
  const bool ck = lane == wone + 1;
  u32 mbq = WS_MBQ + (h * 16 + rb) * 128 + (4 * (lane & 7) + w) * 4, mail = 0, tag = 0;
  PIN(mbq);
  u32x4 xf[28], wf[2][8];
  f32x16 ak = {}, av = {};
  auto rd = [&](int cc, int i) {
    if ((i & 1) == 0) xf[cc * 4 + (i >> 1)] = *(LAS u32x4*)(lds + XS(cc % 3) + (xrd ^ ((i >> 1) << 5)));
    LAS char* p = whi + (cc & 3) * 8192 + (i >> 1) * 1024 + (i & 1) * 4096;
    const u32x2 lo = tr_read(p), hi = tr_read(p + 256);
    wf[cc & 1][i] = (u32x4){lo.x, lo.y, hi.x, hi.y};
  };
#pragma unroll
  for (int cc = 0; cc < 6; ++cc) {
    if (cc == 0) VMCNT(6); else if (cc < 5) VMCNT(8); else VMCNT(7);
    BAR();
    if (cc == 0) { STAMP(1); tag = take_tag(epoch); }
    if (cc == 2) { STAMP(23); } if (cc == 4) { STAMP(24); } if (cc == 5) { STAMP(25); }
    if (cc == 5) {
#pragma unroll
      for (int i = 0; i < 8; ++i) { rd(cc, i); if (i < 7) { qso[i] = WS_WQ + (h * 28 + 4 * chunk(i) + w) * 1024; asm volatile("" : "+s"(qso[i])); } }
      SBAR();
    }
#pragma unroll
    for (int i = 0; i < 8; ++i) {
      if (cc > 0) {
        if (i & 1) av = mfma32(xf[(cc - 1) * 4 + (i >> 1)], wf[(cc - 1) & 1][i], av);
        else ak = mfma32(xf[(cc - 1) * 4 + (i >> 1)], wf[(cc - 1) & 1][i], ak);
      }
      if (cc < 5) rd(cc, i);
      if (cc == 0) { if (i < 2) issue_w(2, i); else if (i < 6) issue_x(2, i - 2); else issue_w(3, i - 6); }
      else if (cc <= 4 && i < 4) issue_x(cc + 2, i);
      else if (cc <= 3 && i < 6) issue_w(cc + 3, i - 4);
      else if (cc == 4 && i == 4) mail = poll32(ws, mbq, lane < 7);
      SBAR();
    }
#pragma unroll
    for (int s = 3; s >= 0; --s) { PIN(wf[cc & 1][2 * s + 1]); PIN(wf[cc & 1][2 * s]); PIN(xf[cc * 4 + s]); }
    if (cc > 0) { PIN(ak); PIN(av); }
  }
  VMCNT(1);
  BAR();
  STAMP(26);
  rd(6, 0); rd(6, 2); rd(6, 4); rd(6, 6);
  SBAR();
#pragma unroll
  for (int s = 0; s < 4; ++s) ak = mfma32(xf[20 + s], wf[1][2 * s], ak);
  PIN(ak);
#pragma unroll
  for (int s = 3; s >= 0; --s) { PIN(wf[0][2 * s]); PIN(xf[24 + s]); }
  rd(6, 1); rd(6, 3); rd(6, 5); rd(6, 7);
  SBAR();
#pragma unroll
  for (int s = 0; s < 4; ++s) ak = mfma32(xf[24 + s], wf[0][2 * s], ak);
  PIN(ak);
  SBAR();
  WAITVM(0);
  PIN(mail);
  qok = __ballot(mail == tag) == 0x7full;
  STAMP(2);
  u32x4 wof[32], d[16], pq[2];
  auto load_wo = [&](int k) { wof[k] = __builtin_amdgcn_raw_buffer_load_b128(rs, l16 + (k & 3) * 1024, wo_src + (k >> 2) * 4096, 16); };
  {
    u32x4 pk[2], pv[2];
    u32 mn = ~0u;
#pragma unroll
    for (int j = 0; j < 8; j += 2) {
      av = mfma32(xf[20 + j], wf[j < 4][2 * (j & 3) + 1], av);
      issue_q(j); if (j < 6) issue_q(j + 1);
      SBAR();
      pk[j >> 2][j & 3] = phi1(ak[2 * j], ak[2 * j + 1]);
      av = mfma32(xf[21 + j], wf[j < 4][2 * (j & 3) + 3], av);
      pk[j >> 2][(j & 3) + 1] = phi1(ak[2 * j + 2], ak[2 * j + 3]);
      mn = pkmin(pkmin(mn, pk[j >> 2][j & 3]), pk[j >> 2][(j & 3) + 1]);
      PIN(mn); PIN(av);
      SBAR();
    }
    phi_eps(pk, mn);
    STAMP(11);
#pragma unroll
    for (int j = 0; j < 8; ++j) { const u32 v = pkbf(av[2 * j], av[2 * j + 1]); pv[j >> 2][j & 3] = r32 == wone ? 0x3F803F80u : v; }
    f32x16 kv = {};
    kv = mfma32(pk[0], pv[0], kv); kv = mfma32(pk[1], pv[1], kv);
    ST1_SC1(tag, rs, tell1);
#pragma unroll
    for (int g = 0; g < 4; ++g) *(LAS f32x4*)(lds + XS(2) + w * 4096 + g * 1024 + l16) = (f32x4){kv[4 * g], kv[4 * g + 1], kv[4 * g + 2], kv[4 * g + 3]};
    STAMP(12);
    BAR();
    f32x4 s4 = *(LAS f32x4*)(lds + XS(2) + tid * 16);
#pragma unroll
    for (int ww = 1; ww < 4; ++ww) s4 += *(LAS f32x4*)(lds + XS(2) + ww * 4096 + tid * 16);
    u32x4 v = __builtin_bit_cast(u32x4, s4);
    const u32 part = v.y + v.z + v.w;
    const u32 c = tag - wave_sum(ck ? part : part + v.x);
    if (ck) v.x = c;
    ST_SC1(v, rs, kvp_dst);
    d[0] = v;
  }
  if (!qok) {
#pragma nounroll
    while (__ballot(poll32(ws, mbq, lane < 7) == tag) != 0x7full && --spin > 0) {}
    qok = true;
#pragma unroll
    for (int cc = 0; cc < 7; ++cc) issue_q(cc);
    WAITVM(0);
    COUNT(13);
  }
  u32 go, need = 0xfffeu;
  mail = poll32(ws, mb1, lane < 16);
#pragma unroll
  for (int k = 0; k < WO_E; ++k) load_wo(k);
  WAITVM(WO_E + 3);
  BAR();
  STAMP(3);
#define QP(cc) (QS(cc) >= WR ? qhi + (QS(cc) - WR) : qlo + QS(cc))
  auto load_kv = [&](int p, bool on = true) { d[p] = __builtin_amdgcn_raw_buffer_load_b128(rs, l16, on ? kvs[p] : WS_END, 16); };
  {
    f32x16 aq = {};
    u32x4 qf[2][4];
    u32 qsum = 0;
    auto read_q = [&](int cc) {
#pragma unroll
      for (int s = 0; s < 4; ++s) qf[cc & 1][s] = *(LAS u32x4*)(QP(cc) + s * QD(cc));
      SBAR();
    };
    read_q(0);
#pragma unroll
    for (int cc = 0; cc < 7; ++cc) {
      if (cc < 6) read_q(cc + 1);
#pragma unroll
      for (int s = 0; s < 4; ++s) {
        aq = mfma32(qf[cc & 1][s], xf[cc * 4 + s], aq);
        qsum = sum4(qsum, qf[cc & 1][s]);
        if (s & 1) load_wo(WO_E + 2 * cc + (s >> 1));
        PIN(aq); PIN(qsum);
        SBAR();
      }
    }
    if (wave_sum(qsum) != 28u * tag) {
      aq = (f32x16){};
#pragma nounroll
      for (int k2 = 0; k2 < 28; ++k2) {
        const int ks = 4 * ((ordc >> (k2 & 28)) & 7) + (k2 & 3);
        u32x4 f;
#pragma nounroll
        for (;;) {
          asm volatile("" ::: "memory");
          f = LDS_SC1(rs, l16, WS_WQ + (h * 28 + ks) * 1024);
          if (wave_sum(sum4(0u, f)) == tag || --spin <= 0) break;
        }
        aq = mfma32(f, __builtin_amdgcn_raw_buffer_load_b128(rx, (128 * rb + 32 * w + r32) * 896 + half * 16, ks * 32, 0), aq);
      }
      COUNT(15);
    }
    STAMP(14);
    go = need & (u32)__ballot(mail == tag); need &= ~go;
    mail = poll32(ws, mb1, lane < 16 && need != 0);
    STAMP(15);
    u32 mn = ~0u;
#pragma unroll
    for (int j = 0; j < 8; ++j) {
      if (j) load_kv(2 * j, go >> (2 * j) & 1);
      load_kv(2 * j + 1, go >> (2 * j + 1) & 1);
      u32 r = phi1(aq[2 * j], aq[2 * j + 1]);
      if (j < 2) r = padlo ? 0x3F803F80u : r; else if (j >= 6) r = padhi ? 0x3F803F80u : r;
      pq[j >> 2][j & 3] = r;
      mn = pkmin(mn, r);
      PIN(mn);
      SBAR();
    }
    phi_eps(pq, mn);
    PIN(pq[0]); PIN(pq[1]);
  }
  STAMP(5);
  PIN(mail);
  STAMP(16);
#pragma nounroll
  while (need != 0 && --spin > 0) {
    go = need & (u32)__ballot(mail == tag); need &= ~go;
#pragma unroll
    for (int p = 1; p < 16; ++p) if (go >> p & 1) load_kv(p);
    if (need != 0) { mail = poll32(ws, mb1, lane < 16); WAITVM(0); }
    COUNT(14);
  }
#pragma unroll
  for (int k = WO_E + 14; k < 32; ++k) load_wo(k);
  WAITVM(18 - WO_E);
  STAMP(18);
#pragma nounroll
  for (;;) {
    u32 m = 0;
#pragma unroll
    for (int p = 0; p < 16; ++p) m = sum4(m, d[p]);
    if (wave_sum(m) == 16u * tag || --spin <= 0) break;
    asm volatile("" ::: "memory");
#pragma unroll
    for (int p = 1; p < 16; ++p) load_kv(p);
    WAITVM(0);
    COUNT(11);
  }
  mail = poll32(ws, mb2, lane < 4);
  SBAR();
  {
    f32x4 s4 = __builtin_bit_cast(f32x4, d[0]);
#pragma unroll
    for (int p = 1; p < 16; ++p) s4 += __builtin_bit_cast(f32x4, d[p]);
    *(LAS u32x2*)(lds + KVI + lane * 32 + w * 8) = (u32x2){pkbf(s4.x, s4.y) & rowmask, pkbf(s4.z, s4.w) & rowmask};
  }
  STAMP(6);
  BAR();
  {
    const u32x4 a0 = *(LAS u32x4*)kvrd, a1 = *(LAS u32x4*)(kvrd + 16);
    f32x16 nm = {};
    nm = mfma32(a0, pq[0], nm); nm = mfma32(a1, pq[1], nm);
    const float den = rbf(rbf(nm[14]) + 1e-6f);
    const float rc = __builtin_amdgcn_rcpf(den);
    u32 o[8];
#pragma unroll
    for (int j = 0; j < 7; ++j) { const u32 t = pkbf(nm[2 * j], nm[2 * j + 1]); o[j] = pkbf(bflo(t) * rc, bfhi(t) * rc); }
    o[7] = (tag - ((o[0] + o[1] + o[2]) + (o[3] + o[4] + o[5]) + o[6])) & ~0x40004000u;
    __builtin_amdgcn_raw_buffer_store_b128((u32x4){o[0], o[1], o[2], o[3]}, rs, ob_dst, 0, 16);
    __builtin_amdgcn_raw_buffer_store_b128((u32x4){o[4], o[5], o[6], o[7]}, rs, ob_dst + 1024, 0, 16);
    ST1_SC1(tag, rs, tell2);
    SBAR();
  }
  STAMP(7);
  auto load_o = [&](int i) {
    DMA16(rs, l16, ob_src + i * 2048, lds + OST + (8 * w + 2 * i) * 1024, 16);
    DMA16(rs, l16, ob_src + i * 2048 + 1024, lds + OST + (8 * w + 2 * i + 1) * 1024, 16);
  };
  WAITVM(3);
  PIN(mail);
  need = 15u;
  bool wo_done = false;
#pragma nounroll
  for (;;) {
    go = need & (u32)__ballot(mail == tag); need &= ~go;
#pragma unroll
    for (int i = 0; i < 4; ++i) if (go >> i & 1) load_o(i);
    mail = poll32(ws, mb2, lane < 4 && need != 0);
    if (!wo_done) {
#pragma nounroll
      for (;;) {
        u32 m = 0;
#pragma unroll
        for (int k = 0; k < 32; ++k) m = sum4(m, wof[k]);
        if (__all(m == 16u * tag) || --spin <= 0) break;
        asm volatile("" ::: "memory");
#pragma unroll
        for (int k = 0; k < 32; ++k) load_wo(k);
        WAITVM(0);
      }
#pragma unroll
      for (int k = 1; k < 32; k += 2) { wof[k].w = 0u; PIN(wof[k]); }
      wo_done = true;
    }
    WAITVM(0);
    COUNT(12);
    if (need == 0 || --spin <= 0) break;
  }
  STAMP(8);
  f32x16 acc;
#pragma nounroll
  for (int it = 0; it < 4096; ++it) {
    BAR();
    STAMP(9);
    u32x4 of[32];
    u32 m = 0;
#pragma unroll
    for (int k = 0; k < 6; ++k) of[k] = *(LAS u32x4*)(lds + OST + k * 1024 + l16);
    acc = (f32x16){};
    SBAR();
#pragma unroll
    for (int k = 0; k < 32; ++k) {
      acc = mfma32(wof[k], of[k], acc);
      if (k + 6 < 32) of[k + 6] = *(LAS u32x4*)(lds + OST + (k + 6) * 1024 + l16);
      m = sum4(m, of[k]);
      PIN(m);
      SBAR();
    }
    if (__all(((16u * tag - m) & 0x3FF83FFFu) == 0u)) break;
    BAR();
    load_o(0); load_o(1); load_o(2); load_o(3);
    WAITVM(0);
    COUNT(13);
  }
  STAMP(22);
  {
    LAS char* p = lds + OTR + r32 * 272 + w * 64 + half * 32;
    *(LAS u32x4*)p = (u32x4){pkbf(acc[0], acc[1]), pkbf(acc[2], acc[3]), pkbf(acc[4], acc[5]), pkbf(acc[6], acc[7])};
    *(LAS u32x4*)(p + 16) = (u32x4){pkbf(acc[8], acc[9]), pkbf(acc[10], acc[11]), pkbf(acc[12], acc[13]), pkbf(acc[14], acc[15])};
  }
  BAR();
  if (ct < 3 || (tid & 15) < 8) {
#pragma unroll
    for (int i = 0; i < 2; ++i)
      __builtin_amdgcn_raw_buffer_store_b128(*(LAS u32x4*)(lds + OTR + (16 * i + (tid >> 4)) * 272 + (tid & 15) * 16), RSRC(out, 2048 * 1792),
                                             (128 * rb + 32 * rt + 16 * i + (tid >> 4)) * 1792 + 896 + ct * 256 + (tid & 15) * 16, 0, 16);
  }
  if (b == 255 && tid == 0) __hip_atomic_store((GAS u32*)ws, tag, RLX_AGENT);
  STAMP(10);
  STAMP_FLUSH();
}

DI u32 hmul1(u32 a, u32 b) { const f16x2 x = __builtin_bit_cast(f16x2, a), y = __builtin_bit_cast(f16x2, b); return __builtin_bit_cast(u32, (f16x2){x.x, x.x} * y); }
DI u32 hmul2(u32 a, u32 b, u32 t) {
  const f16x2 x = __builtin_bit_cast(f16x2, a), y = __builtin_bit_cast(f16x2, b);
  return __builtin_bit_cast(u32, __builtin_elementwise_fma((f16x2){x.y, x.y}, (f16x2){-y.y, y.x}, __builtin_bit_cast(f16x2, t)));
}
DI u32 hmul(u32 a, u32 b) { return hmul2(a, b, hmul1(a, b)); }
DI f32x2 csq(f32x2 a) { return (f32x2){__builtin_fmaf(a.x, a.x, -a.y * a.y), (a.x + a.x) * a.y}; }
DI float exp_acc(float x) {
  const float hi = x * 1.4426950408889634f, lo = __builtin_fmaf(x, 1.9259629911266175e-8f, __builtin_fmaf(x, 1.4426950408889634f, -hi));
  const float e = __builtin_amdgcn_exp2f(hi);
  return __builtin_fmaf(e, lo * 0.6931471805599453f, e);
}
DI f32x2 cexp_p(float a2, float f, float p) {
  const float m = __builtin_amdgcn_exp2f(a2 * p), ph = __builtin_amdgcn_fractf(f * p);
  return (f32x2){m, m} * (f32x2){__builtin_amdgcn_cosf(ph), __builtin_amdgcn_sinf(ph)};
}
DI void role_a(LAS char* lds, const char* __restrict__ u, const char* __restrict__ Dsk, const char* __restrict__ wqkv, const char* __restrict__ wo, char* __restrict__ out, char* ws, u32 epoch,
               u32 pA, u32 pI, u32 pR, u32 pC, u32 ldt) {
  const int a = blockIdx.x, cb = a >> 5, jp = a & 31, tid = threadIdx.x, lane = tid & 63;
  const int w = __builtin_amdgcn_readfirstlane(tid >> 6);
  STAMP_DECL;
  STAMP(0);
  __builtin_amdgcn_s_setprio(3);
  constexpr int SROW = 528, STG = 32 * SROW, UC = 0, UCS = 4160, RV = 36864, RVS = 4608, EX = RV + 2 * RVS, UIM = 4 * STG;
  const int PKS = UIM + 8192 + (w ? 1024 + w * 1024 : 0);
  const __amdgpu_buffer_rsrc_t rs = RSRC(ws, WS_END);
  const int ch = w >> 1, hf = w & 1;
  int spin = SPIN_CAP;
  const int r32 = lane & 31, half = lane >> 5;
  const int pkh_ = w == 0 ? (a & 15) : 2 * (a & 7) + (w - 1);
  {
    const __amdgpu_buffer_rsrc_t rp = RSRC(w == 0 ? wo : wqkv, w == 0 ? 448 * 896 : w < 3 ? 448 * 2688 : 0);
    const u32 vo = (lane >> 2) * (w == 0 ? 896 : 2688) + (lane & 3) * 16;
    const u32 so = w == 0 ? 28 * 896 * pkh_ + (a >> 4) * 64 : 16 * 2688 * (a >> 3) + ((56 * pkh_) & ~15);
    DMA16(rp, vo, so, lds + PKS, 0);
    DMA16(rp, vo, w == 0 ? so + 16 * 896 : 448 * 2688, lds + (w == 0 ? PKS + 1024 : UIM + 8192 + 5120), 0);
  }
  u32x4 dk;
  auto load_tile = [&]() {
#pragma unroll
    for (int i = 0; i < 2; ++i)
      DMA16(RSRC(u, 2048 * 896), (tid >> 3) * 896 + (((tid & 7) ^ ((tid >> 5) & 7)) << 4), (64 * jp + 32 * i) * 896 + cb * 128, lds + UIM + (32 * i + 8 * w) * 128, 2);
    dk = *(const GAS u32x4*)(Dsk + cb * 128 + (tid & 7) * 16);
  };
  PRIO(0);
  int tix[8];
#pragma unroll
  for (int j = 0; j < 8; ++j) {
    const int p = (int)rbf((float)(2047 - 8 * tid - j));
    const int low = p & 7, blk = p >> 8;
    const int k4 = (blk < 4 ? blk : 4) * 4;
    const int sh = (0x32210 >> k4) & 15;
    const int base = blk + ((0xCBA70 >> k4) & 15);
    tix[j] = ((base + (low >> sh)) * 32 + ((p >> 3) & 31)) * 4;
    PIN(tix[j]);
  }
  const u32 tag = take_tag(epoch);
  auto publish_weights = [&]() {
  PRIO(3);
  BAR();
  if (w == 0) {
    const int q = (lane >> 2) & 3, gam = 4 * ((lane >> 4) & 1) + (lane & 3), ng = 4 * (gam & 1) + (gam >> 1);
    LAS char* p = lds + PKS + (14 * half + q) * 64 + 8 * ng;
    const u32x2 r0 = tr_read(p), r1 = tr_read(p + 256), r2 = tr_read(p + 512), r3 = tr_read(p + 768);
    const u32 off = WS_WO + ((a >> 4) * 16 + pkh_) * 2048 + lane * 16;
    ST_SC1(((u32x4){r0.x, r0.y, r1.x, r1.y}), rs, off);
    ST_SC1(((u32x4){r2.x, r2.y, r3.x, tag - ((r0.x + r0.y + r1.x) + (r1.y + r2.x + r2.y) + r3.x)}), rs, off + 1024);
  } else if (w < 3) {
    LAS char* p = lds + PKS + (8 * half + ((lane >> 2) & 3)) * 64 + ((lane >> 4) & 1) * 32 + (lane & 3) * 8;
    const u32x2 r0 = tr_read(p), r1 = tr_read(p + 256);
    u32x4 v = {r0.x, r0.y, r1.x, r1.y};
    const bool ck = lane == ((pkh_ & 1) ? 1 : 29);
    if (ck) v = (u32x4){0u, 0u, 0u, 0u};
    const u32 c = tag - wave_sum(sum4(0u, v));
    if (ck) v.x = c;
    ST_SC1(v, rs, WS_WQ + (pkh_ * 28 + (a >> 3)) * 1024 + lane * 16);
  }
  PRIO(0);
  };
  auto publish_u = [&]() {
  PRIO(3);
  BAR();
  {
    const int n = tid & 15;
    u32x4 v[2];
#pragma unroll
    for (int i = 0; i < 2; ++i) {
      const int jj = 16 * i + (tid >> 4);
      LAS char* p = lds + UIM + 4 * n * 128 + (((jj >> 2) ^ (n & 7)) << 4) + (jj & 3) * 4;
      v[i] = (u32x4){*(LAS u32*)p, *(LAS u32*)(p + 128), *(LAS u32*)(p + 256), *(LAS u32*)(p + 384)};
    }
    const u32 off = WS_UX + (cb * 32 + (tid >> 4)) * 8192 + jp * 256 + n * 16;
    ST_SC1(v[0], rs, off); ST_SC1(v[1], rs, off + 16 * 8192);
    const u32 ck = tag - row_sum2(sum4(0u, v[0]), sum4(0u, v[1]));
    __builtin_amdgcn_raw_buffer_store_b64((u32x2){tag, ck}, rs, n == 15 || n == 0 ? WS_MBU + (cb * 32 + (tid >> 4) + (n ? 0 : 16)) * 256 + jp * 8 : WS_END, 0, 16);
  }
  PRIO(0);
  };
  if (WPUB == 0) { WAITVM(0); publish_weights(); } else WAITVM(2);
  PIN(pA); PIN(pI); PIN(pR); PIN(pC); PIN(ldt);
  STAMP(1);
  const float dt = rbf(exp_acc(ch ? bfhi(ldt) : bflo(ldt)));
  const u32 mx = wave_umax(umax3(umax3(pR & 0x7fffu, (pR >> 16) & 0x7fffu, pC & 0x7fffu), (pC >> 16) & 0x7fffu, 0u));
  int ex = mx >> 7; ex = ex < 13 ? 13 : ex > 254 ? 254 : ex;
  const float sc = __uint_as_float((u32)(266 - ex) << 23), isc = __uint_as_float((u32)(ex - 12) << 23);
  LAS char* stg = lds + w * STG;
  u32x4 ud[3];
  const __amdgpu_buffer_rsrc_t ru = RSRC(ws + WS_UX + a * 8192, 8192);
  const int npc = w < 3 ? 9 : 8;
  u64 mail = 0;
  const u32 mbu = WS_MBU + a * 256 + (8 * w + lane) * 8;
  {
    u32x2 c0, y1, y2, y4, q1, q2, q4, q8, z8, z16, z32, z64, z128;
#pragma unroll
    for (int m = 0; m < 2; ++m) {
      const float la = m ? bfhi(pA) : bflo(pA), ai = m ? bfhi(pI) : bflo(pI), cr = m ? bfhi(pR) : bflo(pR), ci = m ? bfhi(pC) : bflo(pC);
      const float a2 = -rbf(exp_acc(la)) * dt * 1.4426950408889634f, f = ai * dt * 0.15915494309189535f;
      f32x2 a = cexp_p(a2, -f, 1.f), b = cexp_p(a2, -f, 256.f), c = cexp_p(a2, f, 8.f);
      c0[m] = pkh((f32x2){cr * sc, -ci * sc});
      y1[m] = pkh(a); a = csq(a); y2[m] = pkh(a); a = csq(a); y4[m] = pkh(a);
      q1[m] = pkh(b); b = csq(b); q2[m] = pkh(b); b = csq(b); q4[m] = pkh(b); b = csq(b); q8[m] = pkh(b);
      z8[m] = pkh(c); c = csq(c); z16[m] = pkh(c); c = csq(c); z32[m] = pkh(c); c = csq(c); z64[m] = pkh(c); c = csq(c); z128[m] = pkh(c);
    }
#define OPQ(v) asm("" : "+v"(v))
    if (WPUB == 1) { PIN(c0); PIN(y4); PIN(q8); PIN(z128); WAITVM(0); publish_weights(); }
    OPQ(c0); OPQ(y1); OPQ(y2); OPQ(y4); OPQ(q1); OPQ(q2); OPQ(q4); OPQ(q8); OPQ(z8); OPQ(z16); OPQ(z32); OPQ(z64); OPQ(z128);
    LAS char* sw = stg + lane * 8;
    auto put = [&](int r, u32x2 v) { *(LAS u32x2*)(sw + r * SROW) = v; };
    auto mul = [&](u32x2 x, u32x2 y) { const u32 x0 = x.x, x1 = x.y, y0 = y.x, y1_ = y.y; u32x2 r = {hmul(x0, y0), hmul(x1, y1_)}; OPQ(r); return r; };
    put(0, c0); put(21, (u32x2){0x3C00u, 0x3C00u}); put(22, z8); put(23, z16); put(25, z32); put(26, z64); put(28, z128);
    const u32x2 c2 = mul(c0, y2), c4 = mul(c0, y4), c512 = mul(c0, q2), c1024 = mul(c0, q4), z96 = mul(z32, z64);
    put(2, c2); put(4, c4); put(12, c512); put(16, c1024); put(27, z96);
    const u32x2 c6 = mul(c4, y2), c516 = mul(c4, q2), c1536 = mul(c1024, q2);
    put(6, c6); put(13, c516); put(18, c1536);
    put(1, mul(c0, y1)); put(3, mul(c2, y1)); put(5, mul(c4, y1)); put(7, mul(c6, y1));
    put(8, mul(c0, q1)); put(9, mul(c2, q1)); put(10, mul(c4, q1)); put(11, mul(c6, q1));
    put(14, mul(c512, q1)); put(15, mul(c516, q1)); put(17, mul(c1024, q1)); put(19, mul(c1536, q1)); put(20, mul(c0, q8));
    put(24, mul(z8, z16)); put(29, mul(z32, z128)); put(30, mul(z64, z128)); put(31, mul(z96, z128));
  }
  STAMP(2);
  if (WPUB < 2) {
    PRIO(3);
    if (UEARLY == 3) { load_tile(); WAITVM(3); } else WAITVM(0);
    ST1_SC1(tag, rs, lane < 16 && (w == 1 || w == 2) ? WS_MBQ + (pkh_ * 16 + lane) * 128 + (a >> 3) * 4 : WS_END);
    PRIO(0);
  } else {
    WAITVM(0);
    if (UEARLY == 3) { PRIO(3); load_tile(); }
    publish_weights();
  }
  STAMP(13);
  {
    const int c16 = lane & 15, quad = lane >> 4;
    LAS char* py0 = stg + c16 * SROW + quad * 16;
    LAS char* py1 = stg + (16 + (c16 < 4 ? c16 : 4)) * SROW + quad * 16;
    LAS char* pt1 = stg + (21 + (c16 & 3)) * SROW + quad * 16;
    LAS char* pt2 = stg + (c16 < 4 ? 21 : 24 + (c16 >> 2)) * SROW + quad * 16;
    LAS char* pt3 = stg + (28 + (c16 >> 2)) * SROW + quad * 16;
    f32x4 ka[4] = {};
    u32x4 ft[2][3], fy[2][2], xx[2][2];
    auto rdt = [&](int s) { ft[s & 1][0] = *(LAS u32x4*)(pt1 + s * 64); ft[s & 1][1] = *(LAS u32x4*)(pt2 + s * 64); ft[s & 1][2] = *(LAS u32x4*)(pt3 + s * 64); };
    auto rdy = [&](int s) { fy[s & 1][0] = *(LAS u32x4*)(py0 + s * 64); fy[s & 1][1] = *(LAS u32x4*)(py1 + s * 64); };
    auto prod = [&](int s, int i) {
      u32 t1 = ft[s & 1][0][i], t2 = ft[s & 1][1][i], t3 = ft[s & 1][2][i];
      asm("" : "+v"(t2), "+v"(t3));
      const u32 p0 = hmul1(t1, t2), p1 = hmul1(t1, t3);
      xx[s & 1][0][i] = hmul2(t1, t2, p0); xx[s & 1][1][i] = hmul2(t1, t3, p1);
    };
    rdt(0); rdt(1); rdy(0);
    prod(0, 0); prod(0, 1); prod(0, 2); prod(0, 3);
#pragma unroll
    for (int s = 0; s < 8; ++s) {
      if (WPUB == 2 && s == QT) {
        PRIO(3); WAITVM(0);
        ST1_SC1(tag, rs, lane < 16 && (w == 1 || w == 2) ? WS_MBQ + (pkh_ * 16 + lane) * 128 + (a >> 3) * 4 : WS_END);
        PRIO(0);
      }
      if (s < 6) rdt(s + 2);
      if (s < 7) rdy(s + 1);
      SBAR();
#pragma unroll
      for (int i = 0; i < 4; ++i) {
        ka[i] = mfma16h(fy[s & 1][i >> 1], xx[s & 1][i & 1], ka[i]);
        if (s < 7) prod(s + 1, i);
        SBAR();
      }
    }
    if (UEARLY == 0) { PRIO(3); load_tile(); PRIO(0); }
    STAMP(3);
    const float fac = 2.f * dt * isc;
    LAS float* tb = (LAS float*)stg + 4 * quad * 32 + c16;
#pragma unroll
    for (int g = 0; g < 4; ++g) { tb[g * 32] = ka[0][g] * fac; tb[g * 32 + 16] = ka[1][g] * fac; }
    if (quad < 2) {
#pragma unroll
      for (int g = 0; g < 4; ++g) { tb[(16 + g) * 32] = ka[2][g] * fac; tb[(16 + g) * 32 + 16] = ka[3][g] * fac; }
    }
  }
  STAMP(4);
  if (UEARLY == 3) { if (WPUB < 2) WAITVM(1); publish_u(); } else BAR();
  {
    float kk[4][8];
#pragma unroll
    for (int c = 0; c < 4; ++c)
#pragma unroll
      for (int j = 0; j < 8; ++j) kk[c][j] = *(LAS float*)(lds + c * STG + tix[j]);
    SBAR();
#pragma unroll
    for (int c = 0; c < 2; ++c) {
      u32x4 v;
#pragma unroll
      for (int j = 0; j < 4; ++j) v[j] = pkbf(kk[2 * c][2 * j] + kk[2 * c + 1][2 * j], kk[2 * c][2 * j + 1] + kk[2 * c + 1][2 * j + 1]);
      *(LAS u32x4*)(lds + RV + c * RVS + tid * 16) = v;
    }
  }
  STAMP(11);
  if (UEARLY) { PRIO(3); mail = poll64(ws, mbu, lane < npc); PRIO(0); BAR(); } else { WAITVM(0); publish_u(); }
  STAMP(12);
  u32 sk[8];
#pragma unroll
  for (int i = 0; i < 2; ++i) {
    const u32x4 us = *(LAS u32x4*)(lds + UIM + (32 * i + (tid >> 3)) * 128 + (((tid & 7) ^ ((tid >> 5) & 7)) << 4));
#pragma unroll
    for (int k = 0; k < 4; ++k) { sk[4 * i + k] = pkbf(bflo(us[k]) * bflo(dk[k]), bfhi(us[k]) * bfhi(dk[k])); PIN(sk[4 * i + k]); }
  }
  if (!UEARLY) { PRIO(3); mail = poll64(ws, mbu, lane < npc); PRIO(0); }
  STAMP(14);
  if (tid < 64) *(LAS u32x4*)(lds + RV + half * RVS + 4096 + r32 * 16) = (u32x4){0u, 0u, 0u, 0u};
  SBAR();
  PRIO(3);
#pragma nounroll
  for (;;) {
#pragma nounroll
    while (__popcll(__ballot((u32)mail == tag)) != npc && --spin > 0) { mail = poll64(ws, mbu, lane < npc); COUNT(14); }
    ud[0] = LD_SC1(ru, w * 2048 + lane * 16); ud[1] = LD_SC1(ru, w * 2048 + 1024 + lane * 16);
    ud[2] = LD_SC1(ru, lane < 16 ? w * 2048 + 2048 + lane * 16 : 8192);
    if (wave_sum(sum4(sum4(sum4((u32)(mail >> 32), ud[0]), ud[1]), ud[2])) == npc * tag || --spin <= 0) break;
    mail = 0;
    COUNT(11);
  }
  PRIO(0);
  STAMP(15);
#pragma unroll
  for (int i = 0; i < 2; ++i) {
    const u32 nx = __builtin_amdgcn_update_dpp(__builtin_amdgcn_readlane(ud[i + 1].x, 0), ud[i].x, 0x130, 0xf, 0xf, false);
    const u32 dw[5] = {ud[i].x, ud[i].y, ud[i].z, ud[i].w, nx};
    LAS char* p = lds + UC + (128 * w + 64 * i + lane + 4) * 8;
#pragma unroll
    for (int c = 0; c < 2; ++c) {
      u32 pr[4];
#pragma unroll
      for (int k = 0; k < 4; ++k) pr[k] = __builtin_amdgcn_perm(dw[k + 1], dw[k], c ? 0x07060302u : 0x05040100u);
      *(LAS u32x2*)(p + (4 * c) * UCS) = (u32x2){pr[0], pr[2]};
      *(LAS u32x2*)(p + (4 * c + 1) * UCS) = (u32x2){pr[1], pr[3]};
      *(LAS u32*)(p + (4 * c + 2) * UCS - 4) = pr[0]; *(LAS u32*)(p + (4 * c + 2) * UCS) = pr[2];
      *(LAS u32*)(p + (4 * c + 3) * UCS - 4) = pr[1]; *(LAS u32*)(p + (4 * c + 3) * UCS) = pr[3];
    }
  }
  if (w == 0) {
    const u32 u0 = __builtin_amdgcn_readlane(ud[0].x, 0);
    const int cs = lane >> 3, ix = lane & 7, last = (cs & 2) ? 6 : 7;
    const u32 v = ((cs & 1) && ix == last) ? (cs & 4 ? u0 & 0xffff0000u : u0 << 16) : 0u;
    if (ix <= last) *(LAS u32*)(lds + UC + cs * UCS + ix * 4) = v;
  }
  STAMP(16);
  BAR();
  STAMP(5);
  {
    const int c16 = lane & 15, hq = lane >> 4;
    LAS char* pa = lds + RV + ch * RVS + 2 * (2032 - 256 * 7 + 128 * hf - 16 * c16 + 8 * hq);
    LAS char* pb = lds + UC + (4 * ch + ((c16 + 1) & 3)) * UCS + 2 * (128 * hf + 8 * hq + 4 * ((c16 + 1) >> 2));
    f32x4 acc[8] = {};
    u32x4 fa[2][8], fb[2][8];
    auto ldf = [&](int rho, int m) {
      fa[rho & 1][m] = *(LAS u32x4*)(pa + 64 * rho + 512 * (7 - m));
      const u32x2 lo = *(volatile LAS u32x2*)(pb + 64 * rho + 512 * m), hi = *(volatile LAS u32x2*)(pb + 64 * rho + 512 * m + 8);
      fb[rho & 1][m] = (u32x4){lo.x, lo.y, hi.x, hi.y};
    };
#pragma unroll
    for (int m = 0; m < 8; ++m) ldf(0, m);
    u32 ydst = WS_YX + (cb * 32 + 8 * w + hq) * 8192 + jp * 256 + c16 * 16;
    u32 ymb = c16 == 15 || c16 == 0 ? WS_MBY + (cb * 32 + 8 * w + hq + (c16 ? 0 : 4)) * 256 + jp * 8 : WS_END;
    LAS char* exw = lds + EX + w * 8192 + lane * 16;
    LAS char* exr = lds + EX + w * 2048 + lane * 16;
    PIN(ydst); PIN(ymb); PIN(exw); PIN(exr);
#pragma unroll
    for (int rho = 0; rho < 3; ++rho) {
      SBAR();
#pragma unroll
      for (int g = 0; g < 8; ++g) {
#pragma unroll
        for (int aa = 0; aa <= g; ++aa) {
          if (aa + g < 8) acc[aa + g] = mfma16(fa[rho & 1][g], fb[rho & 1][aa], acc[aa + g]);
          if (aa < g && aa + g < 8) acc[aa + g] = mfma16(fa[rho & 1][aa], fb[rho & 1][g], acc[aa + g]);
        }
        ldf(rho + 1, g); SBAR();
      }
    }
#pragma unroll
    for (int t = 0; t < 8; ++t)
#pragma unroll
      for (int aa = 0; aa <= t; ++aa) {
        acc[t] = mfma16(fa[1][t - aa], fb[1][aa], acc[t]);
        SBAR();
#pragma unroll
        for (int f = 0; f < 7; ++f) if (t * (t + 1) / 2 + aa == f * (f + 3) / 2 + 8) { *(LAS f32x4*)(exw + f * 1024) = acc[f]; SBAR(); }
      }
    *(LAS f32x4*)(exw + 7168) = acc[7];
    STAMP(6);
    PRIO(ATAIL);
    BAR();
    STAMP(18);
    f32x4 e[8];
#pragma unroll
    for (int k = 0; k < 8; ++k) e[k] = *(LAS f32x4*)(exr + (k >> 2) * 1024 + (k & 3) * 8192);
    u32x4 v[2];
#pragma unroll
    for (int i = 0; i < 2; ++i) {
      const f32x4 y0 = e[4 * i] + e[4 * i + 1], y1 = e[4 * i + 2] + e[4 * i + 3];
      v[i] = (u32x4){pkbf(y0.x, y1.x), pkbf(y0.y, y1.y), pkbf(y0.z, y1.z), pkbf(y0.w, y1.w)};
      ST_SC1(v[i], rs, ydst + i * 32768);
    }
    const u32 ck = tag - row_sum2(sum4(0u, v[0]), sum4(0u, v[1]));
    __builtin_amdgcn_raw_buffer_store_b64((u32x2){tag, ck}, rs, ymb, 0, 16);
  }
  STAMP(7);
  {
    u32x4 y[2];
    const u32 mby = WS_MBY + a * 256 + (8 * w + lane) * 8;
    mail = poll64(ws, mby, lane < 8);
    float sf[16];
#pragma unroll
    for (int k = 0; k < 8; ++k) { sf[2 * k] = bflo(sk[k]); sf[2 * k + 1] = bfhi(sk[k]); PIN(sf[2 * k]); PIN(sf[2 * k + 1]); }
    u32 ysrc = WS_YX + a * 8192 + w * 2048 + lane * 16, odst = (64 * jp + (tid >> 3)) * 1792 + cb * 128 + (tid & 7) * 16;
    LAS char* tw = lds + (lane & 15) * 144 + (8 * w + (lane >> 4)) * 4;
    LAS char* tr = lds + (tid >> 3) * 144 + (tid & 7) * 16;
    PIN(ysrc); PIN(odst); PIN(tw); PIN(tr);
#pragma nounroll
    for (;;) {
#pragma nounroll
      while (__ballot((u32)mail == tag) != 0xffull && --spin > 0) { mail = poll64(ws, mby, lane < 8); COUNT(13); }
      STAMP(19);
      y[0] = LD_SC1(rs, ysrc); y[1] = LD_SC1(rs, ysrc + 1024);
#pragma unroll
      for (int i = 0; i < 2; ++i)
#pragma unroll
        for (int g = 0; g < 4; ++g) *(LAS u32*)(tw + g * 2304 + i * 16) = y[i][g];
      if (wave_sum(sum4(sum4((u32)(mail >> 32), y[0]), y[1])) == 8u * tag || --spin <= 0) break;
      mail = 0;
      COUNT(12);
    }
    STAMP(8);
    BAR();
#pragma unroll
    for (int i = 0; i < 2; ++i) {
      const u32x4 v = *(LAS u32x4*)(tr + i * 4608);
      u32x4 o;
#pragma unroll
      for (int k = 0; k < 4; ++k) o[k] = pkbf(bflo(v[k]) + sf[8 * i + 2 * k], bfhi(v[k]) + sf[8 * i + 2 * k + 1]);
      __builtin_amdgcn_raw_buffer_store_b128(o, RSRC(out, 2048 * 1792), odst + i * (32 * 1792), 0, 16);
    }
  }
  STAMP(9);
  STAMP_FLUSH();
}

__global__ void NOPK __launch_bounds__(256, 2)
fused_kernel(const void*, const void*, const void*, const void*, const void*, const void*, const void*, const void*, const void*,
             const void*, void*, void*) {
  __shared__ __attribute__((aligned(16))) char smem[81920];
  LAS char* lds = (LAS char*)smem;
  if ((blockIdx.x >> 5) == 7) {
    u64 pc;
    asm volatile("s_getpc_b64 %0" : "=s"(pc));
    if (threadIdx.x < CODE_LINES / 4)
      __builtin_amdgcn_global_load_lds((const GAS void*)((pc & ~127ull) + (threadIdx.x * 4 + ((blockIdx.x >> 3) & 3)) * 128), (LAS void*)lds, 4, 0, 0);
  }
  const __attribute__((address_space(4))) u64* ka = (const __attribute__((address_space(4))) u64*)__builtin_amdgcn_kernarg_segment_ptr();
  asm volatile("" : "+s"(ka));
  const char* u = (const char*)ka[0]; const GAS u32* logA = (const GAS u32*)ka[1]; const GAS u32* Aim = (const GAS u32*)ka[2];
  const GAS u32* Cre = (const GAS u32*)ka[3]; const GAS u32* Cim = (const GAS u32*)ka[4]; const GAS u32* logdt = (const GAS u32*)ka[5];
  const char* Dsk = (const char*)ka[6]; const char* x = (const char*)ka[7]; const char* wqkv = (const char*)ka[8];
  const char* wo = (const char*)ka[9]; char* out = (char*)ka[10]; char* ws = (char*)ka[11];
  asm volatile("" :: "s"(u), "s"(logA), "s"(Aim), "s"(Cre), "s"(Cim), "s"(logdt), "s"(Dsk), "s"(x), "s"(wqkv), "s"(wo), "s"(out), "s"(ws));
  const u32 epoch = request_epoch(ws);
  int bid = blockIdx.x;
  if (bid >= 256) role_b<true>(lds, x, wqkv, out, ws, epoch);
  asm volatile("" : "+s"(bid) :: "memory");
  if (bid >= 224) {
    if (bid >= 256) role_b<false>(lds, x, wqkv, out, ws, epoch);
    else {
      const u32 i = threadIdx.x * 4 + ((bid >> 3) & 3), NW = WS_END / TLBW, NO = NW + 2048 * 1792 / TLBW, NU = NO + 2048 * 896 / TLBW;
      u32 z = 0;
      asm volatile("" : "+v"(z));
      if (i < NO) __hip_atomic_fetch_add((GAS u32*)(i < NW ? ws + i * TLBW + 64 : out + (i - NW) * TLBW), z, RLX_AGENT);
      else if (i <= NU) { z = *(const GAS u32*)(i < NU ? u + (i - NO) * TLBW : Dsk); asm volatile("" :: "v"(z)); }
    }
  } else {
    const int pi = bid * 256 + threadIdx.x;
    const u32 pA = __builtin_nontemporal_load(logA + pi), pI = __builtin_nontemporal_load(Aim + pi);
    const u32 pR = __builtin_nontemporal_load(Cre + pi), pC = __builtin_nontemporal_load(Cim + pi), ldt = logdt[bid];
    SBAR();
    role_a(lds, u, Dsk, wqkv, wo, out, ws, epoch, pA, pI, pR, pC, ldt);
  }
}

extern "C" void kernel_launch(void* const* d_in, const int* in_sizes, int n_in, void* d_out, int out_size, void* d_ws,
                              size_t ws_size, hipStream_t stream) {
  fused_kernel<<<512, 256, 0, stream>>>(d_in[0], d_in[1], d_in[2], d_in[3], d_in[4], d_in[5], d_in[6], d_in[7], d_in[8], d_in[9], d_out, d_ws);
}
```

```cpp
#include <hip/hip_runtime.h>
#include <stdint.h>

typedef unsigned u32;
typedef unsigned long long u64;
typedef short s16x4 __attribute__((ext_vector_type(4)));
typedef __bf16 bf16x8 __attribute__((ext_vector_type(8)));
typedef __bf16 bf16x2 __attribute__((ext_vector_type(2)));
typedef _Float16 f16x8 __attribute__((ext_vector_type(8)));
typedef _Float16 f16x2 __attribute__((ext_vector_type(2)));
typedef float f32x16 __attribute__((ext_vector_type(16)));
typedef float f32x4 __attribute__((ext_vector_type(4)));
typedef float f32x2 __attribute__((ext_vector_type(2)));
typedef u32 u32x4 __attribute__((ext_vector_type(4)));
typedef u32 u32x2 __attribute__((ext_vector_type(2)));
#define LAS __attribute__((address_space(3)))
#define GAS __attribute__((address_space(1)))
#define DI __device__ __forceinline__
#if defined(__HIP_DEVICE_COMPILE__)
#define NOPK __attribute__((target("no-packed-fp32-ops")))
#else
#define NOPK
#endif
#define RLX_AGENT __ATOMIC_RELAXED, __HIP_MEMORY_SCOPE_AGENT

constexpr int CODE_LINES = 212;
constexpr int WO_E = 4;

constexpr u32 WS_MB1 = 4096;
constexpr u32 WS_MB2 = WS_MB1 + 32768;
constexpr u32 WS_MBU = WS_MB2 + 32768;
constexpr u32 WS_MBY = WS_MBU + 57344;
constexpr u32 WS_KVP = 1u << 18;
constexpr u32 WS_OB = WS_KVP + (1u << 20);
constexpr u32 WS_UX = WS_OB + (2u << 20);
constexpr u32 WS_YX = WS_UX + (2u << 20);
constexpr u32 WS_WQ = WS_YX + (2u << 20);
constexpr u32 WS_WO = WS_WQ + (1u << 19);
constexpr u32 WS_MBQ = WS_WO + 458752;
constexpr u32 WS_END = WS_WO + (1u << 19);
constexpr int SPIN_CAP = 1 << 20;

DI float bflo(u32 v) { return __uint_as_float(v << 16); }
DI float bfhi(u32 v) { return __uint_as_float(v & 0xffff0000u); }
DI u32 pkbf(float a, float b) { f32x2 p = {a, b}; return __builtin_bit_cast(u32, __builtin_convertvector(p, bf16x2)); }
DI u32 pkh(f32x2 p) { return __builtin_bit_cast(u32, __builtin_convertvector(p, f16x2)); }
DI float rbf(float a) { return bflo(pkbf(a, a)); }
DI u32 umax3(u32 a, u32 b, u32 c) { u32 m = a > b ? a : b; return m > c ? m : c; }
DI u32 sum4(u32 m, u32x4 v) { return (m + v.x + v.y) + v.z + v.w; }
DI u32 row_sum(u32 v) {
  v += __builtin_amdgcn_update_dpp(0u, v, 0x111, 0xf, 0xf, true); v += __builtin_amdgcn_update_dpp(0u, v, 0x112, 0xf, 0xf, true);
  v += __builtin_amdgcn_update_dpp(0u, v, 0x114, 0xf, 0xf, true); v += __builtin_amdgcn_update_dpp(0u, v, 0x118, 0xf, 0xf, true);
  return v;
}
DI u32 row_sum2(u32 a, u32 b) {
  a += __builtin_amdgcn_update_dpp(0u, a, 0x111, 0xf, 0xf, true); b += __builtin_amdgcn_update_dpp(0u, b, 0x101, 0xf, 0xf, true);
  a += __builtin_amdgcn_update_dpp(0u, a, 0x112, 0xf, 0xf, true); b += __builtin_amdgcn_update_dpp(0u, b, 0x102, 0xf, 0xf, true);
  a += __builtin_amdgcn_update_dpp(0u, a, 0x114, 0xf, 0xf, true); b += __builtin_amdgcn_update_dpp(0u, b, 0x104, 0xf, 0xf, true);
  a += __builtin_amdgcn_update_dpp(0u, a, 0x118, 0xf, 0xf, true); b += __builtin_amdgcn_update_dpp(0u, b, 0x108, 0xf, 0xf, true);
  return (threadIdx.x & 8) ? a : b;
}
DI u32 wave_sum(u32 v) {
  v = row_sum(v);
  return (__builtin_amdgcn_readlane(v, 15) + __builtin_amdgcn_readlane(v, 31)) + (__builtin_amdgcn_readlane(v, 47) + __builtin_amdgcn_readlane(v, 63));
}
DI u32 wave_umax(u32 v) {
  u32 t;
  t = __builtin_amdgcn_update_dpp(0u, v, 0x111, 0xf, 0xf, false); v = v > t ? v : t;
  t = __builtin_amdgcn_update_dpp(0u, v, 0x112, 0xf, 0xf, false); v = v > t ? v : t;
  t = __builtin_amdgcn_update_dpp(0u, v, 0x114, 0xf, 0xf, false); v = v > t ? v : t;
  t = __builtin_amdgcn_update_dpp(0u, v, 0x118, 0xf, 0xf, false); v = v > t ? v : t;
  const u32 a = __builtin_amdgcn_readlane(v, 15), b = __builtin_amdgcn_readlane(v, 31), c = __builtin_amdgcn_readlane(v, 47), d = __builtin_amdgcn_readlane(v, 63);
  const u32 ab = a > b ? a : b, cd = c > d ? c : d;
  return ab > cd ? ab : cd;
}

DI u32 request_epoch(const char* ws) { u32 e; asm volatile("s_load_dword %0, %1, 0x0" : "=s"(e) : "s"(ws) : "memory"); return e; }
DI u32 take_tag(u32 e) { u32 r; asm volatile("s_waitcnt lgkmcnt(0)\n\ts_and_b32 %0, %1, -2" : "=s"(r) : "s"(e) : "memory", "scc"); return r * 1664525u + 2027808447u; }
DI u32 poll32(char* ws, u32 off, bool on) { u32 z = 0, r = 0; asm volatile("" : "+v"(z)); if (on) r = __hip_atomic_fetch_add((GAS u32*)(ws + off), z, RLX_AGENT); return r; }
DI u64 poll64(char* ws, u32 off, bool on) { u64 z = 0, r = 0; asm volatile("" : "+v"(z)); if (on) r = __hip_atomic_fetch_add((GAS u64*)(ws + off), z, RLX_AGENT); return r; }
#define SBAR() __builtin_amdgcn_sched_barrier(0)
#define PRIO(n) do { SBAR(); __builtin_amdgcn_s_setprio(n); SBAR(); } while (0)
#define PIN(x) asm volatile("" : "+v"(x))
DI f32x16 mfma32(u32x4 a, u32x4 b, f32x16 c) { return __builtin_amdgcn_mfma_f32_32x32x16_bf16(__builtin_bit_cast(bf16x8, a), __builtin_bit_cast(bf16x8, b), c, 0, 0, 0); }
DI f32x4 mfma16h(u32x4 a, u32x4 b, f32x4 c) { return __builtin_amdgcn_mfma_f32_16x16x32_f16(__builtin_bit_cast(f16x8, a), __builtin_bit_cast(f16x8, b), c, 0, 0, 0); }
DI f32x4 mfma16(u32x4 a, u32x4 b, f32x4 c) { return __builtin_amdgcn_mfma_f32_16x16x32_bf16(__builtin_bit_cast(bf16x8, a), __builtin_bit_cast(bf16x8, b), c, 0, 0, 0); }
DI u32x2 tr_read(LAS char* p) { return __builtin_bit_cast(u32x2, __builtin_amdgcn_ds_read_tr16_b64_v4i16((LAS s16x4*)p)); }
#define RSRC(p, n) __builtin_amdgcn_make_buffer_rsrc((void*)(p), 0, (n), 0x00020000)
#define DMA16(rs, voff, soff, dst, aux) __builtin_amdgcn_raw_ptr_buffer_load_lds((rs), (LAS void*)(dst), 16, (voff), (soff), 0, (aux))
#define WAITVM(n) do { asm volatile("" ::: "memory"); __builtin_amdgcn_s_waitcnt(0x0F70 | ((n) & 15) | (((n) >> 4) << 14)); asm volatile("" ::: "memory"); } while (0)
#define VMCNT(n) asm volatile("s_waitcnt vmcnt(" #n ")" ::: "memory")
#define BAR() do { asm volatile("s_waitcnt lgkmcnt(0)" ::: "memory"); __builtin_amdgcn_s_barrier(); asm volatile("" ::: "memory"); } while (0)
#define ST_SC1(v, rs, off) __builtin_amdgcn_raw_buffer_store_b128((v), (rs), (off), 0, 16)
#define LD_SC1(rs, off) __builtin_amdgcn_raw_buffer_load_b128((rs), (off), 0, 16)
#define LDS_SC1(rs, off, soff) __builtin_amdgcn_raw_buffer_load_b128((rs), (off), (soff), 16)
#define ST1_SC1(v, rs, off) __builtin_amdgcn_raw_buffer_store_b32((v), (rs), (off), 0, 16)

typedef unsigned short u16x2 __attribute__((ext_vector_type(2)));
DI u32 phi1(float a, float b) {
  const u32 t = pkbf(a, b);
  const f32x2 x = {bflo(t), bfhi(t)};
  const f32x2 y = x * 1.4426950408889634f;
  const float e0 = __builtin_amdgcn_fmed3f(__builtin_amdgcn_exp2f(y.x), 0.f, 1.f);
  const float e1 = __builtin_amdgcn_fmed3f(__builtin_amdgcn_exp2f(y.y), 0.f, 1.f);
  const f32x2 x1 = x + 1.0f;
  return pkbf(__builtin_fmaxf(x1.x, e0), __builtin_fmaxf(x1.y, e1));
}
DI u32 pkmin(u32 a, u32 b) { return __builtin_bit_cast(u32, __builtin_elementwise_min(__builtin_bit_cast(u16x2, a), __builtin_bit_cast(u16x2, b))); }
DI void phi_eps(u32x4 (&p)[2], u32 mn) {
  if (__any((mn & 0xffffu) < 0x3A80u || mn < 0x3A800000u)) {
#pragma unroll
    for (int j = 0; j < 8; ++j) { const u32 r = p[j >> 2][j & 3]; p[j >> 2][j & 3] = pkbf(bflo(r) + 1e-6f, bfhi(r) + 1e-6f); }
  }
}

template <bool HEAD>
DI void role_b(LAS char* lds, const char* __restrict__ x, const char* __restrict__ wqkv, char* __restrict__ out, char* ws, u32 epoch) {
  const int b = blockIdx.x - 256, tid = threadIdx.x, lane = tid & 63;
  const int w = __builtin_amdgcn_readfirstlane(tid >> 6);
  const int rb = 4 * ((b & 7) >> 1) + (b >> 6), h = (b & 1) * 8 + ((b >> 3) & 7);
  const __amdgpu_buffer_rsrc_t rs = RSRC(ws, WS_END), rx = RSRC(x, 2048 * 896), rw = RSRC(wqkv, 448 * 2688);
  constexpr int WR = 49152, KVI = WR + 4096, OST = 16384, OTR = WR;
#define XS(i) ((i) * 16384)
#define QS(cc) ((cc) < 2 ? WR + 24576 + (cc) * 4096 : (cc) < 6 ? XS(1) + ((cc) - 2) * 1024 : WR)
#define QD(cc) ((cc) >= 2 && (cc) < 6 ? 4096 : 1024)
  const int wsb = (56 * h) & ~15;
  const int coff = (h & 1) * 4;
  const int wone = (h & 1) ? 0 : 28;
  const u32 xv0 = (128 * rb + 32 * w + (lane >> 3)) * 896 + (((lane & 7) ^ (lane >> 4)) << 4);
  const u32 xv1 = (xv0 ^ 64) + 8 * 896;
  const u32 wv = (16 * w + (lane >> 2)) * 2688 + wsb + (lane & 3) * 16;
  const u32 l16 = lane * 16;
  int ch_[7];
  auto chunk = [&](int cc) { return ch_[cc]; };
  const int gi = h + rb;
  const u32 ordc = (gi & 2) ? ((gi & 1) ? 0x4652103u : 0x5461032u) : ((gi & 1) ? 0x4650321u : 0x6543210u);
  auto next_chunk = [&](int cc) { int c = (ordc >> (4 * cc)) & 7; asm volatile("" : "+s"(c)); ch_[cc] = c; };
  auto issue_x = [&](int cc, int i) { DMA16(rx, (i & 1) ? xv1 : xv0, chunk(cc) * 128 + (i >> 1) * (16 * 896), lds + XS(cc % 3) + w * 4096 + i * 1024, 0); };
  auto issue_w = [&](int cc, int i) { DMA16(rw, wv, chunk(cc) * (64 * 2688) + 896 * (i + 1), lds + WR + (cc & 3) * 8192 + i * 4096 + w * 1024, 0); };
  bool qok = true;
  int spin = SPIN_CAP;
  u32 qso[7];
  auto issue_q = [&](int cc) { DMA16(rs, l16, qok ? qso[cc] : WS_END, lds + QS(cc) + w * QD(cc), 16); };
#pragma unroll
  for (int cc = 0; cc < 2; ++cc) {
    next_chunk(cc);
    if (HEAD == (cc == 0)) { issue_w(cc, 0); issue_w(cc, 1); issue_x(cc, 0); issue_x(cc, 1); issue_x(cc, 2); issue_x(cc, 3); }
    SBAR();
  }
  if (HEAD) return;
  next_chunk(2); next_chunk(3); next_chunk(4); next_chunk(5); next_chunk(6);
  __builtin_amdgcn_s_setprio(2);
  const int r32 = lane & 31, half = lane >> 5;
  const int xrd = w * 4096 + r32 * 128 + ((half ^ ((r32 >> 1) & 7)) << 4);
  LAS char* whi = lds + WR + (8 * half + ((lane >> 2) & 3)) * 64 + ((lane >> 4) & 1) * 32 + (lane & 3) * 8;
  PIN(whi);
  const int rt = h >> 2, ct = (h + ((h & 8) ? 2 : 1)) & 3;
  u32 wo_src = WS_WO + (4 * ct + w < 14 ? 4 * ct + w : 13) * 32768, kvp_src = WS_KVP + h * 65536 + w * 16384;
  u32 ob_dst = WS_OB + ((rb * 4 + w) * 16 + h) * 2048 + l16, ob_src = WS_OB + ((rb * 4 + rt) * 16 + 4 * w) * 2048;
  asm volatile("" : "+s"(wo_src), "+s"(kvp_src), "+s"(ob_src));
  PIN(ob_dst);
  u32 tell1 = lane < 4 ? WS_MB1 + (h * 16 + 4 * w + lane) * 128 + rb * 4 : WS_END;
  u32 tell2 = lane < 4 ? WS_MB2 + (rb * 16 + 4 * w + lane) * 128 + h * 4 : WS_END;
  u32 mb1 = WS_MB1 + (h * 16 + rb) * 128 + ((lane + rb) & 15) * 4, mb2 = WS_MB2 + (rb * 16 + h) * 128 + ((4 * w + lane) & 15) * 4;
  u32 kvp_dst = kvp_src + rb * 1024 + l16, kvs[16];
#pragma unroll
  for (int p = 1; p < 16; ++p) { kvs[p] = kvp_src + ((p + rb) & 15) * 1024; asm volatile("" : "+s"(kvs[p])); }
  const int dw = 8 * w + 4 * half - coff;
  u32 rowmask = dw < 0 || dw >= 28 ? 0u : ~0u;
  const bool padlo = (h & 1) && !half, padhi = !(h & 1) && half;
  const int oi = (r32 & 3) + 4 * (r32 >> 3);
  LAS char* kvrd = lds + KVI + ((oi < 14 ? 14 * ((r32 >> 2) & 1) + oi + coff : wone) + 32 * half) * 32;
  LAS char* qlo = lds + l16; LAS char* qhi = lds + WR + l16;
  PIN(tell1); PIN(tell2); PIN(mb1); PIN(mb2); PIN(kvp_dst); PIN(rowmask); PIN(kvrd); PIN(qhi);
  const bool ck = lane == wone + 1;
  u32 mbq = WS_MBQ + (h * 16 + rb) * 128 + (4 * (lane & 7) + w) * 4, mail = 0, tag = 0;
  PIN(mbq);
  u32x4 xf[28], wf[2][8];
  f32x16 ak = {}, av = {};
  auto rd = [&](int cc, int i) {
    if ((i & 1) == 0) xf[cc * 4 + (i >> 1)] = *(LAS u32x4*)(lds + XS(cc % 3) + (xrd ^ ((i >> 1) << 5)));
    LAS char* p = whi + (cc & 3) * 8192 + (i >> 1) * 1024 + (i & 1) * 4096;
    const u32x2 lo = tr_read(p), hi = tr_read(p + 256);
    wf[cc & 1][i] = (u32x4){lo.x, lo.y, hi.x, hi.y};
  };
#pragma unroll
  for (int cc = 0; cc < 6; ++cc) {
    if (cc == 0) VMCNT(6); else if (cc < 5) VMCNT(8); else VMCNT(7);
    BAR();
    if (cc == 0) { tag = take_tag(epoch); }
    if (cc == 5) {
#pragma unroll
      for (int i = 0; i < 8; ++i) { rd(cc, i); if (i < 7) { qso[i] = WS_WQ + (h * 28 + 4 * chunk(i) + w) * 1024; asm volatile("" : "+s"(qso[i])); } }
      SBAR();
    }
#pragma unroll
    for (int i = 0; i < 8; ++i) {
      if (cc > 0) {
        if (i & 1) av = mfma32(xf[(cc - 1) * 4 + (i >> 1)], wf[(cc - 1) & 1][i], av);
        else ak = mfma32(xf[(cc - 1) * 4 + (i >> 1)], wf[(cc - 1) & 1][i], ak);
      }
      if (cc < 5) rd(cc, i);
      if (cc == 0) { if (i < 2) issue_w(2, i); else if (i < 6) issue_x(2, i - 2); else issue_w(3, i - 6); }
      else if (cc <= 4 && i < 4) issue_x(cc + 2, i);
      else if (cc <= 3 && i < 6) issue_w(cc + 3, i - 4);
      else if (cc == 4 && i == 4) mail = poll32(ws, mbq, lane < 7);
      SBAR();
    }
#pragma unroll
    for (int s = 3; s >= 0; --s) { PIN(wf[cc & 1][2 * s + 1]); PIN(wf[cc & 1][2 * s]); PIN(xf[cc * 4 + s]); }
    if (cc > 0) { PIN(ak); PIN(av); }
  }
  VMCNT(1);
  BAR();
  rd(6, 0); rd(6, 2); rd(6, 4); rd(6, 6);
  SBAR();
#pragma unroll
  for (int s = 0; s < 4; ++s) ak = mfma32(xf[20 + s], wf[1][2 * s], ak);
  PIN(ak);
#pragma unroll
  for (int s = 3; s >= 0; --s) { PIN(wf[0][2 * s]); PIN(xf[24 + s]); }
  rd(6, 1); rd(6, 3); rd(6, 5); rd(6, 7);
  SBAR();
#pragma unroll
  for (int s = 0; s < 4; ++s) ak = mfma32(xf[24 + s], wf[0][2 * s], ak);
  PIN(ak);
  SBAR();
  WAITVM(0);
  PIN(mail);
  qok = __ballot(mail == tag) == 0x7full;
  u32x4 wof[32], d[16], pq[2];
  auto load_wo = [&](int k) { wof[k] = __builtin_amdgcn_raw_buffer_load_b128(rs, l16 + (k & 3) * 1024, wo_src + (k >> 2) * 4096, 16); };
  {
    u32x4 pk[2], pv[2];
    u32 mn = ~0u;
#pragma unroll
    for (int j = 0; j < 8; j += 2) {
      av = mfma32(xf[20 + j], wf[j < 4][2 * (j & 3) + 1], av);
      issue_q(j); if (j < 6) issue_q(j + 1);
      SBAR();
      pk[j >> 2][j & 3] = phi1(ak[2 * j], ak[2 * j + 1]);
      av = mfma32(xf[21 + j], wf[j < 4][2 * (j & 3) + 3], av);
      pk[j >> 2][(j & 3) + 1] = phi1(ak[2 * j + 2], ak[2 * j + 3]);
      mn = pkmin(pkmin(mn, pk[j >> 2][j & 3]), pk[j >> 2][(j & 3) + 1]);
      PIN(mn); PIN(av);
      SBAR();
    }
    phi_eps(pk, mn);
#pragma unroll
    for (int j = 0; j < 8; ++j) { const u32 v = pkbf(av[2 * j], av[2 * j + 1]); pv[j >> 2][j & 3] = r32 == wone ? 0x3F803F80u : v; }
    f32x16 kv = {};
    kv = mfma32(pk[0], pv[0], kv); kv = mfma32(pk[1], pv[1], kv);
    ST1_SC1(tag, rs, tell1);
#pragma unroll
    for (int g = 0; g < 4; ++g) *(LAS f32x4*)(lds + XS(2) + w * 4096 + g * 1024 + l16) = (f32x4){kv[4 * g], kv[4 * g + 1], kv[4 * g + 2], kv[4 * g + 3]};
    BAR();
    f32x4 s4 = *(LAS f32x4*)(lds + XS(2) + tid * 16);
#pragma unroll
    for (int ww = 1; ww < 4; ++ww) s4 += *(LAS f32x4*)(lds + XS(2) + ww * 4096 + tid * 16);
    u32x4 v = __builtin_bit_cast(u32x4, s4);
    const u32 part = v.y + v.z + v.w;
    const u32 c = tag - wave_sum(ck ? part : part + v.x);
    d[0] = v;
    if (ck) v.x = c;
    v.y += tag;
    ST_SC1(v, rs, kvp_dst);
  }
  if (!qok) {
#pragma nounroll
    while (__ballot(poll32(ws, mbq, lane < 7) == tag) != 0x7full && --spin > 0) {}
    qok = true;
#pragma unroll
    for (int cc = 0; cc < 7; ++cc) issue_q(cc);
    WAITVM(0);
  }
  u32 go, need = 0xfffeu;
  mail = poll32(ws, mb1, lane < 16);
#pragma unroll
  for (int k = 0; k < WO_E; ++k) load_wo(k);
  WAITVM(WO_E + 3);
  BAR();
#define QP(cc) (QS(cc) >= WR ? qhi + (QS(cc) - WR) : qlo + QS(cc))
  auto load_kv = [&](int p, bool on = true) { d[p] = __builtin_amdgcn_raw_buffer_load_b128(rs, l16, on ? kvs[p] : WS_END, 16); };
  {
    f32x16 aq = {};
    u32x4 qf[2][4];
    u32 qsum = 0;
    auto read_q = [&](int cc) {
#pragma unroll
      for (int s = 0; s < 4; ++s) qf[cc & 1][s] = *(LAS u32x4*)(QP(cc) + s * QD(cc));
      SBAR();
    };
    read_q(0);
#pragma unroll
    for (int cc = 0; cc < 7; ++cc) {
      if (cc < 6) read_q(cc + 1);
#pragma unroll
      for (int s = 0; s < 4; ++s) {
        aq = mfma32(qf[cc & 1][s], xf[cc * 4 + s], aq);
        qsum = sum4(qsum, qf[cc & 1][s]);
        if (s & 1) load_wo(WO_E + 2 * cc + (s >> 1));
        PIN(aq); PIN(qsum);
        SBAR();
      }
    }
    if (wave_sum(qsum) != 28u * tag) {
      aq = (f32x16){};
#pragma nounroll
      for (int k2 = 0; k2 < 28; ++k2) {
        const int ks = 4 * ((ordc >> (k2 & 28)) & 7) + (k2 & 3);
        u32x4 f;
#pragma nounroll
        for (;;) {
          asm volatile("" ::: "memory");
          f = LDS_SC1(rs, l16, WS_WQ + (h * 28 + ks) * 1024);
          if (wave_sum(sum4(0u, f)) == tag || --spin <= 0) break;
        }
        aq = mfma32(f, __builtin_amdgcn_raw_buffer_load_b128(rx, (128 * rb + 32 * w + r32) * 896 + half * 16, ks * 32, 0), aq);
      }
    }
    go = need & (u32)__ballot(mail == tag); need &= ~go;
    mail = poll32(ws, mb1, lane < 16 && need != 0);
    u32 mn = ~0u;
#pragma unroll
    for (int j = 0; j < 8; ++j) {
      if (j) load_kv(2 * j, go >> (2 * j) & 1);
      load_kv(2 * j + 1, go >> (2 * j + 1) & 1);
      u32 r = phi1(aq[2 * j], aq[2 * j + 1]);
      if (j < 2) r = padlo ? 0x3F803F80u : r; else if (j >= 6) r = padhi ? 0x3F803F80u : r;
      pq[j >> 2][j & 3] = r;
      mn = pkmin(mn, r);
      PIN(mn);
      SBAR();
    }
    phi_eps(pq, mn);
    PIN(pq[0]); PIN(pq[1]);
  }
  PIN(mail);
#pragma nounroll
  while (need != 0 && --spin > 0) {
    go = need & (u32)__ballot(mail == tag); need &= ~go;
#pragma unroll
    for (int p = 1; p < 16; ++p) if (go >> p & 1) load_kv(p);
    if (need != 0) { mail = poll32(ws, mb1, lane < 16); WAITVM(0); }
  }
#pragma unroll
  for (int k = WO_E + 14; k < 32; ++k) load_wo(k);
  WAITVM(18 - WO_E);
#pragma nounroll
  for (;;) {
    u32 m = 0;
#pragma unroll
    for (int p = 1; p < 16; ++p) m = sum4(m, d[p]);
    if (wave_sum(m) == 15u * 65u * tag || --spin <= 0) break;
    asm volatile("" ::: "memory");
#pragma unroll
    for (int p = 1; p < 16; ++p) load_kv(p);
    WAITVM(0);
  }
  mail = poll32(ws, mb2, lane < 4);
  SBAR();
  {
    f32x4 s4 = __builtin_bit_cast(f32x4, d[0]);
#pragma unroll
    for (int p = 1; p < 16; ++p) { d[p].y -= tag; s4 += __builtin_bit_cast(f32x4, d[p]); }
    *(LAS u32x2*)(lds + KVI + lane * 32 + w * 8) = (u32x2){pkbf(s4.x, s4.y) & rowmask, pkbf(s4.z, s4.w) & rowmask};
  }
  BAR();
  {
    const u32x4 a0 = *(LAS u32x4*)kvrd, a1 = *(LAS u32x4*)(kvrd + 16);
    f32x16 nm = {};
    nm = mfma32(a0, pq[0], nm); nm = mfma32(a1, pq[1], nm);
    const float den = rbf(rbf(nm[14]) + 1e-6f);
    const float rc = __builtin_amdgcn_rcpf(den);
    u32 o[8];
#pragma unroll
    for (int j = 0; j < 7; ++j) { const u32 t = pkbf(nm[2 * j], nm[2 * j + 1]); o[j] = pkbf(bflo(t) * rc, bfhi(t) * rc); }
    o[7] = (tag - ((o[0] + o[1] + o[2]) + (o[3] + o[4] + o[5]) + o[6])) & ~0x40004000u;
    __builtin_amdgcn_raw_buffer_store_b128((u32x4){o[0] + tag, o[1], o[2], o[3]}, rs, ob_dst, 0, 16);
    __builtin_amdgcn_raw_buffer_store_b128((u32x4){o[4], o[5], o[6], o[7]}, rs, ob_dst + 1024, 0, 16);
    ST1_SC1(tag, rs, tell2);
    SBAR();
  }
  auto load_o = [&](int i) {
    DMA16(rs, l16, ob_src + i * 2048, lds + OST + (8 * w + 2 * i) * 1024, 16);
    DMA16(rs, l16, ob_src + i * 2048 + 1024, lds + OST + (8 * w + 2 * i + 1) * 1024, 16);
  };
  WAITVM(3);
  PIN(mail);
  need = 15u;
  bool wo_done = false;
#pragma nounroll
  for (;;) {
    go = need & (u32)__ballot(mail == tag); need &= ~go;
#pragma unroll
    for (int i = 0; i < 4; ++i) if (go >> i & 1) load_o(i);
    mail = poll32(ws, mb2, lane < 4 && need != 0);
    if (!wo_done) {
#pragma nounroll
      for (;;) {
        u32 m = 0;
#pragma unroll
        for (int k = 0; k < 32; ++k) m = sum4(m, wof[k]);
        if (__all(m == 32u * tag) || --spin <= 0) break;
        asm volatile("" ::: "memory");
#pragma unroll
        for (int k = 0; k < 32; ++k) load_wo(k);
        WAITVM(0);
      }
#pragma unroll
      for (int k = 0; k < 32; k += 2) { wof[k].x -= tag; wof[k + 1].w = 0u; PIN(wof[k]); PIN(wof[k + 1]); }
      wo_done = true;
    }
    WAITVM(0);
    if (need == 0 || --spin <= 0) break;
  }
  f32x16 acc;
#pragma nounroll
  for (int it = 0; it < 4096; ++it) {
    BAR();
    u32x4 of[32];
    u32 m = 0;
#pragma unroll
    for (int k = 0; k < 6; ++k) of[k] = *(LAS u32x4*)(lds + OST + k * 1024 + l16);
    acc = (f32x16){};
    SBAR();
#pragma unroll
    for (int k = 0; k < 32; ++k) {
      if (k == 0) of[0].x -= tag;
      if (k & 1 && k < 31) { of[k + 1].x -= tag; PIN(of[k + 1]); }
      acc = mfma32(wof[k], of[k], acc);
      if (k + 6 < 32) of[k + 6] = *(LAS u32x4*)(lds + OST + (k + 6) * 1024 + l16);
      m = sum4(m, of[k]);
      PIN(m);
      SBAR();
    }
    if (__all(((16u * tag - m) & 0x3FF83FFFu) == 0u)) break;
    BAR();
    load_o(0); load_o(1); load_o(2); load_o(3);
    WAITVM(0);
  }
  {
    LAS char* p = lds + OTR + r32 * 272 + w * 64 + half * 32;
    *(LAS u32x4*)p = (u32x4){pkbf(acc[0], acc[1]), pkbf(acc[2], acc[3]), pkbf(acc[4], acc[5]), pkbf(acc[6], acc[7])};
    *(LAS u32x4*)(p + 16) = (u32x4){pkbf(acc[8], acc[9]), pkbf(acc[10], acc[11]), pkbf(acc[12], acc[13]), pkbf(acc[14], acc[15])};
  }
  BAR();
  if (ct < 3 || (tid & 15) < 8) {
#pragma unroll
    for (int i = 0; i < 2; ++i)
      __builtin_amdgcn_raw_buffer_store_b128(*(LAS u32x4*)(lds + OTR + (16 * i + (tid >> 4)) * 272 + (tid & 15) * 16), RSRC(out, 2048 * 1792),
                                             (128 * rb + 32 * rt + 16 * i + (tid >> 4)) * 1792 + 896 + ct * 256 + (tid & 15) * 16, 0, 16);
  }
  if (b == 255 && tid == 0) __hip_atomic_store((GAS u32*)ws, tag, RLX_AGENT);
}

DI u32 hmul1(u32 a, u32 b) { const f16x2 x = __builtin_bit_cast(f16x2, a), y = __builtin_bit_cast(f16x2, b); return __builtin_bit_cast(u32, (f16x2){x.x, x.x} * y); }
DI u32 hmul2(u32 a, u32 b, u32 t) {
  const f16x2 x = __builtin_bit_cast(f16x2, a), y = __builtin_bit_cast(f16x2, b);
  return __builtin_bit_cast(u32, __builtin_elementwise_fma((f16x2){x.y, x.y}, (f16x2){-y.y, y.x}, __builtin_bit_cast(f16x2, t)));
}
DI u32 hmul(u32 a, u32 b) { return hmul2(a, b, hmul1(a, b)); }
DI f32x2 csq(f32x2 a) { return (f32x2){__builtin_fmaf(a.x, a.x, -a.y * a.y), (a.x + a.x) * a.y}; }
DI float exp_acc(float x) {
  const float hi = x * 1.4426950408889634f, lo = __builtin_fmaf(x, 1.9259629911266175e-8f, __builtin_fmaf(x, 1.4426950408889634f, -hi));
  const float e = __builtin_amdgcn_exp2f(hi);
  return __builtin_fmaf(e, lo * 0.6931471805599453f, e);
}
DI f32x2 cexp_p(float a2, float f, float p) {
  const float m = __builtin_amdgcn_exp2f(a2 * p), ph = __builtin_amdgcn_fractf(f * p);
  return (f32x2){m, m} * (f32x2){__builtin_amdgcn_cosf(ph), __builtin_amdgcn_sinf(ph)};
}
DI void role_a(LAS char* lds, const char* __restrict__ u, const char* __restrict__ Dsk, const char* __restrict__ wqkv, const char* __restrict__ wo, char* __restrict__ out, char* ws, u32 epoch,
               u32 pA, u32 pI, u32 pR, u32 pC, u32 ldt) {
  const int a = blockIdx.x, cb = a >> 5, jp = a & 31, tid = threadIdx.x, lane = tid & 63;
  const int w = __builtin_amdgcn_readfirstlane(tid >> 6);
  __builtin_amdgcn_s_setprio(3);
  constexpr int SROW = 528, STG = 32 * SROW, UC = 0, UCS = 4160, RV = 36864, RVS = 4608, EX = RV + 2 * RVS, UIM = 4 * STG;
  const int PKS = UIM + 8192 + (w ? 1024 + w * 1024 : 0);
  const __amdgpu_buffer_rsrc_t rs = RSRC(ws, WS_END);
  const int ch = w >> 1, hf = w & 1;
  int spin = SPIN_CAP;
  const int r32 = lane & 31, half = lane >> 5;
  const int pkh_ = w == 0 ? (a & 15) : 2 * (a & 7) + (w - 1);
  {
    const __amdgpu_buffer_rsrc_t rp = RSRC(w == 0 ? wo : wqkv, w == 0 ? 448 * 896 : w < 3 ? 448 * 2688 : 0);
    const u32 vo = (lane >> 2) * (w == 0 ? 896 : 2688) + (lane & 3) * 16;
    const u32 so = w == 0 ? 28 * 896 * pkh_ + (a >> 4) * 64 : 16 * 2688 * (a >> 3) + ((56 * pkh_) & ~15);
    DMA16(rp, vo, so, lds + PKS, 0);
    DMA16(rp, vo, w == 0 ? so + 16 * 896 : 448 * 2688, lds + (w == 0 ? PKS + 1024 : UIM + 8192 + 5120), 0);
  }
  u32x4 dk;
  auto load_tile = [&]() {
#pragma unroll
    for (int i = 0; i < 2; ++i)
      DMA16(RSRC(u, 2048 * 896), (tid >> 3) * 896 + (((tid & 7) ^ ((tid >> 5) & 7)) << 4), (64 * jp + 32 * i) * 896 + cb * 128, lds + UIM + (32 * i + 8 * w) * 128, 2);
    dk = *(const GAS u32x4*)(Dsk + cb * 128 + (tid & 7) * 16);
  };
  PRIO(0);
  int tix[8];
#pragma unroll
  for (int j = 0; j < 8; ++j) {
    const int p = (int)rbf((float)(2047 - 8 * tid - j));
    const int low = p & 7, blk = p >> 8;
    const int k4 = (blk < 4 ? blk : 4) * 4;
    const int sh = (0x32210 >> k4) & 15;
    const int base = blk + ((0xCBA70 >> k4) & 15);
    tix[j] = ((base + (low >> sh)) * 32 + ((p >> 3) & 31)) * 4;
    PIN(tix[j]);
  }
  const u32 tag = take_tag(epoch);
  auto publish_weights = [&]() {
  PRIO(3);
  BAR();
  if (w == 0) {
    const int q = (lane >> 2) & 3, gam = 4 * ((lane >> 4) & 1) + (lane & 3), ng = 4 * (gam & 1) + (gam >> 1);
    LAS char* p = lds + PKS + (14 * half + q) * 64 + 8 * ng;
    const u32x2 r0 = tr_read(p), r1 = tr_read(p + 256), r2 = tr_read(p + 512), r3 = tr_read(p + 768);
    const u32 off = WS_WO + ((a >> 4) * 16 + pkh_) * 2048 + lane * 16;
    ST_SC1(((u32x4){r0.x + tag, r0.y, r1.x, r1.y}), rs, off);
    ST_SC1(((u32x4){r2.x, r2.y, r3.x, tag - ((r0.x + r0.y + r1.x) + (r1.y + r2.x + r2.y) + r3.x)}), rs, off + 1024);
  } else if (w < 3) {
    LAS char* p = lds + PKS + (8 * half + ((lane >> 2) & 3)) * 64 + ((lane >> 4) & 1) * 32 + (lane & 3) * 8;
    const u32x2 r0 = tr_read(p), r1 = tr_read(p + 256);
    u32x4 v = {r0.x, r0.y, r1.x, r1.y};
    const bool ck = lane == ((pkh_ & 1) ? 1 : 29);
    if (ck) v = (u32x4){0u, 0u, 0u, 0u};
    const u32 c = tag - wave_sum(sum4(0u, v));
    if (ck) v.x = c;
    ST_SC1(v, rs, WS_WQ + (pkh_ * 28 + (a >> 3)) * 1024 + lane * 16);
  }
  PRIO(0);
  };
  auto publish_u = [&]() {
  PRIO(3);
  BAR();
  {
    const int n = tid & 15;
    u32x4 v[2];
#pragma unroll
    for (int i = 0; i < 2; ++i) {
      const int jj = 16 * i + (tid >> 4);
      LAS char* p = lds + UIM + 4 * n * 128 + (((jj >> 2) ^ (n & 7)) << 4) + (jj & 3) * 4;
      v[i] = (u32x4){*(LAS u32*)p, *(LAS u32*)(p + 128), *(LAS u32*)(p + 256), *(LAS u32*)(p + 384)};
    }
    const u32 off = WS_UX + (cb * 32 + (tid >> 4)) * 8192 + jp * 256 + n * 16;
    ST_SC1(((u32x4){v[0].x, v[0].y + tag, v[0].z, v[0].w}), rs, off); ST_SC1(((u32x4){v[1].x, v[1].y + tag, v[1].z, v[1].w}), rs, off + 16 * 8192);
    const u32 ck = tag - row_sum2(sum4(0u, v[0]), sum4(0u, v[1]));
    __builtin_amdgcn_raw_buffer_store_b64((u32x2){tag, ck}, rs, n == 15 || n == 0 ? WS_MBU + (cb * 32 + (tid >> 4) + (n ? 0 : 16)) * 256 + jp * 8 : WS_END, 0, 16);
  }
  PRIO(0);
  };
  WAITVM(0);
  publish_weights();
  PIN(pA); PIN(pI); PIN(pR); PIN(pC); PIN(ldt);
  const float dt = rbf(exp_acc(ch ? bfhi(ldt) : bflo(ldt)));
  const u32 mx = wave_umax(umax3(umax3(pR & 0x7fffu, (pR >> 16) & 0x7fffu, pC & 0x7fffu), (pC >> 16) & 0x7fffu, 0u));
  int ex = mx >> 7; ex = ex < 13 ? 13 : ex > 254 ? 254 : ex;
  const float sc = __uint_as_float((u32)(266 - ex) << 23), isc = __uint_as_float((u32)(ex - 12) << 23);
  LAS char* stg = lds + w * STG;
  u32x4 ud[3];
  const __amdgpu_buffer_rsrc_t ru = RSRC(ws + WS_UX + a * 8192, 8192);
  const int npc = w < 3 ? 9 : 8;
  u64 mail = 0;
  const u32 mbu = WS_MBU + a * 256 + (8 * w + lane) * 8;
  {
    u32x2 c0, y1, y2, y4, q1, q2, q4, q8, z8, z16, z32, z64, z128;
#pragma unroll
    for (int m = 0; m < 2; ++m) {
      const float la = m ? bfhi(pA) : bflo(pA), ai = m ? bfhi(pI) : bflo(pI), cr = m ? bfhi(pR) : bflo(pR), ci = m ? bfhi(pC) : bflo(pC);
      const float a2 = -rbf(exp_acc(la)) * dt * 1.4426950408889634f, f = ai * dt * 0.15915494309189535f;
      f32x2 a = cexp_p(a2, -f, 1.f), b = cexp_p(a2, -f, 256.f), c = cexp_p(a2, f, 8.f);
      c0[m] = pkh((f32x2){cr * sc, -ci * sc});
      y1[m] = pkh(a); a = csq(a); y2[m] = pkh(a); a = csq(a); y4[m] = pkh(a);
      q1[m] = pkh(b); b = csq(b); q2[m] = pkh(b); b = csq(b); q4[m] = pkh(b); b = csq(b); q8[m] = pkh(b);
      z8[m] = pkh(c); c = csq(c); z16[m] = pkh(c); c = csq(c); z32[m] = pkh(c); c = csq(c); z64[m] = pkh(c); c = csq(c); z128[m] = pkh(c);
    }
#define OPQ(v) asm("" : "+v"(v))
    OPQ(c0); OPQ(y1); OPQ(y2); OPQ(y4); OPQ(q1); OPQ(q2); OPQ(q4); OPQ(q8); OPQ(z8); OPQ(z16); OPQ(z32); OPQ(z64); OPQ(z128);
    LAS char* sw = stg + lane * 8;
    auto put = [&](int r, u32x2 v) { *(LAS u32x2*)(sw + r * SROW) = v; };
    auto mul = [&](u32x2 x, u32x2 y) { const u32 x0 = x.x, x1 = x.y, y0 = y.x, y1_ = y.y; u32x2 r = {hmul(x0, y0), hmul(x1, y1_)}; OPQ(r); return r; };
    put(0, c0); put(21, (u32x2){0x3C00u, 0x3C00u}); put(22, z8); put(23, z16); put(25, z32); put(26, z64); put(28, z128);
    const u32x2 c2 = mul(c0, y2), c4 = mul(c0, y4), c512 = mul(c0, q2), c1024 = mul(c0, q4), z96 = mul(z32, z64);
    put(2, c2); put(4, c4); put(12, c512); put(16, c1024); put(27, z96);
    const u32x2 c6 = mul(c4, y2), c516 = mul(c4, q2), c1536 = mul(c1024, q2);
    put(6, c6); put(13, c516); put(18, c1536);
    put(1, mul(c0, y1)); put(3, mul(c2, y1)); put(5, mul(c4, y1)); put(7, mul(c6, y1));
    put(8, mul(c0, q1)); put(9, mul(c2, q1)); put(10, mul(c4, q1)); put(11, mul(c6, q1));
    put(14, mul(c512, q1)); put(15, mul(c516, q1)); put(17, mul(c1024, q1)); put(19, mul(c1536, q1)); put(20, mul(c0, q8));
    put(24, mul(z8, z16)); put(29, mul(z32, z128)); put(30, mul(z64, z128)); put(31, mul(z96, z128));
  }
  PRIO(3);
  load_tile();
  WAITVM(3);
  ST1_SC1(tag, rs, lane < 16 && (w == 1 || w == 2) ? WS_MBQ + (pkh_ * 16 + lane) * 128 + (a >> 3) * 4 : WS_END);
  PRIO(0);
  {
    const int c16 = lane & 15, quad = lane >> 4;
    LAS char* py0 = stg + c16 * SROW + quad * 16;
    LAS char* py1 = stg + (16 + (c16 < 4 ? c16 : 4)) * SROW + quad * 16;
    LAS char* pt1 = stg + (21 + (c16 & 3)) * SROW + quad * 16;
    LAS char* pt2 = stg + (c16 < 4 ? 21 : 24 + (c16 >> 2)) * SROW + quad * 16;
    LAS char* pt3 = stg + (28 + (c16 >> 2)) * SROW + quad * 16;
    f32x4 ka[4] = {};
    u32x4 ft[2][3], fy[2][2], xx[2][2];
    auto rdt = [&](int s) { ft[s & 1][0] = *(LAS u32x4*)(pt1 + s * 64); ft[s & 1][1] = *(LAS u32x4*)(pt2 + s * 64); ft[s & 1][2] = *(LAS u32x4*)(pt3 + s * 64); };
    auto rdy = [&](int s) { fy[s & 1][0] = *(LAS u32x4*)(py0 + s * 64); fy[s & 1][1] = *(LAS u32x4*)(py1 + s * 64); };
    auto prod = [&](int s, int i) {
      u32 t1 = ft[s & 1][0][i], t2 = ft[s & 1][1][i], t3 = ft[s & 1][2][i];
      asm("" : "+v"(t2), "+v"(t3));
      const u32 p0 = hmul1(t1, t2), p1 = hmul1(t1, t3);
      xx[s & 1][0][i] = hmul2(t1, t2, p0); xx[s & 1][1][i] = hmul2(t1, t3, p1);
    };
    rdt(0); rdt(1); rdy(0);
    prod(0, 0); prod(0, 1); prod(0, 2); prod(0, 3);
#pragma unroll
    for (int s = 0; s < 8; ++s) {
      if (s < 6) rdt(s + 2);
      if (s < 7) rdy(s + 1);
      SBAR();
#pragma unroll
      for (int i = 0; i < 4; ++i) {
        ka[i] = mfma16h(fy[s & 1][i >> 1], xx[s & 1][i & 1], ka[i]);
        if (s < 7) prod(s + 1, i);
        SBAR();
      }
    }
    const float fac = 2.f * dt * isc;
    LAS float* tb = (LAS float*)stg + 4 * quad * 32 + c16;
#pragma unroll
    for (int g = 0; g < 4; ++g) { tb[g * 32] = ka[0][g] * fac; tb[g * 32 + 16] = ka[1][g] * fac; }
    if (quad < 2) {
#pragma unroll
      for (int g = 0; g < 4; ++g) { tb[(16 + g) * 32] = ka[2][g] * fac; tb[(16 + g) * 32 + 16] = ka[3][g] * fac; }
    }
  }
  WAITVM(1);
  publish_u();
  {
    float kk[4][8];
#pragma unroll
    for (int c = 0; c < 4; ++c)
#pragma unroll
      for (int j = 0; j < 8; ++j) kk[c][j] = *(LAS float*)(lds + c * STG + tix[j]);
    SBAR();
#pragma unroll
    for (int c = 0; c < 2; ++c) {
      u32x4 v;
#pragma unroll
      for (int j = 0; j < 4; ++j) v[j] = pkbf(kk[2 * c][2 * j] + kk[2 * c + 1][2 * j], kk[2 * c][2 * j + 1] + kk[2 * c + 1][2 * j + 1]);
      *(LAS u32x4*)(lds + RV + c * RVS + tid * 16) = v;
    }
  }
  PRIO(3); mail = poll64(ws, mbu, lane < npc); PRIO(0);
  BAR();
  u32 sk[8];
#pragma unroll
  for (int i = 0; i < 2; ++i) {
    const u32x4 us = *(LAS u32x4*)(lds + UIM + (32 * i + (tid >> 3)) * 128 + (((tid & 7) ^ ((tid >> 5) & 7)) << 4));
#pragma unroll
    for (int k = 0; k < 4; ++k) { sk[4 * i + k] = pkbf(bflo(us[k]) * bflo(dk[k]), bfhi(us[k]) * bfhi(dk[k])); PIN(sk[4 * i + k]); }
  }
  if (tid < 64) *(LAS u32x4*)(lds + RV + half * RVS + 4096 + r32 * 16) = (u32x4){0u, 0u, 0u, 0u};
  SBAR();
  PRIO(3);
#pragma nounroll
  for (;;) {
#pragma nounroll
    while (__popcll(__ballot((u32)mail == tag)) != npc && --spin > 0) { mail = poll64(ws, mbu, lane < npc); }
    ud[0] = LD_SC1(ru, w * 2048 + lane * 16); ud[1] = LD_SC1(ru, w * 2048 + 1024 + lane * 16);
    ud[2] = LD_SC1(ru, lane < 16 ? w * 2048 + 2048 + lane * 16 : 8192);
    if (wave_sum(sum4(sum4(sum4((u32)(mail >> 32), ud[0]), ud[1]), ud[2])) == npc * 17u * tag || --spin <= 0) break;
    mail = 0;
  }
  PRIO(0);
#pragma unroll
  for (int i = 0; i < 2; ++i) {
    const u32 nx = __builtin_amdgcn_update_dpp(__builtin_amdgcn_readlane(ud[i + 1].x, 0), ud[i].x, 0x130, 0xf, 0xf, false);
    const u32 dw[5] = {ud[i].x, ud[i].y - tag, ud[i].z, ud[i].w, nx};
    LAS char* p = lds + UC + (128 * w + 64 * i + lane + 4) * 8;
#pragma unroll
    for (int c = 0; c < 2; ++c) {
      u32 pr[4];
#pragma unroll
      for (int k = 0; k < 4; ++k) pr[k] = __builtin_amdgcn_perm(dw[k + 1], dw[k], c ? 0x07060302u : 0x05040100u);
      *(LAS u32x2*)(p + (4 * c) * UCS) = (u32x2){pr[0], pr[2]};
      *(LAS u32x2*)(p + (4 * c + 1) * UCS) = (u32x2){pr[1], pr[3]};
      *(LAS u32*)(p + (4 * c + 2) * UCS - 4) = pr[0]; *(LAS u32*)(p + (4 * c + 2) * UCS) = pr[2];
      *(LAS u32*)(p + (4 * c + 3) * UCS - 4) = pr[1]; *(LAS u32*)(p + (4 * c + 3) * UCS) = pr[3];
    }
  }
  if (w == 0) {
    const u32 u0 = __builtin_amdgcn_readlane(ud[0].x, 0);
    const int cs = lane >> 3, ix = lane & 7, last = (cs & 2) ? 6 : 7;
    const u32 v = ((cs & 1) && ix == last) ? (cs & 4 ? u0 & 0xffff0000u : u0 << 16) : 0u;
    if (ix <= last) *(LAS u32*)(lds + UC + cs * UCS + ix * 4) = v;
  }
  BAR();
  {
    const int c16 = lane & 15, hq = lane >> 4;
    LAS char* pa = lds + RV + ch * RVS + 2 * (2032 - 256 * 7 + 128 * hf - 16 * c16 + 8 * hq);
    LAS char* pb = lds + UC + (4 * ch + ((c16 + 1) & 3)) * UCS + 2 * (128 * hf + 8 * hq + 4 * ((c16 + 1) >> 2));
    f32x4 acc[8] = {};
    u32x4 fa[2][8], fb[2][8];
    auto ldf = [&](int rho, int m) {
      fa[rho & 1][m] = *(LAS u32x4*)(pa + 64 * rho + 512 * (7 - m));
      const u32x2 lo = *(volatile LAS u32x2*)(pb + 64 * rho + 512 * m), hi = *(volatile LAS u32x2*)(pb + 64 * rho + 512 * m + 8);
      fb[rho & 1][m] = (u32x4){lo.x, lo.y, hi.x, hi.y};
    };
#pragma unroll
    for (int m = 0; m < 8; ++m) ldf(0, m);
    u32 ydst = WS_YX + (cb * 32 + 8 * w + hq) * 8192 + jp * 256 + c16 * 16;
    u32 ymb = c16 == 15 || c16 == 0 ? WS_MBY + (cb * 32 + 8 * w + hq + (c16 ? 0 : 4)) * 256 + jp * 8 : WS_END;
    LAS char* exw = lds + EX + w * 8192 + lane * 16;
    LAS char* exr = lds + EX + w * 2048 + lane * 16;
    PIN(ydst); PIN(ymb); PIN(exw); PIN(exr);
#pragma unroll
    for (int rho = 0; rho < 3; ++rho) {
      SBAR();
#pragma unroll
      for (int g = 0; g < 8; ++g) {
#pragma unroll
        for (int aa = 0; aa <= g; ++aa) {
          if (aa + g < 8) acc[aa + g] = mfma16(fa[rho & 1][g], fb[rho & 1][aa], acc[aa + g]);
          if (aa < g && aa + g < 8) acc[aa + g] = mfma16(fa[rho & 1][aa], fb[rho & 1][g], acc[aa + g]);
        }
        ldf(rho + 1, g); SBAR();
      }
    }
#pragma unroll
    for (int t = 0; t < 8; ++t)
#pragma unroll
      for (int aa = 0; aa <= t; ++aa) {
        acc[t] = mfma16(fa[1][t - aa], fb[1][aa], acc[t]);
        SBAR();
#pragma unroll
        for (int f = 0; f < 7; ++f) if (t * (t + 1) / 2 + aa == f * (f + 3) / 2 + 8) { *(LAS f32x4*)(exw + f * 1024) = acc[f]; SBAR(); }
      }
    *(LAS f32x4*)(exw + 7168) = acc[7];
    PRIO(1);
    BAR();
    f32x4 e[8];
#pragma unroll
    for (int k = 0; k < 8; ++k) e[k] = *(LAS f32x4*)(exr + (k >> 2) * 1024 + (k & 3) * 8192);
    u32x4 v[2];
#pragma unroll
    for (int i = 0; i < 2; ++i) {
      const f32x4 y0 = e[4 * i] + e[4 * i + 1], y1 = e[4 * i + 2] + e[4 * i + 3];
      v[i] = (u32x4){pkbf(y0.x, y1.x), pkbf(y0.y, y1.y), pkbf(y0.z, y1.z), pkbf(y0.w, y1.w)};
      ST_SC1(((u32x4){v[i].x + tag, v[i].y, v[i].z, v[i].w}), rs, ydst + i * 32768);
    }
    const u32 ck = tag - row_sum2(sum4(0u, v[0]), sum4(0u, v[1]));
    __builtin_amdgcn_raw_buffer_store_b64((u32x2){tag, ck}, rs, ymb, 0, 16);
  }
  {
    u32x4 y[2];
    const u32 mby = WS_MBY + a * 256 + (8 * w + lane) * 8;
    mail = poll64(ws, mby, lane < 8);
    float sf[16];
#pragma unroll
    for (int k = 0; k < 8; ++k) { sf[2 * k] = bflo(sk[k]); sf[2 * k + 1] = bfhi(sk[k]); PIN(sf[2 * k]); PIN(sf[2 * k + 1]); }
    u32 ysrc = WS_YX + a * 8192 + w * 2048 + lane * 16, odst = (64 * jp + (tid >> 3)) * 1792 + cb * 128 + (tid & 7) * 16;
    LAS char* tw = lds + (lane & 15) * 144 + (8 * w + (lane >> 4)) * 4;
    LAS char* tr = lds + (tid >> 3) * 144 + (tid & 7) * 16;
    PIN(ysrc); PIN(odst); PIN(tw); PIN(tr);
#pragma nounroll
    for (;;) {
#pragma nounroll
      while (__ballot((u32)mail == tag) != 0xffull && --spin > 0) { mail = poll64(ws, mby, lane < 8); }
      y[0] = LD_SC1(rs, ysrc); y[1] = LD_SC1(rs, ysrc + 1024);
      y[0].x -= tag; y[1].x -= tag;
#pragma unroll
      for (int i = 0; i < 2; ++i)
#pragma unroll
        for (int g = 0; g < 4; ++g) *(LAS u32*)(tw + g * 2304 + i * 16) = y[i][g];
      if (wave_sum(sum4(sum4((u32)(mail >> 32), y[0]), y[1])) == 8u * tag || --spin <= 0) break;
      mail = 0;
    }
    BAR();
#pragma unroll
    for (int i = 0; i < 2; ++i) {
      const u32x4 v = *(LAS u32x4*)(tr + i * 4608);
      u32x4 o;
#pragma unroll
      for (int k = 0; k < 4; ++k) o[k] = pkbf(bflo(v[k]) + sf[8 * i + 2 * k], bfhi(v[k]) + sf[8 * i + 2 * k + 1]);
      __builtin_amdgcn_raw_buffer_store_b128(o, RSRC(out, 2048 * 1792), odst + i * (32 * 1792), 0, 16);
    }
  }
}

__global__ void NOPK __launch_bounds__(256, 2)
fused_kernel(const void*, const void*, const void*, const void*, const void*, const void*, const void*, const void*, const void*,
             const void*, void*, void*) {
  __shared__ __attribute__((aligned(16))) char smem[81920];
  LAS char* lds = (LAS char*)smem;
  if ((blockIdx.x >> 5) == 7) {
    u64 pc;
    asm volatile("s_getpc_b64 %0" : "=s"(pc));
    if (threadIdx.x < CODE_LINES / 4)
      __builtin_amdgcn_global_load_lds((const GAS void*)((pc & ~127ull) + (threadIdx.x * 4 + ((blockIdx.x >> 3) & 3)) * 128), (LAS void*)lds, 4, 0, 0);
  }
  const __attribute__((address_space(4))) u64* ka = (const __attribute__((address_space(4))) u64*)__builtin_amdgcn_kernarg_segment_ptr();
  asm volatile("" : "+s"(ka));
  const char* u = (const char*)ka[0]; const GAS u32* logA = (const GAS u32*)ka[1]; const GAS u32* Aim = (const GAS u32*)ka[2];
  const GAS u32* Cre = (const GAS u32*)ka[3]; const GAS u32* Cim = (const GAS u32*)ka[4]; const GAS u32* logdt = (const GAS u32*)ka[5];
  const char* Dsk = (const char*)ka[6]; const char* x = (const char*)ka[7]; const char* wqkv = (const char*)ka[8];
  const char* wo = (const char*)ka[9]; char* out = (char*)ka[10]; char* ws = (char*)ka[11];
  asm volatile("" :: "s"(u), "s"(logA), "s"(Aim), "s"(Cre), "s"(Cim), "s"(logdt), "s"(Dsk), "s"(x), "s"(wqkv), "s"(wo), "s"(out), "s"(ws));
  const u32 epoch = request_epoch(ws);
  int bid = blockIdx.x;
  if (bid >= 256) role_b<true>(lds, x, wqkv, out, ws, epoch);
  asm volatile("" : "+s"(bid) :: "memory");
  if (bid >= 256) role_b<false>(lds, x, wqkv, out, ws, epoch);
  else if (bid < 224) {
    const int pi = bid * 256 + threadIdx.x;
    const u32 pA = __builtin_nontemporal_load(logA + pi), pI = __builtin_nontemporal_load(Aim + pi);
    const u32 pR = __builtin_nontemporal_load(Cre + pi), pC = __builtin_nontemporal_load(Cim + pi), ldt = logdt[bid];
    SBAR();
    role_a(lds, u, Dsk, wqkv, wo, out, ws, epoch, pA, pI, pR, pC, ldt);
  }
}

extern "C" void kernel_launch(void* const* d_in, const int* in_sizes, int n_in, void* d_out, int out_size, void* d_ws,
                              size_t ws_size, hipStream_t stream) {
  fused_kernel<<<512, 256, 0, stream>>>(d_in[0], d_in[1], d_in[2], d_in[3], d_in[4], d_in[5], d_in[6], d_in[7], d_in[8], d_in[9], d_out, d_ws);
}
```
